```python
import torch

def _softmax(x, axis=-1):
    return torch.softmax(x, dim=axis)
def _gelu(x):
    return torch.nn.functional.gelu(x, approximate="tanh")
def _silu(x):
    return torch.nn.functional.silu(x)
def _sigmoid(x):
    return torch.sigmoid(x)
def _layernorm(x, axis=-1, eps=1e-5):
    m = x.mean(dim=axis, keepdim=True)
    v = x.var(dim=axis, keepdim=True, unbiased=False)
    return (x - m) / torch.sqrt(v + eps)
def _scatter_add(tgt, idx, src, axis=0):
    return tgt.clone().index_add_(axis, idx, src)
def _segment_sum(src, seg, n):
    out = torch.zeros((n,) + src.shape[1:], dtype=src.dtype)
    return out.index_add_(0, seg, src)
def _pad(x, pw):
    flat = tuple(v for pair in reversed(pw) for v in pair)
    return torch.nn.functional.pad(x, flat)

kernel_name = 'interleave_layers__spectral_gfnet__mixer_channel_mlp__prompt'

def setup_inputs():

    _w = (lambda a: (lambda t: t.to(torch.bfloat16) if t.is_floating_point() else t)(torch.as_tensor(a)))
    def _A_setup():
        B, S, D = 1, 2048, 448
        Df = D // 2 + 1
        return {
            "x": _w((lambda *s: torch.randn(*s, dtype=torch.bfloat16) * 0.05)(B, S, D)),
            "w_r": _w((lambda *s: torch.randn(*s, dtype=torch.bfloat16) * 0.8)(S, Df) * 0.02),
            "w_i": _w((lambda *s: torch.randn(*s, dtype=torch.bfloat16) * 0.8)(S, Df) * 0.02),
        }
    _A_in = _A_setup()

    def _B_setup():
        B, S, D, F = 1, 2048, 448, 1792
        return {
            "x": _w((lambda *s: torch.randn(*s, dtype=torch.bfloat16) * 0.05)(B, S, D)),
            "w1": _w((lambda *s: torch.randn(*s, dtype=torch.bfloat16) * 0.8)(D, F) * (D ** -0.5)),
            "b1": _w((lambda *s: torch.randn(*s, dtype=torch.bfloat16) * 0.4)(F)),
            "w2": _w((lambda *s: torch.randn(*s, dtype=torch.bfloat16) * 0.05)(F, D) * (F ** -0.5)),
            "b2": _w((lambda *s: torch.randn(*s, dtype=torch.bfloat16) * 0.05)(D)),
        }
    _B_in = _B_setup()

    return {
        "A_x": _A_in["x"],
        "A_w_r": _A_in["w_r"],
        "A_w_i": _A_in["w_i"],
        "B_x": _B_in["x"],
        "B_w1": _B_in["w1"],
        "B_b1": _B_in["b1"],
        "B_w2": _B_in["w2"],
        "B_b2": _B_in["b2"],
    }

def reference(A_x, A_w_r, A_w_i, B_x, B_w1, B_b1, B_w2, B_b2):
    def _A_ref(x, w_r, w_i):
        B, S, D = x.shape
        X = (lambda x, axes=(-2,-1): torch.fft.rfft2(x.float(), dim=axes))(x, axes=(-2, -1))
        W = w_r + 1j * w_i
        X = X * W
        y = (lambda x, s, axes=(-2,-1): torch.fft.irfft2(x, s=s, dim=axes).to(torch.bfloat16))(X, (S, D), axes=(-2, -1))
        return y

    def _B_ref(x, w1, b1, w2, b2):
        y = _layernorm(x, axis=-1)
        h = _gelu(torch.einsum("bsd,df->bsf", y, w1) + b1)
        return torch.einsum("bsf,fd->bsd", h, w2) + b2

    x = A_x
    x = x + _A_ref(_layernorm(x, axis=-1), A_w_r, A_w_i)
    x = x + _B_ref(_layernorm(x, axis=-1), B_w1, B_b1, B_w2, B_b2)
    x = x + _A_ref(_layernorm(x, axis=-1), A_w_r, A_w_i)
    x = x + _B_ref(_layernorm(x, axis=-1), B_w1, B_b1, B_w2, B_b2)
    return x
```

```cpp
#include <hip/hip_runtime.h>
#include <hip/hip_bf16.h>

#define LAS __attribute__((address_space(3)))
#define GAS __attribute__((address_space(1)))
typedef unsigned short u16;
typedef short bf16x8 __attribute__((ext_vector_type(8)));
typedef short s16x4 __attribute__((ext_vector_type(4)));
typedef float f32x4 __attribute__((ext_vector_type(4)));
typedef float f32x16 __attribute__((ext_vector_type(16)));
typedef unsigned u32x4 __attribute__((ext_vector_type(4)));
typedef unsigned u32x2 __attribute__((ext_vector_type(2)));
typedef __bf16 bf2 __attribute__((ext_vector_type(2)));
typedef __amdgpu_buffer_rsrc_t rsrc_t;

constexpr int S_ = 2048, D_ = 448, F_ = 1792;
constexpr float EPS = 1e-5f;
constexpr float SC_AC = 0.06681531047810609f;
constexpr float SC_DC = 0.04724555912615340f;

constexpr int XT_BYTES = D_ * S_ * 2;
constexpr int OFF_FLAG = 0;
constexpr int OFF_B2H = 4096;
constexpr int OFF_SSQ = 8192;
constexpr int OFF_XT = OFF_SSQ + 2 * 7 * S_ * 4;
constexpr int OFF_WT = OFF_XT + 4 * XT_BYTES;
constexpr int OFF_TAB = OFF_WT + 224 * 8192;
constexpr int OFF_QT = OFF_TAB + 14 * 28 * 1024;
constexpr int OFF_PT = OFF_QT + 56 * 28 * 1024;
constexpr int OFF_W2T = OFF_PT + 14 * 112 * 1024;
constexpr int OFF_H = OFF_W2T + 14 * 112 * 1024;
constexpr int H_BYTES = 64 * 112 * 1024;
constexpr int WS_BYTES = OFF_H + 2 * H_BYTES;

constexpr int AUX_SC1 = 16;

__device__ __forceinline__ unsigned pack2(float a, float b) {
    bf2 v = {(__bf16)a, (__bf16)b};
    return __builtin_bit_cast(unsigned, v);
}
__device__ __forceinline__ float asf(unsigned u) { return __builtin_bit_cast(float, u); }
__device__ __forceinline__ unsigned asu(float f) { return __builtin_bit_cast(unsigned, f); }
__device__ __forceinline__ bf2 asb2(unsigned u) { return __builtin_bit_cast(bf2, u); }
__device__ __forceinline__ float bflo(unsigned u) { return __builtin_bit_cast(float, u << 16); }
__device__ __forceinline__ float bfhi(unsigned u) { return __builtin_bit_cast(float, u & 0xffff0000u); }
__device__ __forceinline__ f32x16 mfma32(u32x4 a, u32x4 b, f32x16 c) {
    return __builtin_amdgcn_mfma_f32_32x32x16_bf16(__builtin_bit_cast(bf16x8, a), __builtin_bit_cast(bf16x8, b), c, 0, 0, 0);
}
__device__ __forceinline__ f32x4 mfma16(u32x4 a, u32x4 b, f32x4 c) {
    return __builtin_amdgcn_mfma_f32_16x16x32_bf16(__builtin_bit_cast(bf16x8, a), __builtin_bit_cast(bf16x8, b), c, 0, 0, 0);
}
template <int AUX> __device__ __forceinline__ u32x4 bld128(rsrc_t r, int off) { return __builtin_amdgcn_raw_buffer_load_b128(r, off, 0, AUX); }
template <int AUX> __device__ __forceinline__ u32x4 bld128s(rsrc_t r, int voff, int soff) { return __builtin_amdgcn_raw_buffer_load_b128(r, voff, soff, AUX); }
template <int AUX> __device__ __forceinline__ u32x2 bld64(rsrc_t r, int off) { return __builtin_amdgcn_raw_buffer_load_b64(r, off, 0, AUX); }
template <int AUX> __device__ __forceinline__ unsigned bld32(rsrc_t r, int off) { return __builtin_amdgcn_raw_buffer_load_b32(r, off, 0, AUX); }
template <int AUX> __device__ __forceinline__ void bst128(u32x4 v, rsrc_t r, int off) { __builtin_amdgcn_raw_buffer_store_b128(v, r, off, 0, AUX); }
template <int AUX> __device__ __forceinline__ void bst64(u32x2 v, rsrc_t r, int off) { __builtin_amdgcn_raw_buffer_store_b64(v, r, off, 0, AUX); }
template <int AUX> __device__ __forceinline__ void bst32(unsigned v, rsrc_t r, int off) { __builtin_amdgcn_raw_buffer_store_b32(v, r, off, 0, AUX); }
__device__ __forceinline__ rsrc_t mkrsrc(const void* p, int bytes) { return __builtin_amdgcn_make_buffer_rsrc((void*)p, 0, bytes, 0x00020000); }
#define SCHED_FENCE() __builtin_amdgcn_sched_barrier(0)
#define LDS_BARRIER() do { asm volatile("s_waitcnt lgkmcnt(0)" ::: "memory"); __builtin_amdgcn_s_barrier(); asm volatile("" ::: "memory"); } while (0)
#define DRAIN_VM() asm volatile("s_waitcnt vmcnt(0)" ::: "memory")
__device__ __forceinline__ float vcos(float rev) { return __builtin_amdgcn_cosf(rev); }
__device__ __forceinline__ float vsin(float rev) { return __builtin_amdgcn_sinf(rev); }
__device__ __forceinline__ u32x4 neg8(u32x4 v) { return v ^ 0x80008000u; }
__device__ __forceinline__ u32x4 pack8(const f32x16& a, int o, float s) {
    u32x4 r;
    r.x = pack2(a[o + 0] * s, a[o + 1] * s); r.y = pack2(a[o + 2] * s, a[o + 3] * s);
    r.z = pack2(a[o + 4] * s, a[o + 5] * s); r.w = pack2(a[o + 6] * s, a[o + 7] * s);
    return r;
}
__device__ __forceinline__ u32x4 pack8n(const f32x16& a, int o) {
    u32x4 r;
    r.x = pack2(a[o + 0], a[o + 1]); r.y = pack2(a[o + 2], a[o + 3]);
    r.z = pack2(a[o + 4], a[o + 5]); r.w = pack2(a[o + 6], a[o + 7]);
    return r;
}

struct Params {
    const u16* x0; const u16* wr; const u16* wi; const u16* w1; const u16* b1; const u16* w2; const u16* b2;
    u16* out; unsigned char* ws;
};

constexpr int RED_ROW = 272, RED_PART = 64 * RED_ROW, RED_BYTES = 4 * RED_PART;
__device__ __forceinline__ void red_write(LAS unsigned char* red, int w, int lane, const f32x16& acc, int colblk, int rowblk) {
    LAS unsigned char* p = red + w * RED_PART + (32 * colblk + (lane & 31)) * RED_ROW + (8 * rowblk + (lane >> 5)) * 16;
#pragma unroll
    for (int r4 = 0; r4 < 4; ++r4) {
        f32x4 v = {acc[4 * r4], acc[4 * r4 + 1], acc[4 * r4 + 2], acc[4 * r4 + 3]};
        *(LAS f32x4*)(p + 32 * r4) = v;
    }
}
__device__ __forceinline__ void red_read(LAS unsigned char* red, int w, int lane, float (&v)[2][8]) {
    LAS unsigned char* p = red + (16 * w + (lane >> 3)) * RED_ROW + (lane & 7) * 32;
#pragma unroll
    for (int i = 0; i < 2; ++i) {
        f32x4 a = *(LAS f32x4*)(p + i * 8 * RED_ROW), b = *(LAS f32x4*)(p + i * 8 * RED_ROW + 16);
#pragma unroll
        for (int q = 1; q < 4; ++q) {
            a += *(LAS f32x4*)(p + q * RED_PART + i * 8 * RED_ROW);
            b += *(LAS f32x4*)(p + q * RED_PART + i * 8 * RED_ROW + 16);
        }
        v[i][0] = a.x; v[i][1] = a.y; v[i][2] = a.z; v[i][3] = a.w;
        v[i][4] = b.x; v[i][5] = b.y; v[i][6] = b.z; v[i][7] = b.w;
    }
}
template <int AUX>
__device__ __forceinline__ void xt_epilogue(rsrc_t ws, LAS float* sred, int w, int lane, float (&v)[2][8], int n0, int t0,
                                            int xt_off, int ssq_off) {
    const int n = n0 + 16 * w + (lane >> 3);
#pragma unroll
    for (int i = 0; i < 2; ++i) {
        u32x4 o = {pack2(v[i][0], v[i][1]), pack2(v[i][2], v[i][3]), pack2(v[i][4], v[i][5]), pack2(v[i][6], v[i][7])};
        bst128<AUX>(o, ws, xt_off + ((n + 8 * i) * S_ + t0 + 8 * (lane & 7)) * 2);
    }
    const float m0 = (n == 0) ? 0.f : 1.f;
    float s[8];
#pragma unroll
    for (int e = 0; e < 8; ++e) s[e] = v[0][e] * v[0][e] * m0 + v[1][e] * v[1][e];
    float u[4];
#pragma unroll
    for (int e = 0; e < 4; ++e) {
        auto r = __builtin_amdgcn_permlane32_swap(__builtin_bit_cast(unsigned, s[e]), __builtin_bit_cast(unsigned, s[e + 4]), false, false);
        u[e] = asf(r[0]) + asf(r[1]);
    }
    float y[2];
#pragma unroll
    for (int e = 0; e < 2; ++e) {
        auto r = __builtin_amdgcn_permlane16_swap(__builtin_bit_cast(unsigned, u[e]), __builtin_bit_cast(unsigned, u[e + 2]), false, false);
        y[e] = asf(r[0]) + asf(r[1]);
    }
    const float z0 = y[0] + __builtin_bit_cast(float, __builtin_amdgcn_mov_dpp(__builtin_bit_cast(int, y[0]), 0x128, 0xf, 0xf, true));
    const float z1 = y[1] + __builtin_bit_cast(float, __builtin_amdgcn_mov_dpp(__builtin_bit_cast(int, y[1]), 0x128, 0xf, 0xf, true));
    const float z = (lane & 8) ? z1 : z0;
    const int tok = 8 * (lane & 7) + 4 * (lane >> 5) + 2 * ((lane >> 4) & 1) + ((lane >> 3) & 1);
    sred[w * 64 + tok] = z;
    LDS_BARRIER();
    if (w == 0) {
        const float tot = (sred[lane] + sred[64 + lane]) + (sred[128 + lane] + sred[192 + lane]);
        bst32<AUX>(__builtin_bit_cast(unsigned, tot), ws, ssq_off + (t0 + lane) * 4);
    }
}

__device__ __forceinline__ void k0_main(const Params& p, rsrc_t ws, LAS unsigned char* lds, int b) {
    const int tid = threadIdx.x, lane = tid & 63, w = __builtin_amdgcn_readfirstlane(tid >> 6), h = lane >> 5, l31 = lane & 31;
    const int r = b >> 3, jt = r % 7, tt = (b & 7) + 8 * (r / 7);
    u32x4 A[2][7];
    const GAS u16* xb = (const GAS u16*)p.x0 + (32 * l31 + tt) * D_ + 112 * w + 8 * h;
#pragma unroll
    for (int t2 = 0; t2 < 2; ++t2)
#pragma unroll
        for (int ks = 0; ks < 7; ++ks) A[t2][ks] = *(const GAS u32x4*)(xb + t2 * 1024 * D_ + 16 * ks);
    SCHED_FENCE();
    u32x4 B[2][7];
#pragma unroll
    for (int j2 = 0; j2 < 2; ++j2) {
        const int j = 64 * jt + 32 * j2 + l31;
        const float cf = (j == 1 ? 224.f : (float)(j >> 1)) * (1.0f / 448.0f);
        float base = cf * (float)(112 * w + 8 * h) + (((j & 1) && j != 1) ? 0.25f : 0.f);
        base = __builtin_amdgcn_fractf(base);
#pragma unroll
        for (int ks = 0; ks < 7; ++ks)
#pragma unroll
            for (int i = 0; i < 4; ++i)
                B[j2][ks][i] = pack2(vcos(__builtin_fmaf(cf, (float)(16 * ks + 2 * i), base)), vcos(__builtin_fmaf(cf, (float)(16 * ks + 2 * i + 1), base)));
    }
    if (tt == 0) {
#pragma unroll
        for (int j2 = 0; j2 < 2; ++j2)
#pragma unroll
            for (int ks = 0; ks < 7; ++ks) bst128<0>(B[j2][ks], ws, OFF_TAB + ((2 * jt + j2) * 28 + 7 * w + ks) * 1024 + lane * 16);
    }
    SCHED_FENCE();
#pragma unroll
    for (int t2 = 0; t2 < 2; ++t2) {
        f32x16 acc[2] = {};
#pragma unroll
        for (int ks = 0; ks < 7; ++ks)
#pragma unroll
            for (int j2 = 0; j2 < 2; ++j2) acc[j2] = mfma32(A[t2][ks], B[j2][ks], acc[j2]);
        red_write(lds, w, lane, acc[0], 0, t2);
        red_write(lds, w, lane, acc[1], 1, t2);
    }
    LDS_BARRIER();
    float v[2][8];
    red_read(lds, w, lane, v);
    const int n = 64 * jt + 16 * w + (lane >> 3);
    const float sc0 = n < 2 ? SC_DC : SC_AC;
#pragma unroll
    for (int e = 0; e < 8; ++e) { v[0][e] *= sc0; v[1][e] *= SC_AC; }
    xt_epilogue<0>(ws, (LAS float*)(lds + RED_BYTES), w, lane, v, 64 * jt, 64 * tt, OFF_XT, OFF_SSQ + jt * S_ * 4);
}

__device__ __forceinline__ void wt_job(const Params& p, rsrc_t ws, LAS unsigned char* lds, int g) {
    const int tid = threadIdx.x;
    const int wq = g & 3, kq = g >> 2;
    u32x4 t[15];
#pragma unroll
    for (int i = 0; i < 15; ++i) {
        int u = tid + 256 * i;
        u = u < 3600 ? u : 3599;
        const int run = u / 450, e = u - run * 450;
        const GAS u16* src = (const GAS u16*)(run < 4 ? p.wr : p.wi) + (16 * wq + 64 * (4 * kq + (run & 3))) * 225 + e * 8;
        t[i] = *(const GAS u32x4*)src;
    }
#pragma unroll
    for (int i = 0; i < 15; ++i) {
        int u = tid + 256 * i;
        u = u < 3600 ? u : 3599;
        *(LAS u32x4*)(lds + u * 16) = t[i];
    }
    LDS_BARRIER();
    const int l15 = tid & 15;
#pragma unroll 2
    for (int i = 0; i < 14; ++i) {
        const int c = (tid >> 4) + 16 * i, cc = c == 0 ? 224 : c;
        LAS u16* q = (LAS u16*)(lds + l15 * 450 + 2 * cc);
        u32x4 o;
#pragma unroll
        for (int r = 0; r < 4; ++r) o[r] = (unsigned)q[r * 3600] | ((unsigned)q[(4 + r) * 3600] << 16);
        bst128<0>(o, ws, OFF_WT + c * 8192 + (wq * 2 + (kq >> 2)) * 1024 + (16 * (kq & 3) + l15) * 16);
    }
}

__global__ void __launch_bounds__(256, 2) k_first(Params p) {
    __shared__ __attribute__((aligned(16))) unsigned char smem[RED_BYTES + 1024];
    LAS unsigned char* lds = (LAS unsigned char*)smem;
    const rsrc_t ws = mkrsrc(p.ws, WS_BYTES);
    const int b = blockIdx.x;
    if (b < 224) k0_main(p, ws, lds, b);
    else {
        if (b == 224) bst128<0>(u32x4{0u, 0u, 0u, 0u}, ws, OFF_FLAG + threadIdx.x * 16);
        wt_job(p, ws, lds, b - 224);
    }
}

__device__ __forceinline__ int c_img(int row, int chunk) { return row * 128 + ((chunk ^ ((row >> 1) & 7)) << 4); }

__device__ __forceinline__ void c_main(rsrc_t ws, LAS unsigned char* lds, int c, int xin, int xout, int ssq) {
    const int tid = threadIdx.x, lane = tid & 63, w = __builtin_amdgcn_readfirstlane(tid >> 6), q = lane >> 4, l15 = lane & 15;
    const int rre = c == 0 ? 1 : 2 * c, rim = c == 0 ? 0 : 2 * c + 1;
    const u32x4 zr = bld128<0>(ws, xin + rre * 4096 + tid * 16);
    const u32x4 zi = bld128<0>(ws, xin + rim * 4096 + tid * 16);
    u32x4 sq[7][2];
#pragma unroll
    for (int i = 0; i < 7; ++i) { sq[i][0] = bld128<0>(ws, ssq + i * 8192 + tid * 32); sq[i][1] = bld128<0>(ws, ssq + i * 8192 + tid * 32 + 16); }
    u32x4 wt[2];
#pragma unroll
    for (int m = 0; m < 2; ++m) wt[m] = bld128<0>(ws, OFF_WT + c * 8192 + (w * 2 + m) * 1024 + lane * 16);
    u32x2 res[2][2];
#pragma unroll
    for (int m = 0; m < 2; ++m) {
        res[0][m] = bld64<0>(ws, xin + rre * 4096 + (64 * (16 * m + l15) + 16 * w + 4 * q) * 2);
        res[1][m] = bld64<0>(ws, xin + rim * 4096 + (64 * (16 * m + l15) + 16 * w + 4 * q) * 2);
    }
    SCHED_FENCE();
    u32x4 C64[2], S64[2], C32[2], S32[2];
    {
        const float a = (float)(16 * w + l15) * (1.0f / 64.0f);
#pragma unroll
        for (int ks = 0; ks < 2; ++ks)
#pragma unroll
            for (int i = 0; i < 4; ++i) {
                const float a0 = a * (float)(32 * ks + 8 * q + 2 * i), a1 = a0 + a;
                C64[ks][i] = pack2(vcos(a0), vcos(a1));
                S64[ks][i] = pack2(vsin(a0), vsin(a1));
            }
#pragma unroll
        for (int m = 0; m < 2; ++m) {
            const float bb = (float)(16 * m + l15) * (1.0f / 32.0f);
#pragma unroll
            for (int i = 0; i < 4; ++i) {
                const float a0 = bb * (float)(16 * (i >> 1) + 4 * q + 2 * (i & 1)), a1 = a0 + bb;
                C32[m][i] = pack2(vcos(a0), vcos(a1));
                S32[m][i] = pack2(vsin(a0), vsin(a1));
            }
        }
    }
    float tc[2][4], ts[2][4], uc[2][4], us[2][4];
#pragma unroll
    for (int m = 0; m < 2; ++m)
#pragma unroll
        for (int r = 0; r < 4; ++r) {
            const float a = (float)((16 * m + 4 * q + r) * (16 * w + l15)) * (1.0f / 2048.0f);
            tc[m][r] = vcos(a); ts[m][r] = vsin(a);
            const float bb = (float)((16 * w + 4 * q + r) * (16 * m + l15)) * (1.0f / 2048.0f);
            uc[m][r] = vcos(bb) * (1.0f / 2048.0f); us[m][r] = vsin(bb) * (1.0f / 2048.0f);
        }
    SCHED_FENCE();
    {
        float rr[8];
#pragma unroll
        for (int e = 0; e < 8; ++e) {
            float s = 0.f;
#pragma unroll
            for (int i = 0; i < 7; ++i) s += asf(sq[i][e >> 2][e & 3]);
            rr[e] = __builtin_amdgcn_rsqf(__builtin_fmaf(s, 1.0f / 448.0f, EPS));
        }
        u32x4 a, bq;
#pragma unroll
        for (int i = 0; i < 4; ++i) {
            a[i] = pack2(bflo(zr[i]) * rr[2 * i], bfhi(zr[i]) * rr[2 * i + 1]);
            bq[i] = c == 0 ? 0u : pack2(bflo(zi[i]) * rr[2 * i], bfhi(zi[i]) * rr[2 * i + 1]);
        }
        *(LAS u32x4*)(lds + c_img(tid >> 3, tid & 7)) = a;
        *(LAS u32x4*)(lds + 4096 + c_img(tid >> 3, tid & 7)) = bq;
    }
    LDS_BARRIER();
    const f32x4 zero4 = {0.f, 0.f, 0.f, 0.f};
    u32x4 Br, Bi;
    {
        f32x4 Yr[2], Yi[2];
        const u32x4 nS0 = neg8(S64[0]), nS1 = neg8(S64[1]);
#pragma unroll
        for (int m = 0; m < 2; ++m) {
            const u32x4 ar0 = *(LAS u32x4*)(lds + c_img(16 * m + l15, q)), ar1 = *(LAS u32x4*)(lds + c_img(16 * m + l15, 4 + q));
            const u32x4 ai0 = *(LAS u32x4*)(lds + 4096 + c_img(16 * m + l15, q)), ai1 = *(LAS u32x4*)(lds + 4096 + c_img(16 * m + l15, 4 + q));
            Yr[m] = mfma16(ar0, C64[0], zero4); Yi[m] = mfma16(ai0, C64[0], zero4);
            Yr[m] = mfma16(ar1, C64[1], Yr[m]); Yi[m] = mfma16(ai1, C64[1], Yi[m]);
            Yr[m] = mfma16(ai0, S64[0], Yr[m]); Yi[m] = mfma16(ar0, nS0, Yi[m]);
            Yr[m] = mfma16(ai1, S64[1], Yr[m]); Yi[m] = mfma16(ar1, nS1, Yi[m]);
        }
#pragma unroll
        for (int m = 0; m < 2; ++m)
#pragma unroll
            for (int i = 0; i < 2; ++i) {
                const int r0 = 2 * i, r1 = 2 * i + 1;
                Br[2 * m + i] = pack2(Yr[m][r0] * tc[m][r0] + Yi[m][r0] * ts[m][r0], Yr[m][r1] * tc[m][r1] + Yi[m][r1] * ts[m][r1]);
                Bi[2 * m + i] = pack2(Yi[m][r0] * tc[m][r0] - Yr[m][r0] * ts[m][r0], Yi[m][r1] * tc[m][r1] - Yr[m][r1] * ts[m][r1]);
            }
    }
    u32x4 nS32[2] = {neg8(S32[0]), neg8(S32[1])};
    u32x4 Ur, Ui;
    {
        f32x4 Xr[2], Xi[2];
#pragma unroll
        for (int m = 0; m < 2; ++m) {
            Xr[m] = mfma16(C32[m], Br, zero4); Xi[m] = mfma16(C32[m], Bi, zero4);
            Xr[m] = mfma16(S32[m], Bi, Xr[m]); Xi[m] = mfma16(nS32[m], Br, Xi[m]);
        }
#pragma unroll
        for (int m = 0; m < 2; ++m)
#pragma unroll
            for (int i = 0; i < 2; ++i) {
                const int r0 = 2 * i, r1 = 2 * i + 1;
                const float wr0 = bflo(wt[m][r0]), wi0 = bfhi(wt[m][r0]), wr1 = bflo(wt[m][r1]), wi1 = bfhi(wt[m][r1]);
                Ur[2 * m + i] = pack2(Xr[m][r0] * wr0 - Xi[m][r0] * wi0, Xr[m][r1] * wr1 - Xi[m][r1] * wi1);
                Ui[2 * m + i] = pack2(Xr[m][r0] * wi0 + Xi[m][r0] * wr0, Xr[m][r1] * wi1 + Xi[m][r1] * wr1);
            }
    }
    {
#pragma unroll
        for (int m = 0; m < 2; ++m) {
            f32x4 Zr = mfma16(Ur, C32[m], zero4), Zi = mfma16(Ui, C32[m], zero4);
            Zr = mfma16(Ui, nS32[m], Zr); Zi = mfma16(Ur, S32[m], Zi);
            u32x2 a, bq;
#pragma unroll
            for (int i = 0; i < 2; ++i) {
                const int r0 = 2 * i, r1 = 2 * i + 1;
                a[i] = pack2(Zr[r0] * uc[m][r0] - Zi[r0] * us[m][r0], Zr[r1] * uc[m][r1] - Zi[r1] * us[m][r1]);
                bq[i] = pack2(Zi[r0] * uc[m][r0] + Zr[r0] * us[m][r0], Zi[r1] * uc[m][r1] + Zr[r1] * us[m][r1]);
            }
            const int o = 8192 + c_img(16 * m + l15, 2 * w + (q >> 1)) + 8 * (q & 1);
            *(LAS u32x2*)(lds + o) = a;
            *(LAS u32x2*)(lds + o + 4096) = bq;
        }
    }
    LDS_BARRIER();
    {
        const u32x4 nS0 = neg8(S64[0]), nS1 = neg8(S64[1]);
#pragma unroll
        for (int m = 0; m < 2; ++m) {
            const u32x4 br0 = *(LAS u32x4*)(lds + 8192 + c_img(16 * m + l15, q)), br1 = *(LAS u32x4*)(lds + 8192 + c_img(16 * m + l15, 4 + q));
            const u32x4 bi0 = *(LAS u32x4*)(lds + 12288 + c_img(16 * m + l15, q)), bi1 = *(LAS u32x4*)(lds + 12288 + c_img(16 * m + l15, 4 + q));
            f32x4 yr = mfma16(C64[0], br0, zero4), yi = mfma16(C64[0], bi0, zero4);
            yr = mfma16(C64[1], br1, yr); yi = mfma16(C64[1], bi1, yi);
            yr = mfma16(nS0, bi0, yr); yi = mfma16(S64[0], br0, yi);
            yr = mfma16(nS1, bi1, yr); yi = mfma16(S64[1], br1, yi);
            u32x2 o0, o1;
#pragma unroll
            for (int i = 0; i < 2; ++i) {
                o0[i] = pack2(bflo(res[0][m][i]) + yr[2 * i], bfhi(res[0][m][i]) + yr[2 * i + 1]);
                o1[i] = c == 0 ? res[1][m][i] : pack2(bflo(res[1][m][i]) + yi[2 * i], bfhi(res[1][m][i]) + yi[2 * i + 1]);
            }
            bst64<0>(o0, ws, xout + rre * 4096 + (64 * (16 * m + l15) + 16 * w + 4 * q) * 2);
            bst64<0>(o1, ws, xout + rim * 4096 + (64 * (16 * m + l15) + 16 * w + 4 * q) * 2);
        }
    }
}

__device__ __forceinline__ u32x4 ident_frag(int lane, int u) {
    const int d = (lane & 31) - 16 * u - 8 * (lane >> 5);
    u32x4 r;
#pragma unroll
    for (int i = 0; i < 4; ++i) r[i] = d == 2 * i ? 0x3f80u : (d == 2 * i + 1 ? 0x3f800000u : 0u);
    return r;
}

__device__ __forceinline__ void p_job(const Params& p, rsrc_t ws, int ft, int nt) {
    const int tid = threadIdx.x, lane = tid & 63, w = __builtin_amdgcn_readfirstlane(tid >> 6), h = lane >> 5, l31 = lane & 31;
    const bool isb = ft == 14;
    if (isb && w != 0) return;
    const GAS u16* ab = isb ? (const GAS u16*)p.b2 + 8 * h : (const GAS u16*)p.w2 + (128 * ft + 32 * w + l31) * D_ + 8 * h;
    const int tb = OFF_TAB + 2 * nt * 28 * 1024 + lane * 16;
    f32x16 acc[2] = {};
    u32x4 A[2][7], B[2][2][7];
#define P_LOAD(buf, ch) \
    _Pragma("unroll") for (int ks = 0; ks < 7; ++ks) { \
        A[buf][ks] = *(const GAS u32x4*)(ab + 16 * (7 * (ch) + ks)); \
        B[buf][0][ks] = bld128<0>(ws, tb + (7 * (ch) + ks) * 1024); \
        B[buf][1][ks] = bld128<0>(ws, tb + (28 + 7 * (ch) + ks) * 1024); } \
    SCHED_FENCE();
    P_LOAD(0, 0)
    u32x4 T[4];
#pragma unroll
    for (int u = 0; u < 4; ++u) T[u] = *(const GAS u32x4*)(ab + 64 * nt + 16 * u);
    P_LOAD(1, 1)
#pragma unroll
    for (int ch = 0; ch < 4; ++ch) {
#pragma unroll
        for (int ks = 0; ks < 7; ++ks) {
            acc[0] = mfma32(A[ch & 1][ks], B[ch & 1][0][ks], acc[0]);
            acc[1] = mfma32(A[ch & 1][ks], B[ch & 1][1][ks], acc[1]);
        }
        SCHED_FENCE();
        if (ch < 2) { P_LOAD(ch & 1, ch + 2) }
    }
#undef P_LOAD
#pragma unroll
    for (int n2 = 0; n2 < 2; ++n2) {
        const int n = 64 * nt + 32 * n2 + l31;
        const float sc = n < 2 ? SC_DC : SC_AC;
        if (isb) { if (h == 0) bst32<0>(__builtin_bit_cast(unsigned, acc[n2][0] * sc), ws, OFF_B2H + n * 4); }
        else {
#pragma unroll
            for (int s = 0; s < 2; ++s) bst128<0>(pack8(acc[n2], 8 * s, sc), ws, OFF_PT + ((2 * nt + n2) * 112 + 8 * ft + 2 * w + s) * 1024 + lane * 16);
        }
    }
    if (!isb) {
        const u32x4 I0 = ident_frag(lane, 0), I1 = ident_frag(lane, 1);
#pragma unroll
        for (int d2 = 0; d2 < 2; ++d2) {
            f32x16 t = {};
            t = mfma32(T[2 * d2], I0, t); t = mfma32(T[2 * d2 + 1], I1, t);
#pragma unroll
            for (int s = 0; s < 2; ++s) bst128<0>(pack8n(t, 8 * s), ws, OFF_W2T + ((2 * nt + d2) * 112 + 8 * ft + 2 * w + s) * 1024 + lane * 16);
        }
    }
}

__device__ __forceinline__ void q_job(const Params& p, rsrc_t ws, int ft, int jt) {
    const int tid = threadIdx.x, lane = tid & 63, w = __builtin_amdgcn_readfirstlane(tid >> 6), h = lane >> 5, l31 = lane & 31;
    const int rho = (l31 & 19) | ((l31 & 4) << 1) | ((l31 & 8) >> 1);
    const GAS u16* ab = (const GAS u16*)p.w1 + rho * F_ + 128 * ft + 32 * w + 8 * h;
    const int tb = OFF_TAB + 2 * jt * 28 * 1024 + lane * 16;
    const u32x4 I0 = ident_frag(lane, 0), I1 = ident_frag(lane, 1);
    f32x16 acc[2] = {};
    u32x4 A[2][2][2], B[2][2][4];
#define Q_LOAD(buf, ch) \
    _Pragma("unroll") for (int k = 0; k < 2; ++k) { \
        A[buf][k][0] = *(const GAS u32x4*)(ab + (2 * (ch) + k) * 32 * F_); \
        A[buf][k][1] = *(const GAS u32x4*)(ab + (2 * (ch) + k) * 32 * F_ + 16); } \
    _Pragma("unroll") for (int k = 0; k < 4; ++k) { \
        B[buf][0][k] = bld128<0>(ws, tb + (4 * (ch) + k) * 1024); \
        B[buf][1][k] = bld128<0>(ws, tb + (28 + 4 * (ch) + k) * 1024); } \
    SCHED_FENCE();
    Q_LOAD(0, 0)
    Q_LOAD(1, 1)
#pragma unroll
    for (int ch = 0; ch < 7; ++ch) {
#pragma unroll
        for (int k = 0; k < 2; ++k) {
            f32x16 t = {};
            t = mfma32(A[ch & 1][k][0], I0, t); t = mfma32(A[ch & 1][k][1], I1, t);
#pragma unroll
            for (int s = 0; s < 2; ++s) {
                const u32x4 f = pack8n(t, 8 * s);
                acc[0] = mfma32(B[ch & 1][0][2 * k + s], f, acc[0]);
                acc[1] = mfma32(B[ch & 1][1][2 * k + s], f, acc[1]);
            }
        }
        SCHED_FENCE();
        if (ch < 5) { Q_LOAD(ch & 1, ch + 2) }
    }
#undef Q_LOAD
    if (jt == 0 && h == 0) { acc[0][0] = 0.f; acc[0][1] *= 0.70710678118654752f; }
#pragma unroll
    for (int j2 = 0; j2 < 2; ++j2)
#pragma unroll
        for (int s = 0; s < 2; ++s) bst128<0>(pack8(acc[j2], 8 * s, SC_AC), ws, OFF_QT + ((4 * ft + w) * 28 + 4 * jt + 2 * j2 + s) * 1024 + lane * 16);
}

template <int LAYER>
__global__ void __launch_bounds__(256, 2) k_col(Params p) {
    __shared__ __attribute__((aligned(16))) unsigned char smem[16384];
    LAS unsigned char* lds = (LAS unsigned char*)smem;
    const rsrc_t ws = mkrsrc(p.ws, WS_BYTES);
    const int b = blockIdx.x;
    if (LAYER == 1 || b < 224) c_main(ws, lds, b, OFF_XT + (2 * LAYER) * XT_BYTES, OFF_XT + (2 * LAYER + 1) * XT_BYTES, OFF_SSQ + LAYER * 7 * S_ * 4);
    else if (b < 224 + 98) q_job(p, ws, (b - 224) / 7, (b - 224) % 7);
    else p_job(p, ws, (b - 322) / 7, (b - 322) % 7);
}

constexpr int G_HALF = 448 * 128;
constexpr int G_STAT = 2 * G_HALF;
constexpr int G_SRED = G_STAT + 1024;
constexpr int G_LDS = G_SRED + 1024;
__device__ __forceinline__ u32x4 tr_frag(LAS unsigned char* a, int off) {
    const s16x4 lo = __builtin_amdgcn_ds_read_tr16_b64_v4i16((LAS s16x4*)(a + off));
    const s16x4 hi = __builtin_amdgcn_ds_read_tr16_b64_v4i16((LAS s16x4*)(a + off + 1024));
    const u32x2 l = __builtin_bit_cast(u32x2, lo), hh = __builtin_bit_cast(u32x2, hi);
    return u32x4{l.x, l.y, hh.x, hh.y};
}
__device__ __forceinline__ float dot8(u32x4 f, float acc) {
#pragma unroll
    for (int i = 0; i < 4; ++i) acc = __builtin_amdgcn_fdot2_f32_bf16(asb2(f[i]), asb2(f[i]), acc, false);
    return acc;
}
__device__ __forceinline__ float gelu1(float x) {
    constexpr float K1 = -2.3022081986f, K2 = -0.10294324f;
    const float e = __builtin_amdgcn_exp2f(x * __builtin_fmaf(x * x, K2, K1));
    return x * __builtin_amdgcn_rcpf(1.0f + e);
}

template <int LAYER>
__global__ void __launch_bounds__(256, 1) k_mlp(Params p) {
    __shared__ __attribute__((aligned(16))) unsigned char smem[G_LDS];
    LAS unsigned char* lds = (LAS unsigned char*)smem;
    const rsrc_t ws = mkrsrc(p.ws, WS_BYTES);
    const int tid = threadIdx.x, lane = tid & 63, w = __builtin_amdgcn_readfirstlane(tid >> 6), h = lane >> 5, l31 = lane & 31;
    const int b = blockIdx.x, r = b >> 3, it = r % 14, pn = (b & 7) + 8 * (r / 14);
    const int nt = it % 7, th = it / 7;
    constexpr int XIN = OFF_XT + (2 * LAYER + 1) * XT_BYTES, XOUT = OFF_XT + 2 * XT_BYTES, HB = OFF_H + LAYER * H_BYTES;
    const int pw = w >> 1, sw = w & 1;

    u32x4 Q[28], X[28];
    {
        const int qb = OFF_QT + (4 * it + w) * 28 * 1024 + lane * 16;
        const int xb = XIN + (8 * w + (lane >> 3)) * 4096 + (128 * pn + 8 * (lane & 7)) * 2;
#pragma unroll
        for (int i = 0; i < 4; ++i) X[i] = bld128s<0>(ws, xb, i * 32 * 4096);
#pragma unroll
        for (int s = 0; s < 8; ++s) Q[s] = bld128s<0>(ws, qb, (s ^ pw) * 1024);
#pragma unroll
        for (int i = 4; i < 10; ++i) X[i] = bld128s<0>(ws, xb, i * 32 * 4096);
#pragma unroll
        for (int s = 8; s < 20; ++s) Q[s] = bld128s<0>(ws, qb, (s ^ pw) * 1024);
#pragma unroll
        for (int i = 10; i < 14; ++i) X[i] = bld128s<0>(ws, xb, i * 32 * 4096);
#pragma unroll
        for (int s = 20; s < 28; ++s) Q[s] = bld128s<0>(ws, qb, (s ^ pw) * 1024);
#pragma unroll
        for (int i = 0; i < 14; ++i) X[14 + i] = bld128s<0>(ws, xb + 128, i * 32 * 4096);
    }
    float b1v[16];
    {
        u32x2 t[4];
#pragma unroll
        for (int r4 = 0; r4 < 4; ++r4) t[r4] = *(const GAS u32x2*)((const GAS u16*)p.b1 + 128 * it + 32 * w + 8 * r4 + 4 * h);
#pragma unroll
        for (int r4 = 0; r4 < 4; ++r4) { b1v[4 * r4] = bflo(t[r4].x); b1v[4 * r4 + 1] = bfhi(t[r4].x); b1v[4 * r4 + 2] = bflo(t[r4].y); b1v[4 * r4 + 3] = bfhi(t[r4].y); }
    }
    SCHED_FENCE();
    LAS unsigned char* wp = lds + (8 * w + (lane >> 3)) * 128 + (((lane & 7) ^ ((lane >> 2) & 4)) << 4);
#define X_WRITE(i) *(LAS u32x4*)(wp + ((i) / 14) * G_HALF + ((i) % 14) * 4096) = X[i]
    LAS unsigned char* rb[2][2];
    {
        const int qq = (lane & 15) >> 2, pp = lane & 3, blk = (lane >> 4) & 1;
#pragma unroll
        for (int sl = 0; sl < 2; ++sl)
#pragma unroll
            for (int par = 0; par < 2; ++par)
                rb[sl][par] = lds + (16 * (par ^ pw) + 4 * h + qq) * 128 + (((4 * (sl ^ sw) + 2 * blk + (pp >> 1)) ^ ((qq >> 1) << 2)) << 4) + 8 * (pp & 1);
    }
    f32x16 acc[4] = {};
    float ss = 0.f;
    const unsigned dcmask = (pw == 0 && h == 0) ? 0xffff0000u : 0xffffffffu;
#define G1_STEP(hf, s) { \
        const u32x4 f0 = tr_frag(rb[0][(s) & 1] + (hf) * G_HALF, ((s) & ~1) * 2048), f1 = tr_frag(rb[1][(s) & 1] + (hf) * G_HALF, ((s) & ~1) * 2048); \
        acc[2 * (hf)] = mfma32(Q[s], f0, acc[2 * (hf)]); acc[2 * (hf) + 1] = mfma32(Q[s], f1, acc[2 * (hf) + 1]); \
        if (((s) & 1) == 0) { u32x4 g = f0; if ((s) == 0) g.x &= dcmask; ss = dot8(g, ss); } }
    const int pb = (LAYER == 0 ? OFF_PT : OFF_W2T) + (2 * nt * 112 + 28 * w) * 1024 + lane * 16;
    u32x4 P[2][28];
#pragma unroll
    for (int i = 0; i < 4; ++i) X_WRITE(i);
    LDS_BARRIER();
#pragma unroll
    for (int s = 0; s < 8; ++s) G1_STEP(0, s)
#pragma unroll
    for (int i = 4; i < 10; ++i) X_WRITE(i);
    LDS_BARRIER();
#pragma unroll
    for (int s = 8; s < 20; ++s) G1_STEP(0, s)
#pragma unroll
    for (int i = 10; i < 14; ++i) X_WRITE(i);
#pragma unroll
    for (int k = 0; k < 14; ++k) P[0][k] = bld128s<0>(ws, pb, k * 1024);
    LDS_BARRIER();
#pragma unroll
    for (int s = 20; s < 28; ++s) G1_STEP(0, s)
#pragma unroll
    for (int i = 14; i < 28; ++i) X_WRITE(i);
#pragma unroll
    for (int k = 14; k < 28; ++k) P[0][k] = bld128s<0>(ws, pb, k * 1024);
    LAS float* stat = (LAS float*)(lds + G_STAT);
    {
        auto rr = __builtin_amdgcn_permlane32_swap(__builtin_bit_cast(unsigned, ss), __builtin_bit_cast(unsigned, ss), false, false);
        const float t = asf(rr[0]) + asf(rr[1]);
        stat[(sw * 2 + pw) * 32 + l31] = t;
        ss = 0.f;
    }
    LDS_BARRIER();
#define G1_OUT(hf, sl) { \
        const int tile = 2 * (hf) + ((sl) ^ sw); \
        const float v = (stat[tile * 64 + l31] + stat[tile * 64 + 32 + l31]) * (1.0f / 448.0f); \
        const float a = __builtin_amdgcn_rsqf(__builtin_fmaf(v, 1.0f + EPS, EPS * EPS)); \
        f32x16 g; \
        _Pragma("unroll") for (int e = 0; e < 16; ++e) g[e] = gelu1(__builtin_fmaf(acc[2 * (hf) + (sl)][e], a, b1v[e])); \
        _Pragma("unroll") for (int s2 = 0; s2 < 2; ++s2) \
            bst128<AUX_SC1>(pack8n(g, 8 * s2), ws, HB + lane * 16 + ((4 * pn + tile) * 112 + 8 * it + 2 * w + s2) * 1024); }
#pragma unroll
    for (int k = 0; k < 28; ++k) P[1][k] = bld128s<0>(ws, pb + 112 * 1024, k * 1024);
#pragma unroll
    for (int s = 0; s < 14; ++s) G1_STEP(1, s)
    G1_OUT(0, 0)
#pragma unroll
    for (int s = 14; s < 28; ++s) G1_STEP(1, s)
    G1_OUT(0, 1)
    {
        auto rr = __builtin_amdgcn_permlane32_swap(__builtin_bit_cast(unsigned, ss), __builtin_bit_cast(unsigned, ss), false, false);
        const float t = asf(rr[0]) + asf(rr[1]);
        stat[(4 + sw * 2 + pw) * 32 + l31] = t;
    }
    LDS_BARRIER();
    G1_OUT(1, 0)
    G1_OUT(1, 1)
    DRAIN_VM();
    __builtin_amdgcn_s_barrier();
    GAS unsigned* flags = (GAS unsigned*)(p.ws + OFF_FLAG) + (LAYER * 16 + pn) * 32;
    if (tid == 0) __hip_atomic_store(flags + it, 1u, __ATOMIC_RELAXED, __HIP_MEMORY_SCOPE_AGENT);
    f32x16 d[2][2] = {};
    u32x4 res[2];
    float bias[2];
    u32x4 b2v;
    if (LAYER == 0) {
        const int n = 64 * nt + 16 * w + (lane >> 3);
#pragma unroll
        for (int i = 0; i < 2; ++i) {
            res[i] = bld128<0>(ws, XIN + ((n + 8 * i) * S_ + 128 * pn + 64 * th + 8 * (lane & 7)) * 2);
            bias[i] = __builtin_bit_cast(float, bld32<0>(ws, OFF_B2H + (n + 8 * i) * 4));
        }
    } else {
        b2v = *(const GAS u32x4*)((const GAS u16*)p.b2 + 64 * nt + 8 * (lane & 7));
        const int qq = (lane & 15) >> 2, pp = lane & 3, blk = (lane >> 4) & 1;
        LAS unsigned char* xb0 = lds + th * G_HALF + (112 * w + 4 * h + qq) * 128 + (((2 * blk + (pp >> 1)) ^ ((qq >> 1) << 2)) << 4) + 8 * (pp & 1);
        LAS unsigned char* xb1 = lds + th * G_HALF + (112 * w + 4 * h + qq) * 128 + (((4 + 2 * blk + (pp >> 1)) ^ ((qq >> 1) << 2)) << 4) + 8 * (pp & 1);
#pragma unroll
        for (int d2 = 0; d2 < 2; ++d2) {
            const int dd = 64 * nt + 32 * d2 + l31;
            const float df = (float)dd * (1.0f / 448.0f);
            const float base = __builtin_amdgcn_fractf(df * (float)(56 * w + 2 * h));
#pragma unroll
            for (int ks = 0; ks < 7; ++ks) {
                u32x4 t;
#pragma unroll
                for (int i = 0; i < 4; ++i) {
                    const float a = __builtin_fmaf(df, (float)(8 * ks + 4 * (i >> 1) + (i & 1)), base);
                    t[i] = pack2(vcos(a) * SC_AC, vsin(a) * -SC_AC);
                }
                if (ks == 0 && w == 0 && h == 0) t[0] = pack2(SC_DC, (dd & 1) ? -SC_DC : SC_DC);
                d[d2][0] = mfma32(t, tr_frag(xb0, ks * 2048), d[d2][0]);
                d[d2][1] = mfma32(t, tr_frag(xb1, ks * 2048), d[d2][1]);
            }
        }
    }
    if (w == 0) {
        for (int spin = 0; spin < (1 << 16); ++spin) {
            const unsigned f = lane < 14 ? __hip_atomic_load(flags + lane, __ATOMIC_RELAXED, __HIP_MEMORY_SCOPE_AGENT) : 1u;
            if (__builtin_amdgcn_ballot_w64(f != 1u) == 0ull) break;
            __builtin_amdgcn_s_sleep(1);
        }
        __builtin_amdgcn_fence(__ATOMIC_ACQUIRE, "wavefront");
    }
    LDS_BARRIER();
    constexpr int HW = LAYER == 0 ? 36 : 28;
    u32x4 Hf[HW];
    const int hv = HB + ((4 * pn + 2 * th) * 112 + 28 * w) * 1024 + lane * 16;
#define H_LOAD(i) bld128s<AUX_SC1>(ws, hv, (((i) / 28) * 112 + (i) % 28) * 1024)
#pragma unroll
    for (int i = 0; i < HW; ++i) Hf[i] = H_LOAD(i);
    SCHED_FENCE();
#pragma unroll
    for (int t2 = 0; t2 < 2; ++t2) {
#pragma unroll
        for (int k = 0; k < 28; ++k) {
            const int i = 28 * t2 + k;
            const u32x4 hc = Hf[i % HW];
            if (LAYER == 0) {
                d[t2][0] = mfma32(hc, P[0][k], d[t2][0]);
                d[t2][1] = mfma32(hc, P[1][k], d[t2][1]);
            } else {
                d[0][t2] = mfma32(P[0][k], hc, d[0][t2]);
                d[1][t2] = mfma32(P[1][k], hc, d[1][t2]);
            }
            if (i + HW < 56) { Hf[i % HW] = H_LOAD(i + HW); SCHED_FENCE(); }
        }
        if (LAYER == 0) { red_write(lds, w, lane, d[t2][0], 0, t2); red_write(lds, w, lane, d[t2][1], 1, t2); }
        else { red_write(lds, w, lane, d[0][t2], t2, 0); red_write(lds, w, lane, d[1][t2], t2, 1); }
    }
    LDS_BARRIER();
    float v[2][8];
    red_read(lds, w, lane, v);
    if (LAYER == 0) {
#pragma unroll
        for (int i = 0; i < 2; ++i)
#pragma unroll
            for (int e = 0; e < 4; ++e) { v[i][2 * e] += bflo(res[i][e]) + bias[i]; v[i][2 * e + 1] += bfhi(res[i][e]) + bias[i]; }
        xt_epilogue<0>(ws, (LAS float*)(lds + G_SRED), w, lane, v, 64 * nt, 128 * pn + 64 * th, XOUT, OFF_SSQ + (7 + nt) * S_ * 4);
    } else {
#pragma unroll
        for (int i = 0; i < 2; ++i) {
            const int n1 = 16 * w + (lane >> 3) + 8 * i, srow = 32 * n1 + 2 * pn + th;
            u32x4 o;
#pragma unroll
            for (int e = 0; e < 4; ++e) o[e] = pack2(v[i][2 * e] + bflo(b2v[e]), v[i][2 * e + 1] + bfhi(b2v[e]));
            *(GAS u32x4*)((GAS u16*)p.out + srow * D_ + 64 * nt + 8 * (lane & 7)) = o;
        }
    }
}

extern "C" void kernel_launch(void* const* d_in, const int* in_sizes, int n_in, void* d_out, int out_size, void* d_ws, size_t ws_size,
                              hipStream_t stream) {
    Params p;
    p.x0 = (const u16*)d_in[0]; p.wr = (const u16*)d_in[1]; p.wi = (const u16*)d_in[2];
    p.w1 = (const u16*)d_in[4]; p.b1 = (const u16*)d_in[5]; p.w2 = (const u16*)d_in[6]; p.b2 = (const u16*)d_in[7];
    p.out = (u16*)d_out; p.ws = (unsigned char*)d_ws;
    k_first<<<256, 256, 0, stream>>>(p);
    k_col<0><<<224 + 98 + 105, 256, 0, stream>>>(p);
    k_mlp<0><<<224, 256, 0, stream>>>(p);
    k_col<1><<<224, 256, 0, stream>>>(p);
    k_mlp<1><<<224, 256, 0, stream>>>(p);
}
```

```cpp
#include <hip/hip_runtime.h>
#include <hip/hip_bf16.h>

#define LAS __attribute__((address_space(3)))
#define GAS __attribute__((address_space(1)))
typedef unsigned short u16;
typedef short bf16x8 __attribute__((ext_vector_type(8)));
typedef short s16x4 __attribute__((ext_vector_type(4)));
typedef float f32x4 __attribute__((ext_vector_type(4)));
typedef float f32x16 __attribute__((ext_vector_type(16)));
typedef unsigned u32x4 __attribute__((ext_vector_type(4)));
typedef unsigned u32x2 __attribute__((ext_vector_type(2)));
typedef __bf16 bf2 __attribute__((ext_vector_type(2)));
typedef __amdgpu_buffer_rsrc_t rsrc_t;

constexpr int S_ = 2048, D_ = 448, F_ = 1792;
constexpr float EPS = 1e-5f;
constexpr float SC_AC = 0.06681531047810609f;
constexpr float SC_DC = 0.04724555912615340f;

constexpr int TILE_B = D_ * 64;
constexpr int XT_BYTES = 64 * TILE_B;
constexpr int OFF_FLAG = 0;
constexpr int OFF_BAR = 57344;
constexpr int RL_ = 2, ST_ = 16, BAR_B = 4 * RL_ * 256 * ST_;
constexpr int OFF_B1C = 131072;
constexpr int OFF_B2H = OFF_B1C + 8192;
constexpr int OFF_B2F = OFF_B2H + 2048;
constexpr int OFF_SSQ = OFF_B2F + 2048;
constexpr int OFF_XT = OFF_SSQ + 65536;
constexpr int OFF_WT = OFF_XT + 4 * XT_BYTES;
constexpr int OFF_TAB = OFF_WT + 224 * 8192;
constexpr int OFF_QT = OFF_TAB + 14 * 28 * 1024;
constexpr int OFF_PT = OFF_QT + 56 * 28 * 1024;
constexpr int OFF_W2T = OFF_PT + 14 * 112 * 1024;
constexpr int OFF_H = OFF_W2T + 14 * 112 * 1024;
constexpr int H_BYTES = 64 * 112 * 1024;
constexpr int WS_BYTES = OFF_H + 2 * H_BYTES;
constexpr float GELU_C = 0.4686686930532063f;

constexpr int AUX_SC1 = 16;

__device__ __forceinline__ unsigned pack2(float a, float b) {
    bf2 v = {(__bf16)a, (__bf16)b};
    return __builtin_bit_cast(unsigned, v);
}
__device__ __forceinline__ float asf(unsigned u) { return __builtin_bit_cast(float, u); }
__device__ __forceinline__ unsigned asu(float f) { return __builtin_bit_cast(unsigned, f); }
__device__ __forceinline__ bf2 asb2(unsigned u) { return __builtin_bit_cast(bf2, u); }
__device__ __forceinline__ float bflo(unsigned u) { return __builtin_bit_cast(float, u << 16); }
__device__ __forceinline__ float bfhi(unsigned u) { return __builtin_bit_cast(float, u & 0xffff0000u); }
__device__ __forceinline__ f32x16 mfma32(u32x4 a, u32x4 b, f32x16 c) {
    return __builtin_amdgcn_mfma_f32_32x32x16_bf16(__builtin_bit_cast(bf16x8, a), __builtin_bit_cast(bf16x8, b), c, 0, 0, 0);
}
__device__ __forceinline__ f32x4 mfma16(u32x4 a, u32x4 b, f32x4 c) {
    return __builtin_amdgcn_mfma_f32_16x16x32_bf16(__builtin_bit_cast(bf16x8, a), __builtin_bit_cast(bf16x8, b), c, 0, 0, 0);
}
template <int AUX> __device__ __forceinline__ u32x4 bld128(rsrc_t r, int off) { return __builtin_amdgcn_raw_buffer_load_b128(r, off, 0, AUX); }
template <int AUX> __device__ __forceinline__ u32x4 bld128s(rsrc_t r, int voff, int soff) { return __builtin_amdgcn_raw_buffer_load_b128(r, voff, soff, AUX); }
template <int AUX> __device__ __forceinline__ u32x2 bld64(rsrc_t r, int off) { return __builtin_amdgcn_raw_buffer_load_b64(r, off, 0, AUX); }
template <int AUX> __device__ __forceinline__ unsigned bld32(rsrc_t r, int off) { return __builtin_amdgcn_raw_buffer_load_b32(r, off, 0, AUX); }
template <int AUX> __device__ __forceinline__ void bst128(u32x4 v, rsrc_t r, int off) { __builtin_amdgcn_raw_buffer_store_b128(v, r, off, 0, AUX); }
template <int AUX> __device__ __forceinline__ void bst64(u32x2 v, rsrc_t r, int off) { __builtin_amdgcn_raw_buffer_store_b64(v, r, off, 0, AUX); }
template <int AUX> __device__ __forceinline__ void bst32(unsigned v, rsrc_t r, int off) { __builtin_amdgcn_raw_buffer_store_b32(v, r, off, 0, AUX); }
__device__ __forceinline__ rsrc_t mkrsrc(const void* p, int bytes) { return __builtin_amdgcn_make_buffer_rsrc((void*)p, 0, bytes, 0x00020000); }
#define SCHED_FENCE() __builtin_amdgcn_sched_barrier(0)
#define LDS_BARRIER() do { asm volatile("s_waitcnt lgkmcnt(0)" ::: "memory"); __builtin_amdgcn_s_barrier(); asm volatile("" ::: "memory"); } while (0)
#define DRAIN_VM() asm volatile("s_waitcnt vmcnt(0)" ::: "memory")
__device__ __forceinline__ float vcos(float rev) { return __builtin_amdgcn_cosf(rev); }
__device__ __forceinline__ float vsin(float rev) { return __builtin_amdgcn_sinf(rev); }
__device__ __forceinline__ u32x4 neg8(u32x4 v) { return v ^ 0x80008000u; }
__device__ __forceinline__ u32x4 pack8(const f32x16& a, int o, float s) {
    u32x4 r;
    r.x = pack2(a[o + 0] * s, a[o + 1] * s); r.y = pack2(a[o + 2] * s, a[o + 3] * s);
    r.z = pack2(a[o + 4] * s, a[o + 5] * s); r.w = pack2(a[o + 6] * s, a[o + 7] * s);
    return r;
}
__device__ __forceinline__ u32x4 pack8n(const f32x16& a, int o) {
    u32x4 r;
    r.x = pack2(a[o + 0], a[o + 1]); r.y = pack2(a[o + 2], a[o + 3]);
    r.z = pack2(a[o + 4], a[o + 5]); r.w = pack2(a[o + 6], a[o + 7]);
    return r;
}


#define G_CLOB "v0", "v1", "v2", "v3", "v4", "v5", "v6", "v7", "v8", "v9", "v10", "v11", "v12", "v13", "v14", "v15", "v16", "v17", "v18", "v19", "v20", "v21", "v22", "v23", "v24", "v25", "v26", "v27", "v28", "v29", "v30", "v31", "v32", "v33", "v34", "v35", "v36", "v37", "v38", "v39", "v40", "v41", "v42", "v43", "v44", "v45", "v46", "v47", "v48", "v49", "v50", "v51", "v52", "v53", "v54", "v55", "v56", "v57", "v58", "v59", "v60", "v61", "v62", "v63", "v64", "v65", "v66", "v67", "v68", "v69", "v70", "v71", "v72", "v73", "v74", "v75", "v76", "v77", "v78", "v79", "v80", "v81", "v82", "v83", "v84", "v85", "v86", "v87", "v88", "v89", "v90", "v91", "v92", "v93", "v94", "v95", "v96", "v97", "v98", "v99", "v100", "v101", "v102", "v103", "v104", "v105", "v106", "v107", "v108", "v109", "v110", "v111", "v112", "v113", "v114", "v115", "v116", "v117", "v118", "v119", "v120", "v121", "v122", "v123", "v124", "v125", "v126", "v127", "v128", "v129", "v130", "v131", "v132", "v133", "v134", "v135", "v136", "v137", "v138", "v139", "v140", "v141", "v142", "v143", "v144", "v145", "v146", "v147", "v148", "v149", "v150", "v151", "v152", "v153", "v154", "v155", "v156", "v157", "v158", "v159", "v160", "v161", "v162", "v163", "v164", "v165", "v166", "v167", "v168", "v169", "v170", "v171", "v172", "v173", "v174", "v175", "v176", "v177", "v178", "v179", "v180", "v181", "v182", "v183", "v184", "v185", "v186", "v187", "v188", "v189", "v190", "v191", "v192", "v193", "v194", "v195", "v196", "v197", "v198", "v199", "v200", "v201", "v202", "v203", "v204", "v205", "v206", "v207", "v208", "v209", "v210", "v211", "v212", "v213", "v214", "v215", "v216", "v217", "v218", "v219", "v220", "v221", "v222", "v223", "v224", "v225", "v226", "v227", "v228", "v229", "v230", "v231", "v232", "v233", "v234", "v235", "v236", "v237", "v238", "v239", "v240", "v241", "v242", "v243", "v244", "v245", "v246", "v247", "v248", "v249", "v250", "v251", "v252", "v253", "v254", "v255", "a0", "a1", "a2", "a3", "a4", "a5", "a6", "a7", "a8", "a9", "a10", "a11", "a12", "a13", "a14", "a15", "a16", "a17", "a18", "a19", "a20", "a21", "a22", "a23", "a24", "a25", "a26", "a27", "a28", "a29", "a30", "a31", "a32", "a33", "a34", "a35", "a36", "a37", "a38", "a39", "a40", "a41", "a42", "a43", "a44", "a45", "a46", "a47", "a48", "a49", "a50", "a51", "a52", "a53", "a54", "a55", "a56", "a57", "a58", "a59", "a60", "a61", "a62", "a63", "a64", "a65", "a66", "a67", "a68", "a69", "a70", "a71", "a72", "a73", "a74", "a75", "a76", "a77", "a78", "a79", "a80", "a81", "a82", "a83", "a84", "a85", "a86", "a87", "a88", "a89", "a90", "a91", "a92", "a93", "a94", "a95", "a96", "a97", "a98", "a99", "a100", "a101", "a102", "a103", "a104", "a105", "a106", "a107", "a108", "a109", "a110", "a111", "a112", "a113", "a114", "a115", "a116", "a117", "a118", "a119", "a120", "a121", "a122", "a123", "a124", "a125", "a126", "a127", "a128", "a129", "a130", "a131", "a132", "a133", "a134", "a135", "a136", "a137", "a138", "a139", "a140", "a141", "a142", "a143", "a144", "a145", "a146", "a147", "a148", "a149", "a150", "a151", "a152", "a153", "a154", "a155", "a156", "a157", "a158", "a159", "a160", "a161", "a162", "a163", "a164", "a165", "a166", "a167", "a168", "a169", "a170", "a171", "a172", "a173", "a174", "a175", "a176", "a177", "a178", "a179", "a180", "a181", "a182", "a183", "a184", "a185", "a186", "a187", "a188", "a189", "a190", "a191", "a192", "a193", "a194", "a195", "a196", "a197", "a198", "a199", "a200", "a201", "a202", "a203", "a204", "a205", "a206", "a207", "a208", "a209", "a210", "a211", "a212", "a213", "a214", "a215", "a216", "a217", "a218", "a219", "a220", "a221", "a222", "a223", "a224", "a225", "a226", "a227", "a228", "a229", "a230", "a231", "a232", "a233", "a234", "a235", "a236", "a237", "a238", "a239", "a240", "a241", "a242", "a243", "a244", "a245", "a246", "a247", "a248", "a249", "a250", "a251", "a252", "a253", "a254", "a255", "s40", "s41", "s42", "s43", "s44", "s45", "s46", "s47", "s48", "s49", "s50", "s51", "s52", "s53", "s54", "s55", "s56", "s57", "s58", "s59", "vcc", "scc", "memory"
struct Params {
    const u16* x0; const u16* wr; const u16* wi; const u16* w1; const u16* b1; const u16* w2; const u16* b2;
    u16* out; unsigned char* ws;
    int layer; int unused;
};

#define SYM_ADDR(dst, sym) asm volatile("s_getpc_b64 s[40:41]\n s_add_u32 s40, s40, " sym "@rel32@lo+4\n s_addc_u32 s41, s41, " sym "@rel32@hi+12\n" \
    "s_mov_b64 %0, s[40:41]" : "=s"(dst) :: "s40", "s41", "scc")
__device__ __forceinline__ unsigned code_touch(int b) {
    unsigned long long a1, a2, a3, a4;
    SYM_ADDR(a1, "_Z6k_mlp06Params"); SYM_ADDR(a2, "_Z6k_mlp16Params");
    SYM_ADDR(a3, "ilg_end0"); SYM_ADDR(a4, "ilg_end1");
    const unsigned long long lo = a1 < a2 ? a1 : a2, hi = a3 > a4 ? a3 : a4;
    const unsigned o = ((b >> 3) * 256 + threadIdx.x) * 64;
    return *(const GAS unsigned*)(lo + (lo + o < hi ? o : 0u));
}

constexpr int F_W2 = 0, F_W1 = 56 * 1024, F_X = F_W1 + 44 * 1024, F_MISC = F_X + 56 * 1024, F_LDS = F_MISC + 1024;
typedef float f32x2 __attribute__((ext_vector_type(2)));
__device__ __forceinline__ u32x4 mkdesc(const void* p, unsigned bytes) {
    return u32x4{(unsigned)(size_t)p, (unsigned)((size_t)p >> 32) & 0xffffu, bytes, 0x00020000u};
}
__device__ __forceinline__ void dma16(u32x4 desc, int voff, int soff, int ldsaddr) {
    asm volatile("s_mov_b32 m0, %3\n s_nop 0\n buffer_load_dwordx4 %0, %1, %2 offen lds" :: "v"(voff), "s"(desc), "s"(soff), "s"(ldsaddr) : "memory");
}
#define WAIT_VM(n) asm volatile("s_waitcnt vmcnt(" #n ")" ::: "memory")
__device__ __forceinline__ void first_main(const Params& p, const int lds, const int b) {
    const int w = __builtin_amdgcn_readfirstlane(threadIdx.x >> 6);
    const int r = b >> 3, jt = r % 7, tt = 4 * (b & 7) + r / 7;
    const int jb = 2 * jt + (w & 1), rb = w >> 1;
    const int full = (tt - 28) >> 31, bias = ((tt ^ 28) - 1) >> 31;
    const u32x4 ws = {(unsigned)(size_t)p.ws, (unsigned)((size_t)p.ws >> 32) & 0xffffu, (unsigned)WS_BYTES, 0x00020000u};
    const size_t fm = (size_t)(long long)full;
    const u32x4 dx = mkdesc(p.x0, S_ * D_ * 2);
    const u32x4 d2 = mkdesc((const void*)((((size_t)p.w2 + tt * 64 * D_ * 2) & fm) | ((size_t)p.b2 & ~fm)), ((64 * D_ * 2) & full) | ((D_ * 2) & bias));
    const u32x4 d1 = mkdesc((const void*)(((size_t)p.w1 & fm) | ((size_t)p.wr & ~fm)), ((D_ * F_ * 2) & full) | ((S_ * 450) & ~full));
    const int hj = 32 + (tt - 28) * 7 + jt, wq = (hj >> 1) & 3, kq = hj >> 3, lh = hj & 1;
    const int rs = (D_ * 2) & full;
    const int s1 = ((8 * w * F_ * 2 + tt * 128) & full) | (((16 * wq + 8 * lh + 64 * (4 * kq + w)) * 450) & ~full);
    const int po = ((OFF_PT + (jb * 112 + 4 * tt + 2 * rb) * 1024) & full) | ((OFF_WT + (wq * 2 + (kq >> 2)) * 1024 + (16 * (kq & 3) + 8 * lh) * 16 + w * 65536) & ~full);
    const int qo = ((OFF_QT + ((2 * tt + rb) * 28 + 2 * jb) * 1024) & full) | ((OFF_B2H + 32 * jb * 4) & ~full);
    asm volatile(
    "v_mbcnt_lo_u32_b32 v224, -1, 0\n"
    "s_lshl_b32 s51, %[w], 10\n"
    "v_mbcnt_hi_u32_b32 v224, -1, v224\n"
    "s_lshl1_add_u32 s50, s51, %[lds]\n"
    "v_lshlrev_b32 v235, 4, v224\n"
    "s_getpc_b64 s[46:47]\n"
    "LP_%=:\n"
    "v_lshl_add_u32 v176, %[w], 10, v235\n"
    "v_lshlrev_b32 v176, 2, v176\n"
    "v_min_u32 v176, LE_%=-LP_%=, v176\n"
    "global_load_dword v238, v176, s[46:47]\n"
    "s_cmp_lt_u32 %[tt], 28\n"
    "s_cbranch_scc1 LS_%=\n"
    "s_lshl2_add_u32 s46, s51, %[lds]\n"
    "s_add_u32 m0, s46, 57344\n"
    "s_mov_b64 s[52:53], %[wi]\n"
    "s_mov_b32 s54, 921600\n"
    "s_mov_b32 s55, 0x20000\n"
    "buffer_load_dwordx4 v235, %[d1], %[s1] offen offset:0 lds\n"
    "buffer_load_dwordx4 v235, %[d1], %[s1] offen offset:1024 lds\n"
    "buffer_load_dwordx4 v235, %[d1], %[s1] offen offset:2048 lds\n"
    "buffer_load_dwordx4 v235, %[d1], %[s1] offen offset:3072 lds\n"
    "s_add_u32 m0, m0, 16384\n"
    "s_and_b32 s53, s53, 0xffff\n"
    "buffer_load_dwordx4 v235, s[52:55], %[s1] offen offset:0 lds\n"
    "buffer_load_dwordx4 v235, s[52:55], %[s1] offen offset:1024 lds\n"
    "buffer_load_dwordx4 v235, s[52:55], %[s1] offen offset:2048 lds\n"
    "buffer_load_dwordx4 v235, s[52:55], %[s1] offen offset:3072 lds\n"
    "LS_%=:\n"
    "v_lshrrev_b32 v176, 4, v224\n"
    "v_lshrrev_b32 v177, 3, v224\n"
    "v_xor_b32 v236, v176, v224\n"
    "v_lshlrev_b32 v178, 2, v177\n"
    "v_and_b32 v236, 7, v236\n"
    "v_or_b32 v178, v178, v177\n"
    "v_lshlrev_b32 v236, 4, v236\n"
    "v_and_b32 v178, 19, v178\n"
    "v_mul_u32_u24 v225, 28672, v178\n"
    "s_lshl_b32 s40, %[jt], 7\n"
    "v_add3_u32 v225, v225, v236, %[sx]\n"
    "s_add_u32 m0, s50, 102400\n"
    "s_nop 0\n"
    "buffer_load_dwordx4 v225, %[dx], s40 offen lds\n"
    "v_xor_b32 v226, 64, v225\n"
    "v_add_u32 v226, 114688, v226\n"
    "s_add_u32 m0, s50, 103424\n"
    "s_nop 0\n"
    "buffer_load_dwordx4 v226, %[dx], s40 offen lds\n"
    "v_mad_u32_u24 v227, v177, %[rs], v236\n"
    "s_lshl_b32 s46, %[rs], 3\n"
    "v_add_u32 v227, %[s2], v227\n"
    "v_and_b32 v176, 1, v176\n"
    "v_xor_b32 v228, 64, v227\n"
    "v_lshlrev_b32 v176, 2, v176\n"
    "v_add_u32 v228, s46, v228\n"
    "v_xor_b32 v176, v176, v224\n"
    "v_mul_u32_u24 v229, 3584, v177\n"
    "v_and_b32 v176, 7, v176\n"
    "v_lshlrev_b32 v176, 4, v176\n"
    "s_add_u32 s51, s51, %[lds]\n"
    "v_add3_u32 v229, v229, v176, %[s1]\n"
    "v_add_u32 v239, 114688, v229\n"
    "s_add_u32 s41, s40, 128\n"
    "s_cmp_eq_u32 s41, 896\n"
    "s_cselect_b32 s41, 0, s41\n"
    "s_add_u32 s52, s41, 128\n"
    "s_cmp_eq_u32 s52, 896\n"
    "s_cselect_b32 s52, 0, s52\n"
    "s_add_u32 s53, s52, 128\n"
    "s_cmp_eq_u32 s53, 896\n"
    "s_cselect_b32 s53, 0, s53\n"
    "s_add_u32 s54, s53, 128\n"
    "s_cmp_eq_u32 s54, 896\n"
    "s_cselect_b32 s54, 0, s54\n"
    "s_add_u32 m0, s50, 110592\n"
    "s_add_u32 s55, s54, 128\n"
    "s_cmp_eq_u32 s55, 896\n"
    "s_cselect_b32 s55, 0, s55\n"
    "buffer_load_dwordx4 v225, %[dx], s41 offen lds\n"
    "s_add_u32 s59, s55, 128\n"
    "s_cmp_eq_u32 s59, 896\n"
    "s_cselect_b32 s59, 0, s59\n"
    "s_lshr_b32 s57, %[w], 1\n"
    "s_lshl_b32 s56, s57, 12\n"
    "s_add_u32 s56, s56, %[lds]\n"
    "s_lshl_b32 s57, s57, 2\n"
    "s_lshl_b32 s58, %[w], 7\n"
    "s_add_u32 m0, s50, 111616\n"
    "s_add_u32 s58, s58, %[lds]\n"
    "buffer_load_dwordx4 v226, %[dx], s41 offen lds\n"
    "s_add_u32 s58, s58, 159744\n"
    "v_and_b32 v176, 31, v224\n"
    "v_add_u32 v176, %[j0], v176\n"
    "v_lshrrev_b32 v177, 1, v176\n"
    "v_cmp_eq_u32 vcc, 1, v176\n"
    "v_mov_b32 v178, 224\n"
    "s_nop 0\n"
    "v_cndmask_b32 v177, v177, v178, vcc\n"
    "s_add_u32 m0, s50, 118784\n"
    "v_and_b32 v178, 1, v176\n"
    "buffer_load_dwordx4 v225, %[dx], s52 offen lds\n"
    "v_cvt_f32_u32 v180, v177\n"
    "v_cndmask_b32_e64 v178, v178, 0, vcc\n"
    "v_mul_u32_u24 v179, s40, v177\n"
    "v_lshrrev_b32 v177, 5, v224\n"
    "v_cvt_f32_u32 v179, v179\n"
    "v_cvt_f32_ubyte0 v181, v178\n"
    "v_mul_f32 v179, 0x3a924925, v179\n"
    "s_add_u32 m0, s50, 119808\n"
    "v_mul_f32 v180, 0x3b124925, v180\n"
    "buffer_load_dwordx4 v226, %[dx], s52 offen lds\n"
    "v_cvt_f32_ubyte0 v177, v177\n"
    "v_mul_f32 v181, 0x3e800000, v181\n"
    "v_mul_f32 v178, 0x41000000, v180\n"
    "v_mul_f32 v182, 0x41800000, v180\n"
    "v_fmac_f32 v181, v178, v177\n"
    "v_fract_f32 v179, v179\n"
    "v_add_f32 v181, v181, v179\n"
    "v_cos_f32 v182, v182\n"
    "s_add_u32 m0, s50, 126976\n"
    "v_cmp_gt_u32 vcc, 2, v176\n"
    "buffer_load_dwordx4 v225, %[dx], s53 offen lds\n"
    "v_mov_b32 v233, 0x3d88d677\n"
    "v_mov_b32 v178, 0x3d41848f\n"
    "v_add_f32 v182, v182, v182\n"
    "v_cndmask_b32 v233, v233, v178, vcc\n"
    "v_fmamk_f32 v201, v180, 0x3f800000, v181\n"
    "v_fmamk_f32 v202, v180, 0x40000000, v181\n"
    "v_fmamk_f32 v203, v180, 0x40400000, v181\n"
    "s_add_u32 m0, s50, 128000\n"
    "v_fmamk_f32 v204, v180, 0x40800000, v181\n"
    "buffer_load_dwordx4 v226, %[dx], s53 offen lds\n"
    "v_fmamk_f32 v205, v180, 0x40a00000, v181\n"
    "v_fmamk_f32 v206, v180, 0x40c00000, v181\n"
    "v_fmamk_f32 v207, v180, 0x40e00000, v181\n"
    "v_cos_f32 v184, v181\n"
    "v_cos_f32 v185, v201\n"
    "s_add_u32 m0, s50, 135168\n"
    "v_cos_f32 v186, v202\n"
    "buffer_load_dwordx4 v225, %[dx], s54 offen lds\n"
    "v_cos_f32 v187, v203\n"
    "v_cos_f32 v188, v204\n"
    "v_cos_f32 v189, v205\n"
    "v_cos_f32 v190, v206\n"
    "s_add_u32 m0, s50, 136192\n"
    "v_cos_f32 v191, v207\n"
    "buffer_load_dwordx4 v226, %[dx], s54 offen lds\n"
    "v_mul_f32 v184, v233, v184\n"
    "v_mul_f32 v185, v233, v185\n"
    "v_mul_f32 v186, v233, v186\n"
    "v_mul_f32 v187, v233, v187\n"
    "v_mul_f32 v188, v233, v188\n"
    "v_mul_f32 v189, v233, v189\n"
    "v_mul_f32 v190, v233, v190\n"
    "v_mul_f32 v191, v233, v191\n"
    "s_add_u32 m0, s50, 143360\n"
    "v_cvt_pk_bf16_f32 v0, v184, v185\n"
    "buffer_load_dwordx4 v225, %[dx], s55 offen lds\n"
    "v_cvt_pk_bf16_f32 v1, v186, v187\n"
    "v_cvt_pk_bf16_f32 v2, v188, v189\n"
    "v_cvt_pk_bf16_f32 v3, v190, v191\n"
    "v_fmamk_f32 v200, v180, 0x41800000, v181\n"
    "v_fmamk_f32 v201, v180, 0x41880000, v181\n"
    "v_fmamk_f32 v202, v180, 0x41900000, v181\n"
    "s_add_u32 m0, s50, 144384\n"
    "v_fmamk_f32 v203, v180, 0x41980000, v181\n"
    "buffer_load_dwordx4 v226, %[dx], s55 offen lds\n"
    "v_fmamk_f32 v204, v180, 0x41a00000, v181\n"
    "v_fmamk_f32 v205, v180, 0x41a80000, v181\n"
    "v_fmamk_f32 v206, v180, 0x41b00000, v181\n"
    "v_fmamk_f32 v207, v180, 0x41b80000, v181\n"
    "v_cos_f32 v192, v200\n"
    "v_cos_f32 v193, v201\n"
    "s_add_u32 m0, s50, 151552\n"
    "v_cos_f32 v194, v202\n"
    "buffer_load_dwordx4 v225, %[dx], s59 offen lds\n"
    "v_cos_f32 v195, v203\n"
    "v_cos_f32 v196, v204\n"
    "v_cos_f32 v197, v205\n"
    "v_cos_f32 v198, v206\n"
    "s_add_u32 m0, s50, 152576\n"
    "v_cos_f32 v199, v207\n"
    "buffer_load_dwordx4 v226, %[dx], s59 offen lds\n"
    "v_mul_f32 v192, v233, v192\n"
    "v_mul_f32 v193, v233, v193\n"
    "v_mul_f32 v194, v233, v194\n"
    "v_mul_f32 v195, v233, v195\n"
    "v_mul_f32 v196, v233, v196\n"
    "v_mul_f32 v197, v233, v197\n"
    "v_mul_f32 v198, v233, v198\n"
    "v_mul_f32 v199, v233, v199\n"
    "v_cvt_pk_bf16_f32 v4, v192, v193\n"
    "v_cvt_pk_bf16_f32 v5, v194, v195\n"
    "v_cvt_pk_bf16_f32 v6, v196, v197\n"
    "v_cvt_pk_bf16_f32 v7, v198, v199\n"
    "v_bfe_u32 v176, v224, 1, 3\n"
    "v_lshrrev_b32 v177, 5, v224\n"
    "v_and_b32 v178, 31, v224\n"
    "v_xor_b32 v176, v176, v177\n"
    "v_lshlrev_b32 v178, 7, v178\n"
    "v_lshl_add_u32 v176, v176, 4, v178\n"
    "v_add_u32 v244, s56, v176\n"
    "v_xor_b32 v245, 32, v244\n"
    "v_xor_b32 v246, 64, v244\n"
    "v_xor_b32 v247, 96, v244\n"
    "v_add_u32 v240, 102400, v244\n"
    "v_add_u32 v241, 102400, v245\n"
    "v_add_u32 v242, 102400, v246\n"
    "v_add_u32 v243, 102400, v247\n"
    "v_pk_fma_f32 v[184:185], v[182:183], v[192:193], v[184:185] op_sel_hi:[0,1,1] neg_lo:[0,0,1] neg_hi:[0,0,1]\n"
    "v_pk_fma_f32 v[186:187], v[182:183], v[194:195], v[186:187] op_sel_hi:[0,1,1] neg_lo:[0,0,1] neg_hi:[0,0,1]\n"
    "v_pk_fma_f32 v[188:189], v[182:183], v[196:197], v[188:189] op_sel_hi:[0,1,1] neg_lo:[0,0,1] neg_hi:[0,0,1]\n"
    "v_pk_fma_f32 v[190:191], v[182:183], v[198:199], v[190:191] op_sel_hi:[0,1,1] neg_lo:[0,0,1] neg_hi:[0,0,1]\n"
    "v_cvt_pk_bf16_f32 v8, v184, v185\n"
    "v_cvt_pk_bf16_f32 v9, v186, v187\n"
    "v_cvt_pk_bf16_f32 v10, v188, v189\n"
    "v_cvt_pk_bf16_f32 v11, v190, v191\n"
    "v_pk_fma_f32 v[192:193], v[182:183], v[184:185], v[192:193] op_sel_hi:[0,1,1] neg_lo:[0,0,1] neg_hi:[0,0,1]\n"
    "v_pk_fma_f32 v[194:195], v[182:183], v[186:187], v[194:195] op_sel_hi:[0,1,1] neg_lo:[0,0,1] neg_hi:[0,0,1]\n"
    "v_pk_fma_f32 v[196:197], v[182:183], v[188:189], v[196:197] op_sel_hi:[0,1,1] neg_lo:[0,0,1] neg_hi:[0,0,1]\n"
    "v_pk_fma_f32 v[198:199], v[182:183], v[190:191], v[198:199] op_sel_hi:[0,1,1] neg_lo:[0,0,1] neg_hi:[0,0,1]\n"
    "v_cvt_pk_bf16_f32 v12, v192, v193\n"
    "v_cvt_pk_bf16_f32 v13, v194, v195\n"
    "v_cvt_pk_bf16_f32 v14, v196, v197\n"
    "v_cvt_pk_bf16_f32 v15, v198, v199\n"
    "v_pk_fma_f32 v[184:185], v[182:183], v[192:193], v[184:185] op_sel_hi:[0,1,1] neg_lo:[0,0,1] neg_hi:[0,0,1]\n"
    "v_pk_fma_f32 v[186:187], v[182:183], v[194:195], v[186:187] op_sel_hi:[0,1,1] neg_lo:[0,0,1] neg_hi:[0,0,1]\n"
    "v_pk_fma_f32 v[188:189], v[182:183], v[196:197], v[188:189] op_sel_hi:[0,1,1] neg_lo:[0,0,1] neg_hi:[0,0,1]\n"
    "v_pk_fma_f32 v[190:191], v[182:183], v[198:199], v[190:191] op_sel_hi:[0,1,1] neg_lo:[0,0,1] neg_hi:[0,0,1]\n"
    "v_cvt_pk_bf16_f32 v16, v184, v185\n"
    "v_cvt_pk_bf16_f32 v17, v186, v187\n"
    "v_cvt_pk_bf16_f32 v18, v188, v189\n"
    "v_cvt_pk_bf16_f32 v19, v190, v191\n"
    "v_pk_fma_f32 v[192:193], v[182:183], v[184:185], v[192:193] op_sel_hi:[0,1,1] neg_lo:[0,0,1] neg_hi:[0,0,1]\n"
    "v_pk_fma_f32 v[194:195], v[182:183], v[186:187], v[194:195] op_sel_hi:[0,1,1] neg_lo:[0,0,1] neg_hi:[0,0,1]\n"
    "v_pk_fma_f32 v[196:197], v[182:183], v[188:189], v[196:197] op_sel_hi:[0,1,1] neg_lo:[0,0,1] neg_hi:[0,0,1]\n"
    "v_pk_fma_f32 v[198:199], v[182:183], v[190:191], v[198:199] op_sel_hi:[0,1,1] neg_lo:[0,0,1] neg_hi:[0,0,1]\n"
    "v_cvt_pk_bf16_f32 v20, v192, v193\n"
    "v_cvt_pk_bf16_f32 v21, v194, v195\n"
    "v_cvt_pk_bf16_f32 v22, v196, v197\n"
    "v_cvt_pk_bf16_f32 v23, v198, v199\n"
    "v_pk_fma_f32 v[184:185], v[182:183], v[192:193], v[184:185] op_sel_hi:[0,1,1] neg_lo:[0,0,1] neg_hi:[0,0,1]\n"
    "v_pk_fma_f32 v[186:187], v[182:183], v[194:195], v[186:187] op_sel_hi:[0,1,1] neg_lo:[0,0,1] neg_hi:[0,0,1]\n"
    "v_pk_fma_f32 v[188:189], v[182:183], v[196:197], v[188:189] op_sel_hi:[0,1,1] neg_lo:[0,0,1] neg_hi:[0,0,1]\n"
    "v_pk_fma_f32 v[190:191], v[182:183], v[198:199], v[190:191] op_sel_hi:[0,1,1] neg_lo:[0,0,1] neg_hi:[0,0,1]\n"
    "v_cvt_pk_bf16_f32 v24, v184, v185\n"
    "v_cvt_pk_bf16_f32 v25, v186, v187\n"
    "v_cvt_pk_bf16_f32 v26, v188, v189\n"
    "v_cvt_pk_bf16_f32 v27, v190, v191\n"
    "v_add_u32 v231, 1, v224\n"
    "v_lshlrev_b32 v232, 30, v224\n"
    "v_lshlrev_b32 v231, 29, v231\n"
    "v_and_b32 v232, 0x80000000, v232\n"
    "v_and_b32 v231, 0x80000000, v231\n"
    "v_or_b32 v232, 1.0, v232\n"
    "v_lshrrev_b32 v177, 16, v231\n"
    "v_or_b32 v231, v231, v177\n"
    "s_cmp_gt_u32 %[tt], 27\n"
    "s_cbranch_scc1 LL_%=\n"
    "s_waitcnt vmcnt(12)\n"
    "s_barrier\n"
    "ds_read_b128 v[160:163], v240 offset:0\n"
    "ds_read_b128 v[164:167], v241 offset:0\n"
    "ds_read_b128 v[168:171], v242 offset:0\n"
    "s_waitcnt lgkmcnt(2)\n"
    "v_mfma_f32_32x32x16_bf16 v[112:127], v[160:163], v[0:3], 0\n"
    "s_add_u32 m0, s50, 0\n"
    "ds_read_b128 v[172:175], v243 offset:0\n"
    "buffer_load_dwordx4 v227, %[d2], s40 offen lds\n"
    "v_xor_b32_dpp v28, v0, v231 quad_perm:[0,1,3,2] row_mask:0xf bank_mask:0xf\n"
    "v_xor_b32_dpp v29, v1, v231 quad_perm:[0,1,3,2] row_mask:0xf bank_mask:0xf\n"
    "s_waitcnt lgkmcnt(2)\n"
    "v_mfma_f32_32x32x16_bf16 v[112:127], v[164:167], v[4:7], v[112:127]\n"
    "s_waitcnt vmcnt(11)\n"
    "s_barrier\n"
    "ds_read_b128 v[160:163], v240 offset:8192\n"
    "v_xor_b32_dpp v30, v2, v231 quad_perm:[0,1,3,2] row_mask:0xf bank_mask:0xf\n"
    "v_xor_b32_dpp v31, v3, v231 quad_perm:[0,1,3,2] row_mask:0xf bank_mask:0xf\n"
    "s_waitcnt lgkmcnt(2)\n"
    "v_mfma_f32_32x32x16_bf16 v[112:127], v[168:171], v[8:11], v[112:127]\n"
    "s_add_u32 m0, s50, 1024\n"
    "ds_read_b128 v[164:167], v241 offset:8192\n"
    "buffer_load_dwordx4 v228, %[d2], s40 offen lds\n"
    "v_xor_b32_dpp v32, v4, v231 quad_perm:[0,1,3,2] row_mask:0xf bank_mask:0xf\n"
    "v_xor_b32_dpp v33, v5, v231 quad_perm:[0,1,3,2] row_mask:0xf bank_mask:0xf\n"
    "s_waitcnt lgkmcnt(2)\n"
    "v_mfma_f32_32x32x16_bf16 v[112:127], v[172:175], v[12:15], v[112:127]\n"
    "ds_read_b128 v[168:171], v242 offset:8192\n"
    "v_xor_b32_dpp v34, v6, v231 quad_perm:[0,1,3,2] row_mask:0xf bank_mask:0xf\n"
    "v_xor_b32_dpp v35, v7, v231 quad_perm:[0,1,3,2] row_mask:0xf bank_mask:0xf\n"
    "s_waitcnt lgkmcnt(2)\n"
    "v_mfma_f32_32x32x16_bf16 v[112:127], v[160:163], v[16:19], v[112:127]\n"
    "s_add_u32 m0, s51, 57344\n"
    "s_mul_i32 s47, s40, 1792\n"
    "buffer_load_dwordx4 v229, %[d1], s47 offen lds\n"
    "ds_read_b128 v[172:175], v243 offset:8192\n"
    "v_xor_b32_dpp v36, v8, v231 quad_perm:[0,1,3,2] row_mask:0xf bank_mask:0xf\n"
    "v_xor_b32_dpp v37, v9, v231 quad_perm:[0,1,3,2] row_mask:0xf bank_mask:0xf\n"
    "s_waitcnt lgkmcnt(2)\n"
    "v_mfma_f32_32x32x16_bf16 v[112:127], v[164:167], v[20:23], v[112:127]\n"
    "s_waitcnt vmcnt(11)\n"
    "s_barrier\n"
    "ds_read_b128 v[160:163], v240 offset:16384\n"
    "v_xor_b32_dpp v38, v10, v231 quad_perm:[0,1,3,2] row_mask:0xf bank_mask:0xf\n"
    "v_xor_b32_dpp v39, v11, v231 quad_perm:[0,1,3,2] row_mask:0xf bank_mask:0xf\n"
    "s_waitcnt lgkmcnt(2)\n"
    "v_mfma_f32_32x32x16_bf16 v[112:127], v[168:171], v[24:27], v[112:127]\n"
    "s_add_u32 m0, s51, 61440\n"
    "ds_read_b128 v[164:167], v241 offset:16384\n"
    "buffer_load_dwordx4 v239, %[d1], s47 offen lds\n"
    "v_xor_b32_dpp v40, v12, v231 quad_perm:[0,1,3,2] row_mask:0xf bank_mask:0xf\n"
    "v_xor_b32_dpp v41, v13, v231 quad_perm:[0,1,3,2] row_mask:0xf bank_mask:0xf\n"
    "v_xor_b32_dpp v42, v14, v231 quad_perm:[0,1,3,2] row_mask:0xf bank_mask:0xf\n"
    "v_xor_b32_dpp v43, v15, v231 quad_perm:[0,1,3,2] row_mask:0xf bank_mask:0xf\n"
    "v_xor_b32_dpp v44, v16, v231 quad_perm:[0,1,3,2] row_mask:0xf bank_mask:0xf\n"
    "v_xor_b32_dpp v45, v17, v231 quad_perm:[0,1,3,2] row_mask:0xf bank_mask:0xf\n"
    "v_xor_b32_dpp v46, v18, v231 quad_perm:[0,1,3,2] row_mask:0xf bank_mask:0xf\n"
    "v_xor_b32_dpp v47, v19, v231 quad_perm:[0,1,3,2] row_mask:0xf bank_mask:0xf\n"
    "v_xor_b32_dpp v48, v20, v231 quad_perm:[0,1,3,2] row_mask:0xf bank_mask:0xf\n"
    "v_xor_b32_dpp v49, v21, v231 quad_perm:[0,1,3,2] row_mask:0xf bank_mask:0xf\n"
    "v_xor_b32_dpp v50, v22, v231 quad_perm:[0,1,3,2] row_mask:0xf bank_mask:0xf\n"
    "v_xor_b32_dpp v51, v23, v231 quad_perm:[0,1,3,2] row_mask:0xf bank_mask:0xf\n"
    "v_xor_b32_dpp v52, v24, v231 quad_perm:[0,1,3,2] row_mask:0xf bank_mask:0xf\n"
    "v_xor_b32_dpp v53, v25, v231 quad_perm:[0,1,3,2] row_mask:0xf bank_mask:0xf\n"
    "v_xor_b32_dpp v54, v26, v231 quad_perm:[0,1,3,2] row_mask:0xf bank_mask:0xf\n"
    "v_xor_b32_dpp v55, v27, v231 quad_perm:[0,1,3,2] row_mask:0xf bank_mask:0xf\n"
    "s_waitcnt lgkmcnt(2)\n"
    "v_mfma_f32_32x32x16_bf16 v[112:127], v[172:175], v[28:31], v[112:127]\n"
    "ds_read_b128 v[168:171], v242 offset:16384\n"
    "v_bfe_u32 v176, v224, 2, 2\n"
    "v_lshrrev_b32 v177, 5, v224\n"
    "v_bfe_u32 v178, v224, 1, 1\n"
    "s_waitcnt lgkmcnt(2)\n"
    "v_mfma_f32_32x32x16_bf16 v[112:127], v[160:163], v[32:35], v[112:127]\n"
    "s_add_u32 m0, s50, 8192\n"
    "ds_read_b128 v[172:175], v243 offset:16384\n"
    "buffer_load_dwordx4 v227, %[d2], s41 offen lds\n"
    "v_lshl_add_u32 v176, v177, 3, v176\n"
    "v_lshrrev_b32 v177, 3, v224\n"
    "v_lshlrev_b32 v230, 7, v176\n"
    "s_waitcnt lgkmcnt(2)\n"
    "v_mfma_f32_32x32x16_bf16 v[112:127], v[164:167], v[36:39], v[112:127]\n"
    "s_waitcnt vmcnt(11)\n"
    "s_barrier\n"
    "ds_read_b128 v[160:163], v240 offset:24576\n"
    "v_and_b32 v176, 2, v177\n"
    "v_and_b32 v177, 1, v177\n"
    "v_add3_u32 v178, v178, v176, s57\n"
    "s_waitcnt lgkmcnt(2)\n"
    "v_mfma_f32_32x32x16_bf16 v[112:127], v[168:171], v[40:43], v[112:127]\n"
    "s_add_u32 m0, s50, 9216\n"
    "ds_read_b128 v[164:167], v241 offset:24576\n"
    "buffer_load_dwordx4 v228, %[d2], s41 offen lds\n"
    "v_lshlrev_b32 v177, 2, v177\n"
    "v_and_b32 v176, 1, v224\n"
    "v_xor_b32 v178, v178, v177\n"
    "s_waitcnt lgkmcnt(2)\n"
    "v_mfma_f32_32x32x16_bf16 v[112:127], v[172:175], v[44:47], v[112:127]\n"
    "ds_read_b128 v[168:171], v242 offset:24576\n"
    "v_lshl_add_u32 v230, v176, 3, v230\n"
    "s_add_u32 s46, %[lds], 57344\n"
    "v_lshl_add_u32 v230, v178, 4, v230\n"
    "s_waitcnt lgkmcnt(2)\n"
    "v_mfma_f32_32x32x16_bf16 v[112:127], v[160:163], v[48:51], v[112:127]\n"
    "s_add_u32 m0, s50, 16384\n"
    "ds_read_b128 v[172:175], v243 offset:24576\n"
    "buffer_load_dwordx4 v227, %[d2], s52 offen lds\n"
    "v_add_u32 v230, s46, v230\n"
    "v_and_b32 v176, 15, v224\n"
    "v_bfe_u32 v177, v224, 4, 1\n"
    "s_waitcnt lgkmcnt(2)\n"
    "v_mfma_f32_32x32x16_bf16 v[112:127], v[164:167], v[52:55], v[112:127]\n"
    "s_waitcnt vmcnt(11)\n"
    "s_barrier\n"
    "ds_read_b128 v[160:163], v240 offset:32768\n"
    "v_lshrrev_b32 v178, 5, v224\n"
    "v_lshlrev_b32 v234, 6, v176\n"
    "v_lshl_add_u32 v234, v177, 5, v234\n"
    "s_waitcnt lgkmcnt(2)\n"
    "v_mfma_f32_32x32x16_bf16 v[96:111], v[168:171], v[0:3], 0\n"
    "s_add_u32 m0, s50, 17408\n"
    "ds_read_b128 v[164:167], v241 offset:32768\n"
    "buffer_load_dwordx4 v228, %[d2], s52 offen lds\n"
    "v_lshl_add_u32 v234, v178, 4, v234\n"
    "s_cmp_eq_u32 %[j0], 0\n"
    "s_cselect_b32 s42, 0xfffefffe, -1\n"
    "s_cselect_b32 s44, -1, 0\n"
    "s_waitcnt lgkmcnt(2)\n"
    "v_mfma_f32_32x32x16_bf16 v[96:111], v[172:175], v[4:7], v[96:111]\n"
    "ds_read_b128 v[168:171], v242 offset:32768\n"
    "s_mov_b32 s43, s42\n"
    "s_mov_b32 s45, 0\n"
    "s_mov_b32 s48, 0xcccccccc\n"
    "s_waitcnt lgkmcnt(2)\n"
    "v_mfma_f32_32x32x16_bf16 v[96:111], v[160:163], v[8:11], v[96:111]\n"
    "s_add_u32 m0, s50, 24576\n"
    "ds_read_b128 v[172:175], v243 offset:32768\n"
    "buffer_load_dwordx4 v227, %[d2], s53 offen lds\n"
    "s_mov_b32 s49, 0xcccccccc\n"
    "s_waitcnt lgkmcnt(2)\n"
    "v_mfma_f32_32x32x16_bf16 v[96:111], v[164:167], v[12:15], v[96:111]\n"
    "s_waitcnt vmcnt(11)\n"
    "s_barrier\n"
    "ds_read_b128 v[160:163], v240 offset:40960\n"
    "s_waitcnt lgkmcnt(2)\n"
    "v_mfma_f32_32x32x16_bf16 v[96:111], v[168:171], v[16:19], v[96:111]\n"
    "s_add_u32 m0, s50, 25600\n"
    "ds_read_b128 v[164:167], v241 offset:40960\n"
    "buffer_load_dwordx4 v228, %[d2], s53 offen lds\n"
    "s_waitcnt lgkmcnt(2)\n"
    "v_mfma_f32_32x32x16_bf16 v[96:111], v[172:175], v[20:23], v[96:111]\n"
    "ds_read_b128 v[168:171], v242 offset:40960\n"
    "s_waitcnt lgkmcnt(2)\n"
    "v_mfma_f32_32x32x16_bf16 v[96:111], v[160:163], v[24:27], v[96:111]\n"
    "s_add_u32 m0, s50, 32768\n"
    "ds_read_b128 v[172:175], v243 offset:40960\n"
    "buffer_load_dwordx4 v227, %[d2], s54 offen lds\n"
    "s_waitcnt lgkmcnt(2)\n"
    "v_mfma_f32_32x32x16_bf16 v[96:111], v[164:167], v[28:31], v[96:111]\n"
    "s_waitcnt vmcnt(11)\n"
    "s_barrier\n"
    "ds_read_b128 v[160:163], v240 offset:49152\n"
    "s_waitcnt lgkmcnt(2)\n"
    "v_mfma_f32_32x32x16_bf16 v[96:111], v[168:171], v[32:35], v[96:111]\n"
    "s_add_u32 m0, s50, 33792\n"
    "ds_read_b128 v[164:167], v241 offset:49152\n"
    "buffer_load_dwordx4 v228, %[d2], s54 offen lds\n"
    "s_waitcnt lgkmcnt(2)\n"
    "v_mfma_f32_32x32x16_bf16 v[96:111], v[172:175], v[36:39], v[96:111]\n"
    "ds_read_b128 v[168:171], v242 offset:49152\n"
    "s_waitcnt lgkmcnt(2)\n"
    "v_mfma_f32_32x32x16_bf16 v[96:111], v[160:163], v[40:43], v[96:111]\n"
    "s_add_u32 m0, s50, 40960\n"
    "ds_read_b128 v[172:175], v243 offset:49152\n"
    "buffer_load_dwordx4 v227, %[d2], s55 offen lds\n"
    "s_waitcnt lgkmcnt(2)\n"
    "v_mfma_f32_32x32x16_bf16 v[96:111], v[164:167], v[44:47], v[96:111]\n"
    "s_waitcnt vmcnt(11)\n"
    "s_barrier\n"
    "ds_read_b128 v[160:163], v244 offset:0\n"
    "s_waitcnt lgkmcnt(2)\n"
    "v_mfma_f32_32x32x16_bf16 v[96:111], v[168:171], v[48:51], v[96:111]\n"
    "s_add_u32 m0, s50, 41984\n"
    "ds_read_b128 v[164:167], v245 offset:0\n"
    "buffer_load_dwordx4 v228, %[d2], s55 offen lds\n"
    "s_waitcnt lgkmcnt(2)\n"
    "v_mfma_f32_32x32x16_bf16 v[96:111], v[172:175], v[52:55], v[96:111]\n"
    "ds_read_b128 v[168:171], v246 offset:0\n"
    "s_waitcnt lgkmcnt(2)\n"
    "v_mfma_f32_32x32x16_bf16 v[128:143], v[160:163], v[0:3], 0\n"
    "s_add_u32 m0, s50, 49152\n"
    "ds_read_b128 v[172:175], v247 offset:0\n"
    "buffer_load_dwordx4 v227, %[d2], s59 offen lds\n"
    "s_waitcnt lgkmcnt(2)\n"
    "v_mfma_f32_32x32x16_bf16 v[128:143], v[164:167], v[4:7], v[128:143]\n"
    "s_waitcnt vmcnt(9)\n"
    "s_barrier\n"
    "ds_read_b128 v[160:163], v244 offset:8192\n"
    "s_waitcnt lgkmcnt(2)\n"
    "v_mfma_f32_32x32x16_bf16 v[128:143], v[168:171], v[8:11], v[128:143]\n"
    "s_add_u32 m0, s50, 50176\n"
    "ds_read_b128 v[164:167], v245 offset:8192\n"
    "buffer_load_dwordx4 v228, %[d2], s59 offen lds\n"
    "v_fmac_f32 v112, v232, v96\n"
    "v_fmac_f32 v113, v232, v97\n"
    "v_fmac_f32 v114, v232, v98\n"
    "s_waitcnt lgkmcnt(2)\n"
    "v_mfma_f32_32x32x16_bf16 v[128:143], v[172:175], v[12:15], v[128:143]\n"
    "ds_read_b128 v[168:171], v246 offset:8192\n"
    "v_fmac_f32 v115, v232, v99\n"
    "v_fmac_f32 v116, v232, v100\n"
    "v_fmac_f32 v117, v232, v101\n"
    "s_waitcnt lgkmcnt(2)\n"
    "v_mfma_f32_32x32x16_bf16 v[128:143], v[160:163], v[16:19], v[128:143]\n"
    "s_add_u32 m0, s51, 65536\n"
    "s_mul_i32 s47, s41, 1792\n"
    "buffer_load_dwordx4 v229, %[d1], s47 offen lds\n"
    "ds_read_b128 v[172:175], v247 offset:8192\n"
    "v_fmac_f32 v118, v232, v102\n"
    "v_fmac_f32 v119, v232, v103\n"
    "v_fmac_f32 v120, v232, v104\n"
    "s_waitcnt lgkmcnt(2)\n"
    "v_mfma_f32_32x32x16_bf16 v[128:143], v[164:167], v[20:23], v[128:143]\n"
    "s_waitcnt vmcnt(9)\n"
    "s_barrier\n"
    "ds_read_b128 v[160:163], v244 offset:16384\n"
    "v_fmac_f32 v121, v232, v105\n"
    "v_fmac_f32 v122, v232, v106\n"
    "v_fmac_f32 v123, v232, v107\n"
    "s_waitcnt lgkmcnt(2)\n"
    "v_mfma_f32_32x32x16_bf16 v[128:143], v[168:171], v[24:27], v[128:143]\n"
    "s_add_u32 m0, s51, 69632\n"
    "ds_read_b128 v[164:167], v245 offset:16384\n"
    "buffer_load_dwordx4 v239, %[d1], s47 offen lds\n"
    "v_fmac_f32 v124, v232, v108\n"
    "v_fmac_f32 v125, v232, v109\n"
    "v_fmac_f32 v126, v232, v110\n"
    "s_waitcnt lgkmcnt(2)\n"
    "v_mfma_f32_32x32x16_bf16 v[128:143], v[172:175], v[28:31], v[128:143]\n"
    "ds_read_b128 v[168:171], v246 offset:16384\n"
    "v_fmac_f32 v127, v232, v111\n"
    "v_cvt_pk_bf16_f32 v184, v112, v113\n"
    "v_cvt_pk_bf16_f32 v185, v114, v115\n"
    "s_waitcnt lgkmcnt(2)\n"
    "v_mfma_f32_32x32x16_bf16 v[128:143], v[160:163], v[32:35], v[128:143]\n"
    "s_add_u32 m0, s51, 73728\n"
    "s_mul_i32 s47, s52, 1792\n"
    "buffer_load_dwordx4 v229, %[d1], s47 offen lds\n"
    "ds_read_b128 v[172:175], v247 offset:16384\n"
    "v_cvt_pk_bf16_f32 v186, v116, v117\n"
    "v_cvt_pk_bf16_f32 v187, v118, v119\n"
    "s_waitcnt lgkmcnt(2)\n"
    "v_mfma_f32_32x32x16_bf16 v[128:143], v[164:167], v[36:39], v[128:143]\n"
    "s_waitcnt vmcnt(9)\n"
    "s_barrier\n"
    "ds_read_b128 v[160:163], v244 offset:24576\n"
    "v_cvt_pk_bf16_f32 v188, v120, v121\n"
    "v_cvt_pk_bf16_f32 v189, v122, v123\n"
    "s_waitcnt lgkmcnt(2)\n"
    "v_mfma_f32_32x32x16_bf16 v[128:143], v[168:171], v[40:43], v[128:143]\n"
    "s_add_u32 m0, s51, 77824\n"
    "ds_read_b128 v[164:167], v245 offset:24576\n"
    "buffer_load_dwordx4 v239, %[d1], s47 offen lds\n"
    "v_cvt_pk_bf16_f32 v190, v124, v125\n"
    "v_cvt_pk_bf16_f32 v191, v126, v127\n"
    "s_waitcnt lgkmcnt(2)\n"
    "v_mfma_f32_32x32x16_bf16 v[128:143], v[172:175], v[44:47], v[128:143]\n"
    "ds_read_b128 v[168:171], v246 offset:24576\n"
    "v_permlane16_swap_b32 v112, v120\n"
    "v_permlane16_swap_b32 v113, v121\n"
    "v_permlane16_swap_b32 v114, v122\n"
    "s_waitcnt lgkmcnt(2)\n"
    "v_mfma_f32_32x32x16_bf16 v[128:143], v[160:163], v[48:51], v[128:143]\n"
    "s_add_u32 m0, s51, 81920\n"
    "s_mul_i32 s47, s53, 1792\n"
    "buffer_load_dwordx4 v229, %[d1], s47 offen lds\n"
    "ds_read_b128 v[172:175], v247 offset:24576\n"
    "v_permlane16_swap_b32 v115, v123\n"
    "v_permlane16_swap_b32 v116, v124\n"
    "v_permlane16_swap_b32 v117, v125\n"
    "s_waitcnt lgkmcnt(2)\n"
    "v_mfma_f32_32x32x16_bf16 v[128:143], v[164:167], v[52:55], v[128:143]\n"
    "s_waitcnt vmcnt(9)\n"
    "s_barrier\n"
    "ds_read_b128 v[160:163], v244 offset:32768\n"
    "v_permlane16_swap_b32 v118, v126\n"
    "v_permlane16_swap_b32 v119, v127\n"
    "v_permlane32_swap_b32 v184, v188\n"
    "s_waitcnt lgkmcnt(2)\n"
    "v_mfma_f32_32x32x16_bf16 v[144:159], v[168:171], v[0:3], 0\n"
    "s_add_u32 m0, s51, 86016\n"
    "ds_read_b128 v[164:167], v245 offset:32768\n"
    "buffer_load_dwordx4 v239, %[d1], s47 offen lds\n"
    "v_permlane32_swap_b32 v185, v189\n"
    "v_permlane32_swap_b32 v186, v190\n"
    "v_permlane32_swap_b32 v187, v191\n"
    "s_waitcnt lgkmcnt(2)\n"
    "v_mfma_f32_32x32x16_bf16 v[144:159], v[172:175], v[4:7], v[144:159]\n"
    "ds_read_b128 v[168:171], v246 offset:32768\n"
    "v_mul_f32 v192, v120, v120\n"
    "v_mul_f32 v193, v121, v121\n"
    "v_mul_f32 v194, v122, v122\n"
    "s_waitcnt lgkmcnt(2)\n"
    "v_mfma_f32_32x32x16_bf16 v[144:159], v[160:163], v[8:11], v[144:159]\n"
    "s_add_u32 m0, s51, 90112\n"
    "s_mul_i32 s47, s54, 1792\n"
    "buffer_load_dwordx4 v229, %[d1], s47 offen lds\n"
    "ds_read_b128 v[172:175], v247 offset:32768\n"
    "v_mul_f32 v195, v123, v123\n"
    "v_permlane16_swap_b32 v184, v188\n"
    "v_permlane16_swap_b32 v185, v189\n"
    "s_waitcnt lgkmcnt(2)\n"
    "v_mfma_f32_32x32x16_bf16 v[144:159], v[164:167], v[12:15], v[144:159]\n"
    "s_waitcnt vmcnt(9)\n"
    "s_barrier\n"
    "ds_read_b128 v[160:163], v244 offset:40960\n"
    "v_permlane16_swap_b32 v186, v190\n"
    "v_permlane16_swap_b32 v187, v191\n"
    "v_mul_f32 v196, v124, v124\n"
    "s_waitcnt lgkmcnt(2)\n"
    "v_mfma_f32_32x32x16_bf16 v[144:159], v[168:171], v[16:19], v[144:159]\n"
    "s_add_u32 m0, s51, 94208\n"
    "ds_read_b128 v[164:167], v245 offset:40960\n"
    "buffer_load_dwordx4 v239, %[d1], s47 offen lds\n"
    "v_mul_f32 v197, v125, v125\n"
    "v_mul_f32 v198, v126, v126\n"
    "v_mul_f32 v199, v127, v127\n"
    "s_waitcnt lgkmcnt(2)\n"
    "v_mfma_f32_32x32x16_bf16 v[144:159], v[172:175], v[20:23], v[144:159]\n"
    "ds_read_b128 v[168:171], v246 offset:40960\n"
    "buffer_store_dwordx4 v[184:187], v234, %[ws], %[xo] offen offset:0 sc1\n"
    "buffer_store_dwordx4 v[188:191], v234, %[ws], %[xo] offen offset:1024 sc1\n"
    "s_mov_b64 exec, s[42:43]\n"
    "v_fmac_f32 v192, v112, v112\n"
    "v_fmac_f32 v193, v113, v113\n"
    "v_fmac_f32 v194, v114, v114\n"
    "v_fmac_f32 v195, v115, v115\n"
    "s_mov_b64 exec, -1\n"
    "s_waitcnt lgkmcnt(2)\n"
    "s_nop 2\n"
    "v_mfma_f32_32x32x16_bf16 v[144:159], v[160:163], v[24:27], v[144:159]\n"
    "s_add_u32 m0, s51, 98304\n"
    "s_mul_i32 s47, s55, 1792\n"
    "buffer_load_dwordx4 v229, %[d1], s47 offen lds\n"
    "ds_read_b128 v[172:175], v247 offset:40960\n"
    "s_mov_b64 exec, s[42:43]\n"
    "v_fmac_f32 v196, v116, v116\n"
    "v_fmac_f32 v197, v117, v117\n"
    "v_fmac_f32 v198, v118, v118\n"
    "v_fmac_f32 v199, v119, v119\n"
    "s_mov_b64 exec, -1\n"
    "s_waitcnt lgkmcnt(2)\n"
    "s_nop 2\n"
    "v_mfma_f32_32x32x16_bf16 v[144:159], v[164:167], v[28:31], v[144:159]\n"
    "s_waitcnt vmcnt(11)\n"
    "s_barrier\n"
    "ds_read_b128 v[160:163], v244 offset:49152\n"
    "v_lshrrev_b32 v176, 1, v224\n"
    "v_lshl_add_u32 v176, v176, 2, s58\n"
    "v_add_f32_dpp v112, v192, v192 row_ror:8 row_mask:0xf bank_mask:0x3\n"
    "s_waitcnt lgkmcnt(2)\n"
    "v_mfma_f32_32x32x16_bf16 v[144:159], v[168:171], v[32:35], v[144:159]\n"
    "s_add_u32 m0, s51, 102400\n"
    "ds_read_b128 v[164:167], v245 offset:49152\n"
    "buffer_load_dwordx4 v239, %[d1], s47 offen lds\n"
    "v_add_f32_dpp v113, v193, v193 row_ror:8 row_mask:0xf bank_mask:0x3\n"
    "v_add_f32_dpp v114, v194, v194 row_ror:8 row_mask:0xf bank_mask:0x3\n"
    "s_waitcnt lgkmcnt(2)\n"
    "v_mfma_f32_32x32x16_bf16 v[144:159], v[172:175], v[36:39], v[144:159]\n"
    "ds_read_b128 v[168:171], v246 offset:49152\n"
    "v_add_f32_dpp v115, v195, v195 row_ror:8 row_mask:0xf bank_mask:0x3\n"
    "v_add_f32_dpp v112, v196, v196 row_ror:8 row_mask:0xf bank_mask:0xc\n"
    "s_waitcnt lgkmcnt(2)\n"
    "v_mfma_f32_32x32x16_bf16 v[144:159], v[160:163], v[40:43], v[144:159]\n"
    "s_add_u32 m0, s51, 106496\n"
    "s_mul_i32 s47, s59, 1792\n"
    "buffer_load_dwordx4 v229, %[d1], s47 offen lds\n"
    "ds_read_b128 v[172:175], v247 offset:49152\n"
    "v_add_f32_dpp v113, v197, v197 row_ror:8 row_mask:0xf bank_mask:0xc\n"
    "v_add_f32_dpp v114, v198, v198 row_ror:8 row_mask:0xf bank_mask:0xc\n"
    "s_waitcnt lgkmcnt(2)\n"
    "v_mfma_f32_32x32x16_bf16 v[144:159], v[164:167], v[44:47], v[144:159]\n"
    "s_barrier\n"
    "ds_read_b64_tr_b16 v[160:161], v230 offset:0\n"
    "ds_read_b64_tr_b16 v[162:163], v230 offset:512\n"
    "v_add_f32_dpp v115, v199, v199 row_ror:8 row_mask:0xf bank_mask:0xc\n"
    "v_add_f32_dpp v192, v112, v112 row_half_mirror row_mask:0xf bank_mask:0x5\n"
    "s_waitcnt lgkmcnt(3)\n"
    "v_mfma_f32_32x32x16_bf16 v[144:159], v[168:171], v[48:51], v[144:159]\n"
    "s_add_u32 m0, s51, 110592\n"
    "ds_read_b64_tr_b16 v[164:165], v230 offset:2048\n"
    "ds_read_b64_tr_b16 v[166:167], v230 offset:2560\n"
    "buffer_load_dwordx4 v239, %[d1], s47 offen lds\n"
    "v_add_f32_dpp v193, v113, v113 row_half_mirror row_mask:0xf bank_mask:0x5\n"
    "v_add_f32_dpp v192, v114, v114 row_half_mirror row_mask:0xf bank_mask:0xa\n"
    "s_waitcnt lgkmcnt(4)\n"
    "v_mfma_f32_32x32x16_bf16 v[144:159], v[172:175], v[52:55], v[144:159]\n"
    "ds_read_b64_tr_b16 v[168:169], v230 offset:4096\n"
    "ds_read_b64_tr_b16 v[170:171], v230 offset:4608\n"
    "v_add_f32_dpp v193, v115, v115 row_half_mirror row_mask:0xf bank_mask:0xa\n"
    "v_cndmask_b32_e64 v112, v192, v193, s[48:49]\n"
    "s_waitcnt lgkmcnt(4)\n"
    "v_mfma_f32_32x32x16_bf16 v[64:79], v[0:3], v[160:163], 0\n"
    "ds_read_b64_tr_b16 v[172:173], v230 offset:6144\n"
    "ds_read_b64_tr_b16 v[174:175], v230 offset:6656\n"
    "v_cndmask_b32_e64 v113, v193, v192, s[48:49]\n"
    "s_nop 1\n"
    "v_add_f32_dpp v112, v113, v112 quad_perm:[2,3,0,1] row_mask:0xf bank_mask:0xf\n"
    "s_waitcnt lgkmcnt(4)\n"
    "v_mfma_f32_32x32x16_bf16 v[64:79], v[4:7], v[164:167], v[64:79]\n"
    "s_waitcnt vmcnt(12)\n"
    "s_barrier\n"
    "ds_read_b64_tr_b16 v[160:161], v230 offset:8192\n"
    "ds_read_b64_tr_b16 v[162:163], v230 offset:8704\n"
    "v_add_f32_dpp v112, v112, v112 quad_perm:[1,0,3,2] row_mask:0xf bank_mask:0xf\n"
    "ds_write_b32 v176, v112\n"
    "s_waitcnt lgkmcnt(5)\n"
    "v_mfma_f32_32x32x16_bf16 v[64:79], v[8:11], v[168:171], v[64:79]\n"
    "ds_read_b64_tr_b16 v[164:165], v230 offset:10240\n"
    "ds_read_b64_tr_b16 v[166:167], v230 offset:10752\n"
    "v_fmac_f32 v128, v232, v144\n"
    "v_fmac_f32 v129, v232, v145\n"
    "v_fmac_f32 v130, v232, v146\n"
    "s_waitcnt lgkmcnt(5)\n"
    "v_mfma_f32_32x32x16_bf16 v[64:79], v[12:15], v[172:175], v[64:79]\n"
    "ds_read_b64_tr_b16 v[168:169], v230 offset:12288\n"
    "ds_read_b64_tr_b16 v[170:171], v230 offset:12800\n"
    "v_fmac_f32 v131, v232, v147\n"
    "v_fmac_f32 v132, v232, v148\n"
    "v_fmac_f32 v133, v232, v149\n"
    "s_waitcnt lgkmcnt(5)\n"
    "v_mfma_f32_32x32x16_bf16 v[64:79], v[16:19], v[160:163], v[64:79]\n"
    "ds_read_b64_tr_b16 v[172:173], v230 offset:14336\n"
    "ds_read_b64_tr_b16 v[174:175], v230 offset:14848\n"
    "v_fmac_f32 v134, v232, v150\n"
    "v_fmac_f32 v135, v232, v151\n"
    "v_fmac_f32 v136, v232, v152\n"
    "s_waitcnt lgkmcnt(4)\n"
    "v_mfma_f32_32x32x16_bf16 v[64:79], v[20:23], v[164:167], v[64:79]\n"
    "s_waitcnt vmcnt(10)\n"
    "s_barrier\n"
    "ds_read_b64_tr_b16 v[160:161], v230 offset:16384\n"
    "ds_read_b64_tr_b16 v[162:163], v230 offset:16896\n"
    "v_fmac_f32 v137, v232, v153\n"
    "v_fmac_f32 v138, v232, v154\n"
    "v_fmac_f32 v139, v232, v155\n"
    "s_waitcnt lgkmcnt(4)\n"
    "v_mfma_f32_32x32x16_bf16 v[64:79], v[24:27], v[168:171], v[64:79]\n"
    "ds_read_b64_tr_b16 v[164:165], v230 offset:18432\n"
    "ds_read_b64_tr_b16 v[166:167], v230 offset:18944\n"
    "v_fmac_f32 v140, v232, v156\n"
    "v_fmac_f32 v141, v232, v157\n"
    "v_fmac_f32 v142, v232, v158\n"
    "s_waitcnt lgkmcnt(4)\n"
    "v_mfma_f32_32x32x16_bf16 v[64:79], v[28:31], v[172:175], v[64:79]\n"
    "ds_read_b64_tr_b16 v[168:169], v230 offset:20480\n"
    "ds_read_b64_tr_b16 v[170:171], v230 offset:20992\n"
    "v_fmac_f32 v143, v232, v159\n"
    "v_cvt_pk_bf16_f32 v184, v128, v129\n"
    "v_cvt_pk_bf16_f32 v185, v130, v131\n"
    "s_waitcnt lgkmcnt(4)\n"
    "v_mfma_f32_32x32x16_bf16 v[64:79], v[32:35], v[160:163], v[64:79]\n"
    "ds_read_b64_tr_b16 v[172:173], v230 offset:22528\n"
    "ds_read_b64_tr_b16 v[174:175], v230 offset:23040\n"
    "v_cvt_pk_bf16_f32 v186, v132, v133\n"
    "v_cvt_pk_bf16_f32 v187, v134, v135\n"
    "s_waitcnt lgkmcnt(4)\n"
    "v_mfma_f32_32x32x16_bf16 v[64:79], v[36:39], v[164:167], v[64:79]\n"
    "s_waitcnt vmcnt(8)\n"
    "s_barrier\n"
    "ds_read_b64_tr_b16 v[160:161], v230 offset:24576\n"
    "ds_read_b64_tr_b16 v[162:163], v230 offset:25088\n"
    "v_cvt_pk_bf16_f32 v188, v136, v137\n"
    "v_cvt_pk_bf16_f32 v189, v138, v139\n"
    "s_waitcnt lgkmcnt(4)\n"
    "v_mfma_f32_32x32x16_bf16 v[64:79], v[40:43], v[168:171], v[64:79]\n"
    "ds_read_b64_tr_b16 v[164:165], v230 offset:26624\n"
    "ds_read_b64_tr_b16 v[166:167], v230 offset:27136\n"
    "v_cvt_pk_bf16_f32 v190, v140, v141\n"
    "v_cvt_pk_bf16_f32 v191, v142, v143\n"
    "s_waitcnt lgkmcnt(4)\n"
    "v_mfma_f32_32x32x16_bf16 v[64:79], v[44:47], v[172:175], v[64:79]\n"
    "ds_read_b64_tr_b16 v[168:169], v230 offset:28672\n"
    "ds_read_b64_tr_b16 v[170:171], v230 offset:29184\n"
    "buffer_store_dwordx4 v[184:187], v235, %[ws], %[po] offen offset:0 sc1\n"
    "buffer_store_dwordx4 v[188:191], v235, %[ws], %[po] offen offset:1024 sc1\n"
    "s_waitcnt lgkmcnt(4)\n"
    "v_mfma_f32_32x32x16_bf16 v[64:79], v[48:51], v[160:163], v[64:79]\n"
    "ds_read_b64_tr_b16 v[172:173], v230 offset:30720\n"
    "ds_read_b64_tr_b16 v[174:175], v230 offset:31232\n"
    "s_waitcnt lgkmcnt(4)\n"
    "v_mfma_f32_32x32x16_bf16 v[64:79], v[52:55], v[164:167], v[64:79]\n"
    "s_waitcnt vmcnt(8)\n"
    "s_barrier\n"
    "ds_read_b64_tr_b16 v[160:161], v230 offset:32768\n"
    "ds_read_b64_tr_b16 v[162:163], v230 offset:33280\n"
    "s_waitcnt lgkmcnt(4)\n"
    "v_mfma_f32_32x32x16_bf16 v[80:95], v[0:3], v[168:171], 0\n"
    "ds_read_b64_tr_b16 v[164:165], v230 offset:34816\n"
    "ds_read_b64_tr_b16 v[166:167], v230 offset:35328\n"
    "s_waitcnt lgkmcnt(4)\n"
    "v_mfma_f32_32x32x16_bf16 v[80:95], v[4:7], v[172:175], v[80:95]\n"
    "ds_read_b64_tr_b16 v[168:169], v230 offset:36864\n"
    "ds_read_b64_tr_b16 v[170:171], v230 offset:37376\n"
    "s_waitcnt lgkmcnt(4)\n"
    "v_mfma_f32_32x32x16_bf16 v[80:95], v[8:11], v[160:163], v[80:95]\n"
    "ds_read_b64_tr_b16 v[172:173], v230 offset:38912\n"
    "ds_read_b64_tr_b16 v[174:175], v230 offset:39424\n"
    "s_waitcnt lgkmcnt(4)\n"
    "v_mfma_f32_32x32x16_bf16 v[80:95], v[12:15], v[164:167], v[80:95]\n"
    "s_waitcnt vmcnt(4)\n"
    "s_barrier\n"
    "ds_read_b64_tr_b16 v[160:161], v230 offset:40960\n"
    "ds_read_b64_tr_b16 v[162:163], v230 offset:41472\n"
    "s_waitcnt lgkmcnt(4)\n"
    "v_mfma_f32_32x32x16_bf16 v[80:95], v[16:19], v[168:171], v[80:95]\n"
    "ds_read_b64_tr_b16 v[164:165], v230 offset:43008\n"
    "ds_read_b64_tr_b16 v[166:167], v230 offset:43520\n"
    "s_waitcnt lgkmcnt(4)\n"
    "v_mfma_f32_32x32x16_bf16 v[80:95], v[20:23], v[172:175], v[80:95]\n"
    "ds_read_b64_tr_b16 v[168:169], v230 offset:45056\n"
    "ds_read_b64_tr_b16 v[170:171], v230 offset:45568\n"
    "s_waitcnt lgkmcnt(4)\n"
    "v_mfma_f32_32x32x16_bf16 v[80:95], v[24:27], v[160:163], v[80:95]\n"
    "ds_read_b64_tr_b16 v[172:173], v230 offset:47104\n"
    "ds_read_b64_tr_b16 v[174:175], v230 offset:47616\n"
    "s_waitcnt lgkmcnt(4)\n"
    "v_mfma_f32_32x32x16_bf16 v[80:95], v[28:31], v[164:167], v[80:95]\n"
    "s_waitcnt vmcnt(2)\n"
    "s_barrier\n"
    "ds_read_b64_tr_b16 v[160:161], v230 offset:49152\n"
    "ds_read_b64_tr_b16 v[162:163], v230 offset:49664\n"
    "s_waitcnt lgkmcnt(4)\n"
    "v_mfma_f32_32x32x16_bf16 v[80:95], v[32:35], v[168:171], v[80:95]\n"
    "ds_read_b64_tr_b16 v[164:165], v230 offset:51200\n"
    "ds_read_b64_tr_b16 v[166:167], v230 offset:51712\n"
    "s_waitcnt lgkmcnt(4)\n"
    "v_mfma_f32_32x32x16_bf16 v[80:95], v[36:39], v[172:175], v[80:95]\n"
    "ds_read_b64_tr_b16 v[168:169], v230 offset:53248\n"
    "ds_read_b64_tr_b16 v[170:171], v230 offset:53760\n"
    "s_waitcnt lgkmcnt(4)\n"
    "v_mfma_f32_32x32x16_bf16 v[80:95], v[40:43], v[160:163], v[80:95]\n"
    "ds_read_b64_tr_b16 v[172:173], v230 offset:55296\n"
    "ds_read_b64_tr_b16 v[174:175], v230 offset:55808\n"
    "s_waitcnt lgkmcnt(4)\n"
    "v_mfma_f32_32x32x16_bf16 v[80:95], v[44:47], v[164:167], v[80:95]\n"
    "s_waitcnt lgkmcnt(2)\n"
    "v_mfma_f32_32x32x16_bf16 v[80:95], v[48:51], v[168:171], v[80:95]\n"
    "s_waitcnt lgkmcnt(0)\n"
    "v_mfma_f32_32x32x16_bf16 v[80:95], v[52:55], v[172:175], v[80:95]\n"
    "s_nop 11\n"
    "v_pk_add_f32 v[64:65], v[64:65], v[80:81]\n"
    "v_pk_add_f32 v[66:67], v[66:67], v[82:83] neg_lo:[0,1] neg_hi:[0,1]\n"
    "v_pk_add_f32 v[68:69], v[68:69], v[84:85]\n"
    "v_pk_add_f32 v[70:71], v[70:71], v[86:87] neg_lo:[0,1] neg_hi:[0,1]\n"
    "s_mov_b64 exec, s[44:45]\n"
    "v_mov_b32 v64, 0\n"
    "s_mov_b64 exec, -1\n"
    "v_cvt_pk_bf16_f32 v184, v64, v65\n"
    "v_cvt_pk_bf16_f32 v185, v66, v67\n"
    "v_cvt_pk_bf16_f32 v186, v68, v69\n"
    "v_cvt_pk_bf16_f32 v187, v70, v71\n"
    "buffer_store_dwordx4 v[184:187], v235, %[ws], %[qo] offen offset:0 sc1\n"
    "v_pk_add_f32 v[72:73], v[72:73], v[88:89]\n"
    "v_pk_add_f32 v[74:75], v[74:75], v[90:91] neg_lo:[0,1] neg_hi:[0,1]\n"
    "v_pk_add_f32 v[76:77], v[76:77], v[92:93]\n"
    "v_pk_add_f32 v[78:79], v[78:79], v[94:95] neg_lo:[0,1] neg_hi:[0,1]\n"
    "v_cvt_pk_bf16_f32 v188, v72, v73\n"
    "v_cvt_pk_bf16_f32 v189, v74, v75\n"
    "v_cvt_pk_bf16_f32 v190, v76, v77\n"
    "v_cvt_pk_bf16_f32 v191, v78, v79\n"
    "buffer_store_dwordx4 v[188:191], v235, %[ws], %[qo] offen offset:1024 sc1\n"
    "s_cmp_eq_u32 %[w], 3\n"
    "s_cbranch_scc0 LFa_%=\n"
    "v_and_b32 v176, 32, v224\n"
    "s_add_u32 s46, %[lds], 159744\n"
    "v_add_u32 v176, v176, v224\n"
    "v_lshl_add_u32 v176, v176, 2, s46\n"
    "ds_read2_b32 v[180:181], v176 offset1:32\n"
    "v_lshlrev_b32 v179, 10, v224\n"
    "v_lshlrev_b32 v176, 1, v224\n"
    "v_and_b32 v179, 4096, v179\n"
    "v_and_b32 v176, 0x70, v176\n"
    "v_and_b32 v236, 3, v224\n"
    "v_or_b32 v176, v176, v179\n"
    "v_lshl_or_b32 v176, v236, 2, v176\n"
    "s_waitcnt lgkmcnt(0)\n"
    "v_add_f32 v177, v180, v181\n"
    "v_cvt_pk_bf16_f32 v177, v177, 0\n"
    "s_cmp_eq_u32 %[jt], 6\n"
    "s_cbranch_scc1 LGa_%=\n"
    "buffer_store_short v177, v176, %[ws], %[so] offen\n"
    "s_branch LFa_%=\n"
    "LGa_%=:\n"
    "buffer_store_dword v177, v176, %[ws], %[so] offen\n"
    "LFa_%=:\n"
    "s_branch LE_%=\n"
    "LL_%=:\n"
    "s_nop 4\n"
    "s_add_u32 m0, s50, 0\n"
    "s_nop 0\n"
    "buffer_load_dwordx4 v227, %[d2], s40 offen lds\n"
    "v_xor_b32_dpp v28, v0, v231 quad_perm:[0,1,3,2] row_mask:0xf bank_mask:0xf\n"
    "v_xor_b32_dpp v29, v1, v231 quad_perm:[0,1,3,2] row_mask:0xf bank_mask:0xf\n"
    "v_xor_b32_dpp v30, v2, v231 quad_perm:[0,1,3,2] row_mask:0xf bank_mask:0xf\n"
    "v_xor_b32_dpp v31, v3, v231 quad_perm:[0,1,3,2] row_mask:0xf bank_mask:0xf\n"
    "v_xor_b32_dpp v32, v4, v231 quad_perm:[0,1,3,2] row_mask:0xf bank_mask:0xf\n"
    "v_xor_b32_dpp v33, v5, v231 quad_perm:[0,1,3,2] row_mask:0xf bank_mask:0xf\n"
    "s_add_u32 m0, s50, 1024\n"
    "s_nop 0\n"
    "buffer_load_dwordx4 v228, %[d2], s40 offen lds\n"
    "v_xor_b32_dpp v34, v6, v231 quad_perm:[0,1,3,2] row_mask:0xf bank_mask:0xf\n"
    "v_xor_b32_dpp v35, v7, v231 quad_perm:[0,1,3,2] row_mask:0xf bank_mask:0xf\n"
    "v_xor_b32_dpp v36, v8, v231 quad_perm:[0,1,3,2] row_mask:0xf bank_mask:0xf\n"
    "v_xor_b32_dpp v37, v9, v231 quad_perm:[0,1,3,2] row_mask:0xf bank_mask:0xf\n"
    "v_xor_b32_dpp v38, v10, v231 quad_perm:[0,1,3,2] row_mask:0xf bank_mask:0xf\n"
    "v_xor_b32_dpp v39, v11, v231 quad_perm:[0,1,3,2] row_mask:0xf bank_mask:0xf\n"
    "s_add_u32 m0, s50, 8192\n"
    "s_nop 0\n"
    "buffer_load_dwordx4 v227, %[d2], s41 offen lds\n"
    "v_xor_b32_dpp v40, v12, v231 quad_perm:[0,1,3,2] row_mask:0xf bank_mask:0xf\n"
    "v_xor_b32_dpp v41, v13, v231 quad_perm:[0,1,3,2] row_mask:0xf bank_mask:0xf\n"
    "v_xor_b32_dpp v42, v14, v231 quad_perm:[0,1,3,2] row_mask:0xf bank_mask:0xf\n"
    "v_xor_b32_dpp v43, v15, v231 quad_perm:[0,1,3,2] row_mask:0xf bank_mask:0xf\n"
    "v_xor_b32_dpp v44, v16, v231 quad_perm:[0,1,3,2] row_mask:0xf bank_mask:0xf\n"
    "v_xor_b32_dpp v45, v17, v231 quad_perm:[0,1,3,2] row_mask:0xf bank_mask:0xf\n"
    "s_add_u32 m0, s50, 9216\n"
    "s_nop 0\n"
    "buffer_load_dwordx4 v228, %[d2], s41 offen lds\n"
    "v_xor_b32_dpp v46, v18, v231 quad_perm:[0,1,3,2] row_mask:0xf bank_mask:0xf\n"
    "v_xor_b32_dpp v47, v19, v231 quad_perm:[0,1,3,2] row_mask:0xf bank_mask:0xf\n"
    "v_xor_b32_dpp v48, v20, v231 quad_perm:[0,1,3,2] row_mask:0xf bank_mask:0xf\n"
    "v_xor_b32_dpp v49, v21, v231 quad_perm:[0,1,3,2] row_mask:0xf bank_mask:0xf\n"
    "v_xor_b32_dpp v50, v22, v231 quad_perm:[0,1,3,2] row_mask:0xf bank_mask:0xf\n"
    "v_xor_b32_dpp v51, v23, v231 quad_perm:[0,1,3,2] row_mask:0xf bank_mask:0xf\n"
    "s_add_u32 m0, s50, 16384\n"
    "s_nop 0\n"
    "buffer_load_dwordx4 v227, %[d2], s52 offen lds\n"
    "v_xor_b32_dpp v52, v24, v231 quad_perm:[0,1,3,2] row_mask:0xf bank_mask:0xf\n"
    "v_xor_b32_dpp v53, v25, v231 quad_perm:[0,1,3,2] row_mask:0xf bank_mask:0xf\n"
    "v_xor_b32_dpp v54, v26, v231 quad_perm:[0,1,3,2] row_mask:0xf bank_mask:0xf\n"
    "v_xor_b32_dpp v55, v27, v231 quad_perm:[0,1,3,2] row_mask:0xf bank_mask:0xf\n"
    "v_bfe_u32 v176, v224, 2, 2\n"
    "v_lshrrev_b32 v177, 5, v224\n"
    "v_bfe_u32 v178, v224, 1, 1\n"
    "s_add_u32 m0, s50, 17408\n"
    "s_nop 0\n"
    "buffer_load_dwordx4 v228, %[d2], s52 offen lds\n"
    "v_lshl_add_u32 v176, v177, 3, v176\n"
    "v_lshrrev_b32 v177, 3, v224\n"
    "v_lshlrev_b32 v230, 7, v176\n"
    "v_and_b32 v176, 2, v177\n"
    "v_and_b32 v177, 1, v177\n"
    "v_add3_u32 v178, v178, v176, s57\n"
    "v_lshlrev_b32 v177, 2, v177\n"
    "s_add_u32 m0, s50, 24576\n"
    "s_nop 0\n"
    "buffer_load_dwordx4 v227, %[d2], s53 offen lds\n"
    "v_and_b32 v176, 1, v224\n"
    "v_xor_b32 v178, v178, v177\n"
    "v_lshl_add_u32 v230, v176, 3, v230\n"
    "s_add_u32 s46, %[lds], 57344\n"
    "v_lshl_add_u32 v230, v178, 4, v230\n"
    "v_add_u32 v230, s46, v230\n"
    "v_and_b32 v176, 15, v224\n"
    "s_add_u32 m0, s50, 25600\n"
    "s_nop 0\n"
    "buffer_load_dwordx4 v228, %[d2], s53 offen lds\n"
    "v_bfe_u32 v177, v224, 4, 1\n"
    "v_lshrrev_b32 v178, 5, v224\n"
    "v_lshlrev_b32 v234, 6, v176\n"
    "v_lshl_add_u32 v234, v177, 5, v234\n"
    "v_lshl_add_u32 v234, v178, 4, v234\n"
    "s_cmp_eq_u32 %[j0], 0\n"
    "s_cselect_b32 s42, 0xfffefffe, -1\n"
    "s_cselect_b32 s44, -1, 0\n"
    "s_add_u32 m0, s50, 32768\n"
    "s_nop 0\n"
    "buffer_load_dwordx4 v227, %[d2], s54 offen lds\n"
    "s_mov_b32 s43, s42\n"
    "s_mov_b32 s45, 0\n"
    "s_mov_b32 s48, 0xcccccccc\n"
    "s_mov_b32 s49, 0xcccccccc\n"
    "s_add_u32 m0, s50, 33792\n"
    "s_nop 0\n"
    "buffer_load_dwordx4 v228, %[d2], s54 offen lds\n"
    "s_add_u32 m0, s50, 40960\n"
    "s_nop 0\n"
    "buffer_load_dwordx4 v227, %[d2], s55 offen lds\n"
    "s_add_u32 m0, s50, 41984\n"
    "s_nop 0\n"
    "buffer_load_dwordx4 v228, %[d2], s55 offen lds\n"
    "s_add_u32 m0, s50, 49152\n"
    "s_nop 0\n"
    "buffer_load_dwordx4 v227, %[d2], s59 offen lds\n"
    "s_add_u32 m0, s50, 50176\n"
    "s_nop 0\n"
    "buffer_load_dwordx4 v228, %[d2], s59 offen lds\n"
    "v_and_b32 v176, 7, v224\n"
    "v_lshrrev_b32 v177, 3, v224\n"
    "s_add_u32 s46, %[lds], 57344\n"
    "v_lshlrev_b32 v227, 4, v176\n"
    "v_mul_u32_u24 v176, 450, v176\n"
    "v_lshl_add_u32 v227, v177, 13, v227\n"
    "v_lshl_add_u32 v177, %[w], 3, v177\n"
    "v_add_u32 v176, s46, v176\n"
    "v_cmp_eq_u32 vcc, 0, v177\n"
    "v_lshl_add_u32 v230, v177, 1, v176\n"
    "v_add_u32 v176, 448, v230\n"
    "v_cndmask_b32 v229, v230, v176, vcc\n"
    "s_mov_b32 s46, %[po]\n"
    "s_waitcnt vmcnt(28)\n"
    "s_barrier\n"
    "ds_read_u16 v208, v229 offset:0\n"
    "ds_read_u16 v209, v229 offset:16384\n"
    "ds_read_u16 v210, v229 offset:4096\n"
    "ds_read_u16 v211, v229 offset:20480\n"
    "ds_read_u16 v212, v229 offset:8192\n"
    "ds_read_u16 v213, v229 offset:24576\n"
    "ds_read_u16 v214, v229 offset:12288\n"
    "ds_read_u16 v215, v229 offset:28672\n"
    "s_waitcnt lgkmcnt(6)\n"
    "ds_read_u16 v216, v230 offset:64\n"
    "ds_read_u16 v217, v230 offset:16448\n"
    "ds_read_u16 v218, v230 offset:4160\n"
    "ds_read_u16 v219, v230 offset:20544\n"
    "ds_read_u16 v220, v230 offset:8256\n"
    "ds_read_u16 v221, v230 offset:24640\n"
    "ds_read_u16 v222, v230 offset:12352\n"
    "ds_read_u16 v223, v230 offset:28736\n"
    "s_waitcnt lgkmcnt(8)\n"
    "v_lshl_or_b32 v128, v209, 16, v208\n"
    "v_lshl_or_b32 v129, v211, 16, v210\n"
    "v_lshl_or_b32 v130, v213, 16, v212\n"
    "v_lshl_or_b32 v131, v215, 16, v214\n"
    "buffer_store_dwordx4 v[128:131], v227, %[ws], s46 offen sc1\n"
    "s_add_u32 s46, s46, 262144\n"
    "s_waitcnt lgkmcnt(6)\n"
    "ds_read_u16 v208, v230 offset:128\n"
    "ds_read_u16 v209, v230 offset:16512\n"
    "ds_read_u16 v210, v230 offset:4224\n"
    "ds_read_u16 v211, v230 offset:20608\n"
    "ds_read_u16 v212, v230 offset:8320\n"
    "ds_read_u16 v213, v230 offset:24704\n"
    "ds_read_u16 v214, v230 offset:12416\n"
    "ds_read_u16 v215, v230 offset:28800\n"
    "s_waitcnt lgkmcnt(8)\n"
    "v_lshl_or_b32 v132, v217, 16, v216\n"
    "v_lshl_or_b32 v133, v219, 16, v218\n"
    "v_lshl_or_b32 v134, v221, 16, v220\n"
    "v_lshl_or_b32 v135, v223, 16, v222\n"
    "buffer_store_dwordx4 v[132:135], v227, %[ws], s46 offen sc1\n"
    "s_add_u32 s46, s46, 262144\n"
    "s_waitcnt lgkmcnt(6)\n"
    "ds_read_u16 v216, v230 offset:192\n"
    "ds_read_u16 v217, v230 offset:16576\n"
    "ds_read_u16 v218, v230 offset:4288\n"
    "ds_read_u16 v219, v230 offset:20672\n"
    "ds_read_u16 v220, v230 offset:8384\n"
    "ds_read_u16 v221, v230 offset:24768\n"
    "ds_read_u16 v222, v230 offset:12480\n"
    "ds_read_u16 v223, v230 offset:28864\n"
    "s_waitcnt lgkmcnt(8)\n"
    "v_lshl_or_b32 v136, v209, 16, v208\n"
    "v_lshl_or_b32 v137, v211, 16, v210\n"
    "v_lshl_or_b32 v138, v213, 16, v212\n"
    "v_lshl_or_b32 v139, v215, 16, v214\n"
    "buffer_store_dwordx4 v[136:139], v227, %[ws], s46 offen sc1\n"
    "s_add_u32 s46, s46, 262144\n"
    "s_waitcnt lgkmcnt(6)\n"
    "ds_read_u16 v208, v230 offset:256\n"
    "ds_read_u16 v209, v230 offset:16640\n"
    "ds_read_u16 v210, v230 offset:4352\n"
    "ds_read_u16 v211, v230 offset:20736\n"
    "ds_read_u16 v212, v230 offset:8448\n"
    "ds_read_u16 v213, v230 offset:24832\n"
    "ds_read_u16 v214, v230 offset:12544\n"
    "ds_read_u16 v215, v230 offset:28928\n"
    "s_waitcnt lgkmcnt(8)\n"
    "v_lshl_or_b32 v140, v217, 16, v216\n"
    "v_lshl_or_b32 v141, v219, 16, v218\n"
    "v_lshl_or_b32 v142, v221, 16, v220\n"
    "v_lshl_or_b32 v143, v223, 16, v222\n"
    "buffer_store_dwordx4 v[140:143], v227, %[ws], s46 offen sc1\n"
    "s_add_u32 s46, s46, 262144\n"
    "s_waitcnt lgkmcnt(6)\n"
    "ds_read_u16 v216, v230 offset:320\n"
    "ds_read_u16 v217, v230 offset:16704\n"
    "ds_read_u16 v218, v230 offset:4416\n"
    "ds_read_u16 v219, v230 offset:20800\n"
    "ds_read_u16 v220, v230 offset:8512\n"
    "ds_read_u16 v221, v230 offset:24896\n"
    "ds_read_u16 v222, v230 offset:12608\n"
    "ds_read_u16 v223, v230 offset:28992\n"
    "s_waitcnt lgkmcnt(8)\n"
    "v_lshl_or_b32 v144, v209, 16, v208\n"
    "v_lshl_or_b32 v145, v211, 16, v210\n"
    "v_lshl_or_b32 v146, v213, 16, v212\n"
    "v_lshl_or_b32 v147, v215, 16, v214\n"
    "buffer_store_dwordx4 v[144:147], v227, %[ws], s46 offen sc1\n"
    "s_add_u32 s46, s46, 262144\n"
    "s_waitcnt lgkmcnt(6)\n"
    "ds_read_u16 v208, v230 offset:384\n"
    "ds_read_u16 v209, v230 offset:16768\n"
    "ds_read_u16 v210, v230 offset:4480\n"
    "ds_read_u16 v211, v230 offset:20864\n"
    "ds_read_u16 v212, v230 offset:8576\n"
    "ds_read_u16 v213, v230 offset:24960\n"
    "ds_read_u16 v214, v230 offset:12672\n"
    "ds_read_u16 v215, v230 offset:29056\n"
    "s_waitcnt lgkmcnt(8)\n"
    "v_lshl_or_b32 v148, v217, 16, v216\n"
    "v_lshl_or_b32 v149, v219, 16, v218\n"
    "v_lshl_or_b32 v150, v221, 16, v220\n"
    "v_lshl_or_b32 v151, v223, 16, v222\n"
    "buffer_store_dwordx4 v[148:151], v227, %[ws], s46 offen sc1\n"
    "s_add_u32 s46, s46, 262144\n"
    "s_waitcnt lgkmcnt(0)\n"
    "v_lshl_or_b32 v152, v209, 16, v208\n"
    "v_lshl_or_b32 v153, v211, 16, v210\n"
    "v_lshl_or_b32 v154, v213, 16, v212\n"
    "v_lshl_or_b32 v155, v215, 16, v214\n"
    "buffer_store_dwordx4 v[152:155], v227, %[ws], s46 offen sc1\n"
    "s_waitcnt vmcnt(21)\n"
    "s_barrier\n"
    "ds_read_b128 v[160:163], v240 offset:0\n"
    "ds_read_b128 v[164:167], v241 offset:0\n"
    "ds_read_b128 v[168:171], v242 offset:0\n"
    "s_waitcnt lgkmcnt(2)\n"
    "v_mfma_f32_32x32x16_bf16 v[112:127], v[160:163], v[0:3], 0\n"
    "ds_read_b128 v[172:175], v243 offset:0\n"
    "s_waitcnt lgkmcnt(2)\n"
    "v_mfma_f32_32x32x16_bf16 v[112:127], v[164:167], v[4:7], v[112:127]\n"
    "ds_read_b128 v[160:163], v240 offset:8192\n"
    "s_waitcnt lgkmcnt(2)\n"
    "v_mfma_f32_32x32x16_bf16 v[112:127], v[168:171], v[8:11], v[112:127]\n"
    "ds_read_b128 v[164:167], v241 offset:8192\n"
    "s_waitcnt lgkmcnt(2)\n"
    "v_mfma_f32_32x32x16_bf16 v[112:127], v[172:175], v[12:15], v[112:127]\n"
    "ds_read_b128 v[168:171], v242 offset:8192\n"
    "s_waitcnt lgkmcnt(2)\n"
    "v_mfma_f32_32x32x16_bf16 v[112:127], v[160:163], v[16:19], v[112:127]\n"
    "ds_read_b128 v[172:175], v243 offset:8192\n"
    "s_waitcnt lgkmcnt(2)\n"
    "v_mfma_f32_32x32x16_bf16 v[112:127], v[164:167], v[20:23], v[112:127]\n"
    "ds_read_b128 v[160:163], v240 offset:16384\n"
    "s_waitcnt lgkmcnt(2)\n"
    "v_mfma_f32_32x32x16_bf16 v[112:127], v[168:171], v[24:27], v[112:127]\n"
    "ds_read_b128 v[164:167], v241 offset:16384\n"
    "s_waitcnt lgkmcnt(2)\n"
    "v_mfma_f32_32x32x16_bf16 v[112:127], v[172:175], v[28:31], v[112:127]\n"
    "ds_read_b128 v[168:171], v242 offset:16384\n"
    "s_waitcnt lgkmcnt(2)\n"
    "v_mfma_f32_32x32x16_bf16 v[112:127], v[160:163], v[32:35], v[112:127]\n"
    "ds_read_b128 v[172:175], v243 offset:16384\n"
    "s_waitcnt lgkmcnt(2)\n"
    "v_mfma_f32_32x32x16_bf16 v[112:127], v[164:167], v[36:39], v[112:127]\n"
    "ds_read_b128 v[160:163], v240 offset:24576\n"
    "s_waitcnt lgkmcnt(2)\n"
    "v_mfma_f32_32x32x16_bf16 v[112:127], v[168:171], v[40:43], v[112:127]\n"
    "ds_read_b128 v[164:167], v241 offset:24576\n"
    "s_waitcnt lgkmcnt(2)\n"
    "v_mfma_f32_32x32x16_bf16 v[112:127], v[172:175], v[44:47], v[112:127]\n"
    "ds_read_b128 v[168:171], v242 offset:24576\n"
    "s_waitcnt lgkmcnt(2)\n"
    "v_mfma_f32_32x32x16_bf16 v[112:127], v[160:163], v[48:51], v[112:127]\n"
    "ds_read_b128 v[172:175], v243 offset:24576\n"
    "s_waitcnt lgkmcnt(2)\n"
    "v_mfma_f32_32x32x16_bf16 v[112:127], v[164:167], v[52:55], v[112:127]\n"
    "ds_read_b128 v[160:163], v240 offset:32768\n"
    "s_waitcnt lgkmcnt(2)\n"
    "v_mfma_f32_32x32x16_bf16 v[96:111], v[168:171], v[0:3], 0\n"
    "ds_read_b128 v[164:167], v241 offset:32768\n"
    "s_waitcnt lgkmcnt(2)\n"
    "v_mfma_f32_32x32x16_bf16 v[96:111], v[172:175], v[4:7], v[96:111]\n"
    "ds_read_b128 v[168:171], v242 offset:32768\n"
    "s_waitcnt lgkmcnt(2)\n"
    "v_mfma_f32_32x32x16_bf16 v[96:111], v[160:163], v[8:11], v[96:111]\n"
    "ds_read_b128 v[172:175], v243 offset:32768\n"
    "s_waitcnt lgkmcnt(2)\n"
    "v_mfma_f32_32x32x16_bf16 v[96:111], v[164:167], v[12:15], v[96:111]\n"
    "ds_read_b128 v[160:163], v240 offset:40960\n"
    "s_waitcnt lgkmcnt(2)\n"
    "v_mfma_f32_32x32x16_bf16 v[96:111], v[168:171], v[16:19], v[96:111]\n"
    "ds_read_b128 v[164:167], v241 offset:40960\n"
    "s_waitcnt lgkmcnt(2)\n"
    "v_mfma_f32_32x32x16_bf16 v[96:111], v[172:175], v[20:23], v[96:111]\n"
    "ds_read_b128 v[168:171], v242 offset:40960\n"
    "s_waitcnt lgkmcnt(2)\n"
    "v_mfma_f32_32x32x16_bf16 v[96:111], v[160:163], v[24:27], v[96:111]\n"
    "ds_read_b128 v[172:175], v243 offset:40960\n"
    "s_waitcnt lgkmcnt(2)\n"
    "v_mfma_f32_32x32x16_bf16 v[96:111], v[164:167], v[28:31], v[96:111]\n"
    "ds_read_b128 v[160:163], v240 offset:49152\n"
    "s_waitcnt lgkmcnt(2)\n"
    "v_mfma_f32_32x32x16_bf16 v[96:111], v[168:171], v[32:35], v[96:111]\n"
    "ds_read_b128 v[164:167], v241 offset:49152\n"
    "s_waitcnt lgkmcnt(2)\n"
    "v_mfma_f32_32x32x16_bf16 v[96:111], v[172:175], v[36:39], v[96:111]\n"
    "ds_read_b128 v[168:171], v242 offset:49152\n"
    "s_waitcnt lgkmcnt(2)\n"
    "v_mfma_f32_32x32x16_bf16 v[96:111], v[160:163], v[40:43], v[96:111]\n"
    "ds_read_b128 v[172:175], v243 offset:49152\n"
    "s_waitcnt lgkmcnt(2)\n"
    "v_mfma_f32_32x32x16_bf16 v[96:111], v[164:167], v[44:47], v[96:111]\n"
    "s_waitcnt lgkmcnt(1)\n"
    "v_mfma_f32_32x32x16_bf16 v[96:111], v[168:171], v[48:51], v[96:111]\n"
    "s_waitcnt lgkmcnt(0)\n"
    "v_mfma_f32_32x32x16_bf16 v[96:111], v[172:175], v[52:55], v[96:111]\n"
    "s_nop 11\n"
    "v_fmac_f32 v112, v232, v96\n"
    "v_fmac_f32 v113, v232, v97\n"
    "v_fmac_f32 v114, v232, v98\n"
    "v_fmac_f32 v115, v232, v99\n"
    "v_fmac_f32 v116, v232, v100\n"
    "v_fmac_f32 v117, v232, v101\n"
    "v_fmac_f32 v118, v232, v102\n"
    "v_fmac_f32 v119, v232, v103\n"
    "v_fmac_f32 v120, v232, v104\n"
    "v_fmac_f32 v121, v232, v105\n"
    "v_fmac_f32 v122, v232, v106\n"
    "v_fmac_f32 v123, v232, v107\n"
    "v_fmac_f32 v124, v232, v108\n"
    "v_fmac_f32 v125, v232, v109\n"
    "v_fmac_f32 v126, v232, v110\n"
    "v_fmac_f32 v127, v232, v111\n"
    "v_cvt_pk_bf16_f32 v184, v112, v113\n"
    "v_cvt_pk_bf16_f32 v185, v114, v115\n"
    "v_cvt_pk_bf16_f32 v186, v116, v117\n"
    "v_cvt_pk_bf16_f32 v187, v118, v119\n"
    "v_cvt_pk_bf16_f32 v188, v120, v121\n"
    "v_cvt_pk_bf16_f32 v189, v122, v123\n"
    "v_cvt_pk_bf16_f32 v190, v124, v125\n"
    "v_cvt_pk_bf16_f32 v191, v126, v127\n"
    "v_permlane16_swap_b32 v112, v120\n"
    "v_permlane16_swap_b32 v113, v121\n"
    "v_permlane16_swap_b32 v114, v122\n"
    "v_permlane16_swap_b32 v115, v123\n"
    "v_permlane16_swap_b32 v116, v124\n"
    "v_permlane16_swap_b32 v117, v125\n"
    "v_permlane16_swap_b32 v118, v126\n"
    "v_permlane16_swap_b32 v119, v127\n"
    "v_permlane32_swap_b32 v184, v188\n"
    "v_permlane32_swap_b32 v185, v189\n"
    "v_permlane32_swap_b32 v186, v190\n"
    "v_permlane32_swap_b32 v187, v191\n"
    "v_mul_f32 v192, v120, v120\n"
    "v_mul_f32 v193, v121, v121\n"
    "v_mul_f32 v194, v122, v122\n"
    "v_mul_f32 v195, v123, v123\n"
    "v_permlane16_swap_b32 v184, v188\n"
    "v_permlane16_swap_b32 v185, v189\n"
    "v_permlane16_swap_b32 v186, v190\n"
    "v_permlane16_swap_b32 v187, v191\n"
    "v_mul_f32 v196, v124, v124\n"
    "v_mul_f32 v197, v125, v125\n"
    "v_mul_f32 v198, v126, v126\n"
    "v_mul_f32 v199, v127, v127\n"
    "buffer_store_dwordx4 v[184:187], v234, %[ws], %[xo] offen offset:0 sc1\n"
    "buffer_store_dwordx4 v[188:191], v234, %[ws], %[xo] offen offset:1024 sc1\n"
    "s_mov_b64 exec, s[42:43]\n"
    "v_fmac_f32 v192, v112, v112\n"
    "v_fmac_f32 v193, v113, v113\n"
    "v_fmac_f32 v194, v114, v114\n"
    "v_fmac_f32 v195, v115, v115\n"
    "s_mov_b64 exec, -1\n"
    "s_mov_b64 exec, s[42:43]\n"
    "v_fmac_f32 v196, v116, v116\n"
    "v_fmac_f32 v197, v117, v117\n"
    "v_fmac_f32 v198, v118, v118\n"
    "v_fmac_f32 v199, v119, v119\n"
    "s_mov_b64 exec, -1\n"
    "v_lshrrev_b32 v176, 1, v224\n"
    "v_lshl_add_u32 v176, v176, 2, s58\n"
    "s_nop 1\n"
    "v_add_f32_dpp v112, v192, v192 row_ror:8 row_mask:0xf bank_mask:0x3\n"
    "v_add_f32_dpp v113, v193, v193 row_ror:8 row_mask:0xf bank_mask:0x3\n"
    "v_add_f32_dpp v114, v194, v194 row_ror:8 row_mask:0xf bank_mask:0x3\n"
    "v_add_f32_dpp v115, v195, v195 row_ror:8 row_mask:0xf bank_mask:0x3\n"
    "v_add_f32_dpp v112, v196, v196 row_ror:8 row_mask:0xf bank_mask:0xc\n"
    "v_add_f32_dpp v113, v197, v197 row_ror:8 row_mask:0xf bank_mask:0xc\n"
    "v_add_f32_dpp v114, v198, v198 row_ror:8 row_mask:0xf bank_mask:0xc\n"
    "v_add_f32_dpp v115, v199, v199 row_ror:8 row_mask:0xf bank_mask:0xc\n"
    "v_add_f32_dpp v192, v112, v112 row_half_mirror row_mask:0xf bank_mask:0x5\n"
    "v_add_f32_dpp v193, v113, v113 row_half_mirror row_mask:0xf bank_mask:0x5\n"
    "s_nop 0\n"
    "v_add_f32_dpp v192, v114, v114 row_half_mirror row_mask:0xf bank_mask:0xa\n"
    "v_add_f32_dpp v193, v115, v115 row_half_mirror row_mask:0xf bank_mask:0xa\n"
    "v_cndmask_b32_e64 v112, v192, v193, s[48:49]\n"
    "v_cndmask_b32_e64 v113, v193, v192, s[48:49]\n"
    "s_nop 1\n"
    "v_add_f32_dpp v112, v113, v112 quad_perm:[2,3,0,1] row_mask:0xf bank_mask:0xf\n"
    "s_nop 1\n"
    "v_add_f32_dpp v112, v112, v112 quad_perm:[1,0,3,2] row_mask:0xf bank_mask:0xf\n"
    "ds_write_b32 v176, v112\n"
    "s_waitcnt vmcnt(9) lgkmcnt(0)\n"
    "s_barrier\n"
    "s_cmp_eq_u32 %[w], 3\n"
    "s_cbranch_scc0 LFb_%=\n"
    "v_and_b32 v176, 32, v224\n"
    "s_add_u32 s46, %[lds], 159744\n"
    "v_add_u32 v176, v176, v224\n"
    "v_lshl_add_u32 v176, v176, 2, s46\n"
    "ds_read2_b32 v[180:181], v176 offset1:32\n"
    "v_lshlrev_b32 v179, 10, v224\n"
    "v_lshlrev_b32 v176, 1, v224\n"
    "v_and_b32 v179, 4096, v179\n"
    "v_and_b32 v176, 0x70, v176\n"
    "v_and_b32 v236, 3, v224\n"
    "v_or_b32 v176, v176, v179\n"
    "v_lshl_or_b32 v176, v236, 2, v176\n"
    "s_waitcnt lgkmcnt(0)\n"
    "v_add_f32 v177, v180, v181\n"
    "v_cvt_pk_bf16_f32 v177, v177, 0\n"
    "s_cmp_eq_u32 %[jt], 6\n"
    "s_cbranch_scc1 LGb_%=\n"
    "buffer_store_short v177, v176, %[ws], %[so] offen\n"
    "s_branch LFb_%=\n"
    "LGb_%=:\n"
    "buffer_store_dword v177, v176, %[ws], %[so] offen\n"
    "LFb_%=:\n"
    "s_cmp_eq_u32 %[tt], 28\n"
    "s_cbranch_scc0 LE_%=\n"
    "ds_read_b128 v[160:163], v244 offset:0\n"
    "ds_read_b128 v[164:167], v245 offset:0\n"
    "ds_read_b128 v[168:171], v246 offset:0\n"
    "s_waitcnt lgkmcnt(2)\n"
    "v_mfma_f32_32x32x16_bf16 v[128:143], v[160:163], v[0:3], 0\n"
    "ds_read_b128 v[172:175], v247 offset:0\n"
    "s_waitcnt lgkmcnt(2)\n"
    "v_mfma_f32_32x32x16_bf16 v[128:143], v[164:167], v[4:7], v[128:143]\n"
    "ds_read_b128 v[160:163], v244 offset:8192\n"
    "s_waitcnt lgkmcnt(2)\n"
    "v_mfma_f32_32x32x16_bf16 v[128:143], v[168:171], v[8:11], v[128:143]\n"
    "ds_read_b128 v[164:167], v245 offset:8192\n"
    "s_waitcnt lgkmcnt(2)\n"
    "v_mfma_f32_32x32x16_bf16 v[128:143], v[172:175], v[12:15], v[128:143]\n"
    "ds_read_b128 v[168:171], v246 offset:8192\n"
    "s_waitcnt lgkmcnt(2)\n"
    "v_mfma_f32_32x32x16_bf16 v[128:143], v[160:163], v[16:19], v[128:143]\n"
    "ds_read_b128 v[172:175], v247 offset:8192\n"
    "s_waitcnt lgkmcnt(2)\n"
    "v_mfma_f32_32x32x16_bf16 v[128:143], v[164:167], v[20:23], v[128:143]\n"
    "ds_read_b128 v[160:163], v244 offset:16384\n"
    "s_waitcnt lgkmcnt(2)\n"
    "v_mfma_f32_32x32x16_bf16 v[128:143], v[168:171], v[24:27], v[128:143]\n"
    "ds_read_b128 v[164:167], v245 offset:16384\n"
    "s_waitcnt lgkmcnt(2)\n"
    "v_mfma_f32_32x32x16_bf16 v[128:143], v[172:175], v[28:31], v[128:143]\n"
    "ds_read_b128 v[168:171], v246 offset:16384\n"
    "s_waitcnt lgkmcnt(2)\n"
    "v_mfma_f32_32x32x16_bf16 v[128:143], v[160:163], v[32:35], v[128:143]\n"
    "ds_read_b128 v[172:175], v247 offset:16384\n"
    "s_waitcnt lgkmcnt(2)\n"
    "v_mfma_f32_32x32x16_bf16 v[128:143], v[164:167], v[36:39], v[128:143]\n"
    "ds_read_b128 v[160:163], v244 offset:24576\n"
    "s_waitcnt lgkmcnt(2)\n"
    "v_mfma_f32_32x32x16_bf16 v[128:143], v[168:171], v[40:43], v[128:143]\n"
    "ds_read_b128 v[164:167], v245 offset:24576\n"
    "s_waitcnt lgkmcnt(2)\n"
    "v_mfma_f32_32x32x16_bf16 v[128:143], v[172:175], v[44:47], v[128:143]\n"
    "ds_read_b128 v[168:171], v246 offset:24576\n"
    "s_waitcnt lgkmcnt(2)\n"
    "v_mfma_f32_32x32x16_bf16 v[128:143], v[160:163], v[48:51], v[128:143]\n"
    "ds_read_b128 v[172:175], v247 offset:24576\n"
    "s_waitcnt lgkmcnt(2)\n"
    "v_mfma_f32_32x32x16_bf16 v[128:143], v[164:167], v[52:55], v[128:143]\n"
    "ds_read_b128 v[160:163], v244 offset:32768\n"
    "s_waitcnt lgkmcnt(2)\n"
    "v_mfma_f32_32x32x16_bf16 v[144:159], v[168:171], v[0:3], 0\n"
    "ds_read_b128 v[164:167], v245 offset:32768\n"
    "s_waitcnt lgkmcnt(2)\n"
    "v_mfma_f32_32x32x16_bf16 v[144:159], v[172:175], v[4:7], v[144:159]\n"
    "ds_read_b128 v[168:171], v246 offset:32768\n"
    "s_waitcnt lgkmcnt(2)\n"
    "v_mfma_f32_32x32x16_bf16 v[144:159], v[160:163], v[8:11], v[144:159]\n"
    "ds_read_b128 v[172:175], v247 offset:32768\n"
    "s_waitcnt lgkmcnt(2)\n"
    "v_mfma_f32_32x32x16_bf16 v[144:159], v[164:167], v[12:15], v[144:159]\n"
    "ds_read_b128 v[160:163], v244 offset:40960\n"
    "s_waitcnt lgkmcnt(2)\n"
    "v_mfma_f32_32x32x16_bf16 v[144:159], v[168:171], v[16:19], v[144:159]\n"
    "ds_read_b128 v[164:167], v245 offset:40960\n"
    "s_waitcnt lgkmcnt(2)\n"
    "v_mfma_f32_32x32x16_bf16 v[144:159], v[172:175], v[20:23], v[144:159]\n"
    "ds_read_b128 v[168:171], v246 offset:40960\n"
    "s_waitcnt lgkmcnt(2)\n"
    "v_mfma_f32_32x32x16_bf16 v[144:159], v[160:163], v[24:27], v[144:159]\n"
    "ds_read_b128 v[172:175], v247 offset:40960\n"
    "s_waitcnt lgkmcnt(2)\n"
    "v_mfma_f32_32x32x16_bf16 v[144:159], v[164:167], v[28:31], v[144:159]\n"
    "ds_read_b128 v[160:163], v244 offset:49152\n"
    "s_waitcnt lgkmcnt(2)\n"
    "v_mfma_f32_32x32x16_bf16 v[144:159], v[168:171], v[32:35], v[144:159]\n"
    "ds_read_b128 v[164:167], v245 offset:49152\n"
    "s_waitcnt lgkmcnt(2)\n"
    "v_mfma_f32_32x32x16_bf16 v[144:159], v[172:175], v[36:39], v[144:159]\n"
    "ds_read_b128 v[168:171], v246 offset:49152\n"
    "s_waitcnt lgkmcnt(2)\n"
    "v_mfma_f32_32x32x16_bf16 v[144:159], v[160:163], v[40:43], v[144:159]\n"
    "ds_read_b128 v[172:175], v247 offset:49152\n"
    "s_waitcnt lgkmcnt(2)\n"
    "v_mfma_f32_32x32x16_bf16 v[144:159], v[164:167], v[44:47], v[144:159]\n"
    "s_waitcnt lgkmcnt(1)\n"
    "v_mfma_f32_32x32x16_bf16 v[144:159], v[168:171], v[48:51], v[144:159]\n"
    "s_waitcnt lgkmcnt(0)\n"
    "v_mfma_f32_32x32x16_bf16 v[144:159], v[172:175], v[52:55], v[144:159]\n"
    "s_nop 11\n"
    "v_fmac_f32 v128, v232, v144\n"
    "v_lshlrev_b32 v176, 2, v224\n"
    "s_cmp_gt_u32 %[w], 1\n"
    "s_cbranch_scc1 LE_%=\n"
    "s_mov_b32 exec_hi, 0\n"
    "buffer_store_dword v128, v176, %[ws], %[qo] offen\n"
    "s_mov_b32 exec_hi, -1\n"
    "LE_%=:\n"
    "s_waitcnt vmcnt(0)\n"
        :: [ws] "s"(ws), [dx] "s"(dx), [d2] "s"(d2), [d1] "s"(d1), [wi] "s"((size_t)p.wi), [sx] "s"(tt * 896 + (32 * (w >> 1) + 8 * (w & 1)) * 32 * 896),
           [s2] "s"(16 * w * rs), [rs] "s"(rs), [s1] "s"(s1), [lds] "s"(lds), [j0] "s"(32 * jb), [w] "s"(w), [tt] "s"(tt), [jt] "s"(jt),
           [xo] "s"(OFF_XT + (2 * tt + rb) * TILE_B + 32 * jb * 64), [po] "s"(po), [qo] "s"(qo),
           [so] "s"(OFF_SSQ + (jt >> 1) * 8192 + (jt & 1) * 2 + 8 * tt * 16) : G_CLOB);
}

__device__ __forceinline__ void wt_load(const Params& p, int lb, int hj) {
    const int w = __builtin_amdgcn_readfirstlane(threadIdx.x >> 6), v = (threadIdx.x & 63) * 16;
    const int so = (16 * ((hj >> 1) & 3) + 8 * (hj & 1) + 64 * (4 * (hj >> 3) + w)) * 450;
    const u32x4 dr = mkdesc(p.wr, S_ * 450), di = mkdesc(p.wi, S_ * 450);
#pragma unroll
    for (int i = 0; i < 4; ++i) {
        dma16(dr, v, so + 1024 * i, lb + w * 4096 + 1024 * i);
        dma16(di, v, so + 1024 * i, lb + 16384 + w * 4096 + 1024 * i);
    }
}
__device__ __forceinline__ void wt_chunk(rsrc_t ws, LAS unsigned char* img, int hj, int c) {
    const int l8 = threadIdx.x & 7, cc = c == 0 ? 224 : c;
    LAS u16* q = (LAS u16*)(img + l8 * 450 + 2 * cc);
    u32x4 o;
#pragma unroll
    for (int r = 0; r < 4; ++r) o[r] = (unsigned)q[r * 2048] | ((unsigned)q[(4 + r) * 2048] << 16);
    bst128<16>(o, ws, OFF_WT + c * 8192 + (((hj >> 1) & 3) * 2 + (hj >> 5)) * 1024 + (16 * ((hj >> 3) & 3) + 8 * (hj & 1) + l8) * 16);
}

__global__ void __launch_bounds__(256, 1) k_first(Params p) {
    __shared__ __attribute__((aligned(16))) unsigned char smem[F_LDS];
    LAS unsigned char* lds = (LAS unsigned char*)smem;
    const rsrc_t ws = mkrsrc(p.ws, WS_BYTES);
    const int b = blockIdx.x;
    if (b < 224) first_main(p, (int)(size_t)lds, b);
    else {
        const int tid = threadIdx.x, a = b - 224;
        const int hx = 60 + ((a & 7) >> 1), cx = 28 * (2 * (a >> 3) + (a & 1));
        wt_load(p, (int)(size_t)lds, a);
        wt_load(p, (int)(size_t)lds + 32768, hx);
        WAIT_VM(8);
        __builtin_amdgcn_s_barrier();
        asm volatile("" ::: "memory");
#pragma unroll
        for (int i = 0; i < 7; ++i) wt_chunk(ws, lds, a, (tid >> 3) + 32 * i);
        asm volatile("" ::: "memory");
        WAIT_VM(7);
        __builtin_amdgcn_s_barrier();
        asm volatile("" ::: "memory");
        if (tid < 224) wt_chunk(ws, lds + 32768, hx, cx + (tid >> 3));
        const unsigned ct = code_touch(a);
        bst128<16>(u32x4{0u, 0u, 0u, 0u}, ws, OFF_FLAG + a * 4096 + tid * 16);
        if (tid >= 240) bst128<16>(u32x4{0u, 0u, 0u, 0u}, ws, OFF_SSQ + 32768 + 3 * 8192 + a * 256 + (tid - 240) * 16);
        if (a < 2 && tid < (a == 0 ? 224 : 56)) {
            const u32x4 t = *(const GAS u32x4*)((const GAS u16*)(a == 0 ? p.b1 : p.b2) + tid * 8);
            const float sc = a == 0 ? GELU_C : 1.0f;
            const int o = (a == 0 ? OFF_B1C : OFF_B2F) + tid * 32;
            bst128<16>(u32x4{asu(bflo(t.x) * sc), asu(bfhi(t.x) * sc), asu(bflo(t.y) * sc), asu(bfhi(t.y) * sc)}, ws, o);
            bst128<16>(u32x4{asu(bflo(t.z) * sc), asu(bfhi(t.z) * sc), asu(bflo(t.w) * sc), asu(bfhi(t.w) * sc)}, ws, o + 16);
        }
        asm volatile("" :: "v"(ct));
    }
}

constexpr int G_LDS = 154112;
template <int LAYER>
__device__ __forceinline__ void mlp_body(const Params& p, const int lds) {
    const int w = __builtin_amdgcn_readfirstlane(threadIdx.x >> 6);
    const int b = blockIdx.x, r = b >> 3, side = b & 1, j7 = r % 7, it = 7 * side + j7, pn = 2 * (b & 6) + r / 7;
    const int nt = j7 < 6 ? 4 * side + (j7 >> 1) : 3, th = j7 < 6 ? j7 & 1 : side;
    constexpr int XIN = OFF_XT + (2 * LAYER + 1) * XT_BYTES, XOUT = OFF_XT + 2 * XT_BYTES, HB = OFF_H + LAYER * H_BYTES;
    const u32x4 ws = {(unsigned)(size_t)p.ws, (unsigned)((size_t)p.ws >> 32) & 0xffffu, (unsigned)WS_BYTES, 0x00020000u};
    const int xg = XIN + (4 * pn + w) * TILE_B, xl = lds + w * TILE_B, q = OFF_QT + (4 * it + w) * TILE_B;
    const int pp = (LAYER == 0 ? OFF_PT : OFF_W2T) + (2 * nt * 112 + 28 * w) * 1024;
    const int hs = HB + (4 * pn * 112 + 8 * it + 2 * w) * 1024, hl = HB + ((4 * pn + th) * 112 + 28 * w) * 1024;
    const int fr = OFF_FLAG + (LAYER * 16 + pn) * 14 * 128, fw = fr + it * 128 + w * 4;
    const int b1 = OFF_B1C + (128 * it + 32 * w) * 4, b2 = (LAYER == 0 ? OFF_B2H : OFF_B2F) + nt * 256;
    const int zr = OFF_XT + 2 * LAYER * XT_BYTES + (b == 0 ? 1 : 2 * b) * 64, zi = OFF_XT + 2 * LAYER * XT_BYTES + (b == 0 ? 0 : 2 * b + 1) * 64;
    const int wt = OFF_WT + b * 8192 + w * 2048, keep = 0 - ((b + 255) >> 8), pq = OFF_QT + ((b & 1) * 98 + min((b >> 3) * 4 + w, 97)) * 8192;
    const int ci = r * 4 + w, pt = ci < 32 ? (LAYER == 0 ? OFF_PT : OFF_W2T) + ((2 * (ci < 24 ? 4 * side + (ci >> 3) : 3) + ((ci >> 2) & 1)) * 112 + 28 * (ci & 3)) * 1024 : pq;
    const int bw = OFF_BAR + LAYER * BAR_B + (b / 56) * 64 * ST_ + (b % 56) * ST_, br = OFF_BAR + LAYER * BAR_B + (b % (4 * RL_)) * 256 * ST_;
#define G_OPS [ws] "s"(ws), [lds] "s"(lds), [w] "s"(w), [xg] "s"(xg), [xl] "s"(xl), [q] "s"(q), [p] "s"(pp), [hs] "s"(hs), [hl] "s"(hl), \
    [fr] "s"(fr), [fw] "s"(fw), [b1] "s"(b1), [b2] "s"(b2), [sq] "s"(OFF_SSQ + LAYER * 32768), [zr] "s"(zr), [zi] "s"(zi), [wt] "s"(wt), \
    [keep] "s"(keep), [pq] "s"(pq), [pp] "s"(pt), [bw] "s"(bw), [br] "s"(br)
    if (LAYER == 0) {
        const int xo = XOUT + (4 * pn + th) * TILE_B + nt * 4096 + w * 16, rl = lds + th * TILE_B + nt * 4096 + w * 16;
        const int so = OFF_SSQ + 32768 + (nt >> 1) * 8192 + (nt & 1) * 2 + ((4 * pn + th) * 4 + w) * 16, mk = nt == 0 ? 0 : 0x3f800000;
        unsigned long long c0, c1;
        SYM_ADDR(c0, "_Z6k_mlp16Params"); SYM_ADDR(c1, "ilg_end1");
        const unsigned long long ct = c0 + (unsigned)min((r * 4 + w) * 192, (int)(c1 - c0) - 192);
        asm volatile(
    "s_nop 4\n"
    "s_mov_b32 s58, %[p]\n"
    "s_add_u32 s59, %[p], 114688\n"
    "v_mbcnt_lo_u32_b32 v56, -1, 0\n"
    "s_lshl_b32 s40, %[w], 6\n"
    "v_mbcnt_hi_u32_b32 v56, -1, v56\n"
    "s_add_u32 s41, %[sq], 8192\n"
    "v_add_u32 v70, s40, v56\n"
    "v_lshlrev_b32 v58, 4, v70\n"
    "s_add_u32 s42, %[sq], 16384\n"
    "s_add_u32 s43, %[sq], 24576\n"
    "buffer_load_dwordx4 v[8:11], v58, %[ws], %[sq] offen\n"
    "v_add_u32 v60, 4096, v58\n"
    "v_lshrrev_b32 v57, 2, v70\n"
    "buffer_load_dwordx4 v[12:15], v60, %[ws], %[sq] offen\n"
    "v_mul_u32_u24 v57, 28672, v57\n"
    "v_and_b32 v61, 48, v58\n"
    "v_add_u32 v57, v57, v61\n"
    "buffer_load_dwordx4 v[0:3], v57, %[ws], %[zr] offen\n"
    "v_and_b32 v61, 15, v56\n"
    "s_lshl_b32 s44, %[w], 4\n"
    "v_lshrrev_b32 v62, 4, v56\n"
    "v_add_u32 v176, s44, v61\n"
    "v_cvt_f32_ubyte0 v177, v62\n"
    "v_cvt_f32_ubyte0 v176, v176\n"
    "v_lshlrev_b32 v63, 31, v56\n"
    "buffer_load_dwordx4 v[16:19], v58, %[ws], s41 offen\n"
    "v_mul_f32 v178, 0x3c800000, v176\n"
    "v_mul_f32 v179, 0x3e000000, v176\n"
    "v_lshrrev_b32 v71, 16, v63\n"
    "v_mul_f32 v180, v179, v177\n"
    "v_or_b32 v63, v63, v71\n"
    "v_fmamk_f32 v181, v178, 0x3f800000, v180\n"
    "v_fmamk_f32 v182, v178, 0x40000000, v180\n"
    "buffer_load_dwordx4 v[20:23], v60, %[ws], s41 offen\n"
    "v_fmamk_f32 v183, v178, 0x40400000, v180\n"
    "v_fmamk_f32 v184, v178, 0x40800000, v180\n"
    "v_fmamk_f32 v185, v178, 0x40a00000, v180\n"
    "v_fmamk_f32 v186, v178, 0x40c00000, v180\n"
    "v_fmamk_f32 v187, v178, 0x40e00000, v180\n"
    "v_cos_f32 v188, v180\n"
    "buffer_load_dwordx4 v[4:7], v57, %[ws], %[zi] offen\n"
    "v_cos_f32 v189, v181\n"
    "v_sin_f32 v190, v180\n"
    "v_sin_f32 v191, v181\n"
    "v_cvt_pk_bf16_f32 v72, v188, v189\n"
    "buffer_load_dwordx4 v[24:27], v58, %[ws], s42 offen\n"
    "v_cvt_pk_bf16_f32 v80, v190, v191\n"
    "v_cvt_pk_bf16_f32 v88, -v190, -v191\n"
    "v_cos_f32 v188, v182\n"
    "v_cos_f32 v189, v183\n"
    "v_sin_f32 v190, v182\n"
    "buffer_load_dwordx4 v[28:31], v60, %[ws], s42 offen\n"
    "v_sin_f32 v191, v183\n"
    "v_cvt_pk_bf16_f32 v73, v188, v189\n"
    "v_cvt_pk_bf16_f32 v81, v190, v191\n"
    "v_cvt_pk_bf16_f32 v89, -v190, -v191\n"
    "v_cos_f32 v188, v184\n"
    "buffer_load_dwordx4 v[32:35], v58, %[ws], s43 offen\n"
    "v_cos_f32 v189, v185\n"
    "v_sin_f32 v190, v184\n"
    "v_sin_f32 v191, v185\n"
    "v_cvt_pk_bf16_f32 v74, v188, v189\n"
    "buffer_load_dwordx4 v[36:39], v60, %[ws], s43 offen\n"
    "s_lshr_b32 s45, %[w], 1\n"
    "v_lshlrev_b32 v60, 4, v56\n"
    "s_and_b32 s46, %[w], 1\n"
    "v_lshl_add_u32 v59, v61, 1, s45\n"
    "s_lshl_b32 s46, s46, 5\n"
    "v_mul_u32_u24 v59, 28672, v59\n"
    "v_lshl_add_u32 v70, v62, 3, s46\n"
    "v_add_u32 v59, v59, v70\n"
    "v_cvt_pk_bf16_f32 v82, v190, v191\n"
    "v_cvt_pk_bf16_f32 v90, -v190, -v191\n"
    "v_cos_f32 v188, v186\n"
    "v_cos_f32 v189, v187\n"
    "v_sin_f32 v190, v186\n"
    "buffer_load_dwordx4 v[40:43], v60, %[ws], %[wt] offen\n"
    "v_sin_f32 v191, v187\n"
    "v_cvt_pk_bf16_f32 v75, v188, v189\n"
    "v_cvt_pk_bf16_f32 v83, v190, v191\n"
    "v_cvt_pk_bf16_f32 v91, -v190, -v191\n"
    "v_lshrrev_b32 v64, 7, v58\n"
    "v_lshrrev_b32 v70, 4, v58\n"
    "buffer_load_dwordx4 v[44:47], v60, %[ws], %[wt] offen offset:1024\n"
    "v_lshrrev_b32 v65, 8, v58\n"
    "v_xor_b32 v70, v70, v65\n"
    "v_and_b32 v70, 7, v70\n"
    "v_lshlrev_b32 v64, 7, v64\n"
    "v_lshl_add_u32 v64, v70, 4, v64\n"
    "v_add_u32 v64, %[lds], v64\n"
    "v_lshrrev_b32 v70, 1, v61\n"
    "v_lshlrev_b32 v66, 7, v61\n"
    "v_xor_b32 v65, v62, v70\n"
    "v_add_u32 v66, %[lds], v66\n"
    "s_lshl_b32 s49, %[w], 1\n"
    "v_lshrrev_b32 v67, 1, v62\n"
    "v_lshl_add_u32 v65, v65, 4, v66\n"
    "v_add_u32 v67, s49, v67\n"
    "v_xor_b32 v67, v67, v70\n"
    "v_lshl_add_u32 v67, v67, 4, v66\n"
    "v_and_b32 v70, 1, v62\n"
    "v_xor_b32 v66, 64, v65\n"
    "v_lshl_add_u32 v67, v70, 3, v67\n"
    "v_mov_b32 v69, 0x3f803f80\n"
    "s_mov_b32 s50, 0x3b92ccf7\n"
    "v_xor_b32 v76, v63, v72\n"
    "v_xor_b32 v84, v63, v80\n"
    "v_xor_b32 v92, v63, v88\n"
    "v_xor_b32 v77, v63, v73\n"
    "v_xor_b32 v85, v63, v81\n"
    "v_xor_b32 v93, v63, v89\n"
    "v_xor_b32 v78, v63, v74\n"
    "v_xor_b32 v86, v63, v82\n"
    "v_xor_b32 v94, v63, v90\n"
    "v_xor_b32 v79, v63, v75\n"
    "v_xor_b32 v87, v63, v83\n"
    "v_xor_b32 v95, v63, v91\n"
    "v_mul_f32 v178, 0x3a000000, v176\n"
    "v_mul_f32 v179, 0x3b000000, v176\n"
    "v_mul_f32 v180, v179, v177\n"
    "v_fmamk_f32 v181, v178, 0x3f800000, v180\n"
    "v_fmamk_f32 v182, v178, 0x40000000, v180\n"
    "v_fmamk_f32 v183, v178, 0x40400000, v180\n"
    "v_fmamk_f32 v184, v178, 0x41800000, v180\n"
    "v_fmamk_f32 v185, v178, 0x41880000, v180\n"
    "v_fmamk_f32 v186, v178, 0x41900000, v180\n"
    "v_fmamk_f32 v187, v178, 0x41980000, v180\n"
    "v_cos_f32 v120, v180\n"
    "v_sin_f32 v128, v180\n"
    "v_cos_f32 v121, v181\n"
    "v_sin_f32 v129, v181\n"
    "v_cos_f32 v122, v182\n"
    "v_sin_f32 v130, v182\n"
    "v_cos_f32 v123, v183\n"
    "v_sin_f32 v131, v183\n"
    "v_cos_f32 v124, v184\n"
    "v_sin_f32 v132, v184\n"
    "v_cos_f32 v125, v185\n"
    "v_sin_f32 v133, v185\n"
    "v_cos_f32 v126, v186\n"
    "v_sin_f32 v134, v186\n"
    "v_cos_f32 v127, v187\n"
    "v_sin_f32 v135, v187\n"
    "v_cvt_f32_ubyte0 v178, v61\n"
    "v_xor_b32 v71, 0x80000000, v63\n"
    "v_mul_f32 v179, 0x3e000000, v178\n"
    "v_mul_f32 v192, 0x3d000000, v178\n"
    "v_mul_f32 v180, v179, v177\n"
    "v_fmamk_f32 v181, v192, 0x3f800000, v180\n"
    "v_fmamk_f32 v182, v192, 0x40000000, v180\n"
    "v_fmamk_f32 v183, v192, 0x40400000, v180\n"
    "v_cos_f32 v188, v180\n"
    "v_cos_f32 v189, v181\n"
    "v_sin_f32 v190, v180\n"
    "v_sin_f32 v191, v181\n"
    "v_cvt_pk_bf16_f32 v96, v188, v189\n"
    "v_cvt_pk_bf16_f32 v104, v190, v191\n"
    "v_cvt_pk_bf16_f32 v112, -v190, -v191\n"
    "v_cos_f32 v188, v182\n"
    "v_cos_f32 v189, v183\n"
    "v_sin_f32 v190, v182\n"
    "v_sin_f32 v191, v183\n"
    "v_cvt_pk_bf16_f32 v97, v188, v189\n"
    "v_cvt_pk_bf16_f32 v105, v190, v191\n"
    "v_cvt_pk_bf16_f32 v113, -v190, -v191\n"
    "v_xor_b32 v98, v63, v96\n"
    "v_xor_b32 v100, 0x80000000, v96\n"
    "v_xor_b32 v102, v71, v96\n"
    "v_xor_b32 v99, v63, v97\n"
    "v_xor_b32 v101, 0x80000000, v97\n"
    "v_xor_b32 v103, v71, v97\n"
    "v_xor_b32 v106, v63, v104\n"
    "v_xor_b32 v108, 0x80000000, v104\n"
    "v_xor_b32 v110, v71, v104\n"
    "v_xor_b32 v107, v63, v105\n"
    "v_xor_b32 v109, 0x80000000, v105\n"
    "v_xor_b32 v111, v71, v105\n"
    "v_xor_b32 v114, v63, v112\n"
    "v_xor_b32 v116, 0x80000000, v112\n"
    "v_xor_b32 v118, v71, v112\n"
    "v_xor_b32 v115, v63, v113\n"
    "v_xor_b32 v117, 0x80000000, v113\n"
    "v_xor_b32 v119, v71, v113\n"
    "v_cvt_f32_u32 v179, s44\n"
    "v_mul_f32 v192, 0x3a000000, v178\n"
    "v_fmamk_f32 v179, v177, 0x40800000, v179\n"
    "v_add_f32 v193, 0x3c000000, v192\n"
    "v_mov_b32 v194, 0x3c2953fd\n"
    "v_mul_f32 v180, v179, v192\n"
    "v_mul_f32 v184, v179, v193\n"
    "v_fmamk_f32 v181, v192, 0x3f800000, v180\n"
    "v_fmamk_f32 v182, v192, 0x40000000, v180\n"
    "v_fmamk_f32 v183, v192, 0x40400000, v180\n"
    "v_fmamk_f32 v185, v193, 0x3f800000, v184\n"
    "v_fmamk_f32 v186, v193, 0x40000000, v184\n"
    "v_fmamk_f32 v187, v193, 0x40400000, v184\n"
    "v_cos_f32 v136, v180\n"
    "v_sin_f32 v144, v180\n"
    "v_cos_f32 v137, v181\n"
    "v_sin_f32 v145, v181\n"
    "v_cos_f32 v138, v182\n"
    "v_sin_f32 v146, v182\n"
    "v_cos_f32 v139, v183\n"
    "v_sin_f32 v147, v183\n"
    "v_cos_f32 v140, v184\n"
    "v_sin_f32 v148, v184\n"
    "v_cos_f32 v141, v185\n"
    "v_sin_f32 v149, v185\n"
    "v_cos_f32 v142, v186\n"
    "v_sin_f32 v150, v186\n"
    "v_cos_f32 v143, v187\n"
    "v_sin_f32 v151, v187\n"
    "v_pk_mul_f32 v[136:137], v[136:137], v[194:195] op_sel_hi:[1,0]\n"
    "v_pk_mul_f32 v[138:139], v[138:139], v[194:195] op_sel_hi:[1,0]\n"
    "v_pk_mul_f32 v[140:141], v[140:141], v[194:195] op_sel_hi:[1,0]\n"
    "v_pk_mul_f32 v[142:143], v[142:143], v[194:195] op_sel_hi:[1,0]\n"
    "v_pk_mul_f32 v[144:145], v[144:145], v[194:195] op_sel_hi:[1,0]\n"
    "v_pk_mul_f32 v[146:147], v[146:147], v[194:195] op_sel_hi:[1,0]\n"
    "v_pk_mul_f32 v[148:149], v[148:149], v[194:195] op_sel_hi:[1,0]\n"
    "v_pk_mul_f32 v[150:151], v[150:151], v[194:195] op_sel_hi:[1,0]\n"
    "s_waitcnt vmcnt(10)\n"
    "v_dot2_f32_bf16 v168, v8, v69, s50\n"
    "v_dot2_f32_bf16 v169, v9, v69, s50\n"
    "v_dot2_f32_bf16 v170, v10, v69, s50\n"
    "v_dot2_f32_bf16 v171, v11, v69, s50\n"
    "v_dot2_f32_bf16 v172, v12, v69, s50\n"
    "v_dot2_f32_bf16 v173, v13, v69, s50\n"
    "v_dot2_f32_bf16 v174, v14, v69, s50\n"
    "v_dot2_f32_bf16 v175, v15, v69, s50\n"
    "s_waitcnt vmcnt(9)\n"
    "ds_write_b128 v64, v[0:3] offset:16384\n"
    "v_lshlrev_b32 v192, 16, v0\n"
    "v_and_b32 v193, 0xffff0000, v0\n"
    "v_lshlrev_b32 v194, 16, v1\n"
    "v_and_b32 v195, 0xffff0000, v1\n"
    "v_lshlrev_b32 v196, 16, v2\n"
    "v_and_b32 v197, 0xffff0000, v2\n"
    "v_lshlrev_b32 v198, 16, v3\n"
    "v_and_b32 v199, 0xffff0000, v3\n"
    "s_waitcnt vmcnt(7)\n"
    "v_dot2c_f32_bf16 v168, v16, v69\n"
    "v_dot2c_f32_bf16 v169, v17, v69\n"
    "v_dot2c_f32_bf16 v170, v18, v69\n"
    "v_dot2c_f32_bf16 v171, v19, v69\n"
    "v_dot2c_f32_bf16 v172, v20, v69\n"
    "v_dot2c_f32_bf16 v173, v21, v69\n"
    "v_dot2c_f32_bf16 v174, v22, v69\n"
    "v_dot2c_f32_bf16 v175, v23, v69\n"
    "s_waitcnt vmcnt(6)\n"
    "ds_write_b128 v64, v[4:7] offset:20480\n"
    "v_lshlrev_b32 v200, 16, v4\n"
    "v_and_b32 v201, 0xffff0000, v4\n"
    "v_lshlrev_b32 v202, 16, v5\n"
    "v_and_b32 v203, 0xffff0000, v5\n"
    "v_lshlrev_b32 v204, 16, v6\n"
    "v_and_b32 v205, 0xffff0000, v6\n"
    "v_lshlrev_b32 v206, 16, v7\n"
    "v_and_b32 v207, 0xffff0000, v7\n"
    "s_waitcnt vmcnt(4)\n"
    "v_dot2c_f32_bf16 v168, v24, v69\n"
    "v_dot2c_f32_bf16 v169, v25, v69\n"
    "v_dot2c_f32_bf16 v170, v26, v69\n"
    "v_dot2c_f32_bf16 v171, v27, v69\n"
    "v_dot2c_f32_bf16 v172, v28, v69\n"
    "v_dot2c_f32_bf16 v173, v29, v69\n"
    "v_dot2c_f32_bf16 v174, v30, v69\n"
    "v_dot2c_f32_bf16 v175, v31, v69\n"
    "s_waitcnt vmcnt(2)\n"
    "v_dot2c_f32_bf16 v168, v32, v69\n"
    "v_dot2c_f32_bf16 v169, v33, v69\n"
    "v_dot2c_f32_bf16 v170, v34, v69\n"
    "v_dot2c_f32_bf16 v171, v35, v69\n"
    "v_dot2c_f32_bf16 v172, v36, v69\n"
    "v_dot2c_f32_bf16 v173, v37, v69\n"
    "v_dot2c_f32_bf16 v174, v38, v69\n"
    "v_dot2c_f32_bf16 v175, v39, v69\n"
    "v_mov_b32 v68, %[keep]\n"
    "v_rsq_f32 v168, v168\n"
    "v_rsq_f32 v169, v169\n"
    "v_rsq_f32 v170, v170\n"
    "v_rsq_f32 v171, v171\n"
    "v_rsq_f32 v172, v172\n"
    "v_rsq_f32 v173, v173\n"
    "v_rsq_f32 v174, v174\n"
    "v_rsq_f32 v175, v175\n"
    "v_pk_mul_f32 v[192:193], v[192:193], v[168:169]\n"
    "v_pk_mul_f32 v[194:195], v[194:195], v[170:171]\n"
    "v_pk_mul_f32 v[196:197], v[196:197], v[172:173]\n"
    "v_pk_mul_f32 v[198:199], v[198:199], v[174:175]\n"
    "v_cvt_pk_bf16_f32 v208, v192, v193\n"
    "v_cvt_pk_bf16_f32 v209, v194, v195\n"
    "v_cvt_pk_bf16_f32 v210, v196, v197\n"
    "v_cvt_pk_bf16_f32 v211, v198, v199\n"
    "ds_write_b128 v64, v[208:211]\n"
    "v_pk_mul_f32 v[200:201], v[200:201], v[168:169]\n"
    "v_pk_mul_f32 v[202:203], v[202:203], v[170:171]\n"
    "v_pk_mul_f32 v[204:205], v[204:205], v[172:173]\n"
    "v_pk_mul_f32 v[206:207], v[206:207], v[174:175]\n"
    "v_cvt_pk_bf16_f32 v212, v200, v201\n"
    "v_cvt_pk_bf16_f32 v213, v202, v203\n"
    "v_cvt_pk_bf16_f32 v214, v204, v205\n"
    "v_cvt_pk_bf16_f32 v215, v206, v207\n"
    "v_and_b32 v212, v68, v212\n"
    "v_and_b32 v213, v68, v213\n"
    "v_and_b32 v214, v68, v214\n"
    "v_and_b32 v215, v68, v215\n"
    "ds_write_b128 v64, v[212:215] offset:4096\n"
    "s_waitcnt lgkmcnt(0)\n"
    "s_barrier\n"
    "ds_read_b128 v[208:211], v65 offset:0\n"
    "ds_read_b128 v[212:215], v66 offset:0\n"
    "ds_read_b128 v[216:219], v65 offset:4096\n"
    "ds_read_b128 v[220:223], v66 offset:4096\n"
    "ds_read_b128 v[224:227], v65 offset:2048\n"
    "ds_read_b128 v[228:231], v66 offset:2048\n"
    "ds_read_b128 v[232:235], v65 offset:6144\n"
    "ds_read_b128 v[236:239], v66 offset:6144\n"
    "s_waitcnt vmcnt(0)\n"
    "v_mul_u32_u24 v70, 128, v56\n"
    "buffer_load_dword a224, v70, %[ws], %[pq] offen\n"
    "v_lshlrev_b32 v152, 16, v40\n"
    "v_and_b32 v160, 0xffff0000, v40\n"
    "v_lshlrev_b32 v153, 16, v41\n"
    "v_and_b32 v161, 0xffff0000, v41\n"
    "v_lshlrev_b32 v154, 16, v42\n"
    "v_and_b32 v162, 0xffff0000, v42\n"
    "v_lshlrev_b32 v155, 16, v43\n"
    "v_and_b32 v163, 0xffff0000, v43\n"
    "v_lshlrev_b32 v156, 16, v44\n"
    "v_and_b32 v164, 0xffff0000, v44\n"
    "v_lshlrev_b32 v157, 16, v45\n"
    "v_and_b32 v165, 0xffff0000, v45\n"
    "v_lshlrev_b32 v158, 16, v46\n"
    "v_and_b32 v166, 0xffff0000, v46\n"
    "v_lshlrev_b32 v159, 16, v47\n"
    "v_and_b32 v167, 0xffff0000, v47\n"
    "s_waitcnt lgkmcnt(7)\n"
    "v_mfma_f32_16x16x32_bf16 v[8:11], v[208:211], v[72:75], 0\n"
    "v_mfma_f32_16x16x32_bf16 v[16:19], v[208:211], v[88:91], 0\n"
    "s_waitcnt lgkmcnt(6)\n"
    "v_mfma_f32_16x16x32_bf16 v[8:11], v[212:215], v[76:79], v[8:11]\n"
    "v_mfma_f32_16x16x32_bf16 v[16:19], v[212:215], v[92:95], v[16:19]\n"
    "s_waitcnt lgkmcnt(5)\n"
    "v_mfma_f32_16x16x32_bf16 v[8:11], v[216:219], v[80:83], v[8:11]\n"
    "v_mfma_f32_16x16x32_bf16 v[16:19], v[216:219], v[72:75], v[16:19]\n"
    "s_waitcnt lgkmcnt(4)\n"
    "v_mfma_f32_16x16x32_bf16 v[8:11], v[220:223], v[84:87], v[8:11]\n"
    "v_mfma_f32_16x16x32_bf16 v[16:19], v[220:223], v[76:79], v[16:19]\n"
    "s_waitcnt lgkmcnt(3)\n"
    "v_mfma_f32_16x16x32_bf16 v[12:15], v[224:227], v[72:75], 0\n"
    "v_mfma_f32_16x16x32_bf16 v[20:23], v[224:227], v[88:91], 0\n"
    "s_nop 3\n"
    "v_pk_mul_f32 v[196:197], v[8:9], v[120:121]\n"
    "s_waitcnt lgkmcnt(2)\n"
    "s_nop 0\n"
    "v_mfma_f32_16x16x32_bf16 v[12:15], v[228:231], v[76:79], v[12:15]\n"
    "v_mfma_f32_16x16x32_bf16 v[20:23], v[228:231], v[92:95], v[20:23]\n"
    "v_pk_mul_f32 v[200:201], v[16:17], v[120:121]\n"
    "v_pk_mul_f32 v[198:199], v[10:11], v[122:123]\n"
    "v_pk_mul_f32 v[202:203], v[18:19], v[122:123]\n"
    "v_pk_fma_f32 v[196:197], v[16:17], v[128:129], v[196:197]\n"
    "s_waitcnt lgkmcnt(1)\n"
    "s_nop 0\n"
    "v_mfma_f32_16x16x32_bf16 v[12:15], v[232:235], v[80:83], v[12:15]\n"
    "v_mfma_f32_16x16x32_bf16 v[20:23], v[232:235], v[72:75], v[20:23]\n"
    "v_pk_fma_f32 v[200:201], v[8:9], v[128:129], v[200:201] neg_lo:[1,0,0] neg_hi:[1,0,0]\n"
    "v_pk_fma_f32 v[198:199], v[18:19], v[130:131], v[198:199]\n"
    "v_pk_fma_f32 v[202:203], v[10:11], v[130:131], v[202:203] neg_lo:[1,0,0] neg_hi:[1,0,0]\n"
    "v_cvt_pk_bf16_f32 v24, v196, v197\n"
    "s_waitcnt lgkmcnt(0)\n"
    "v_mfma_f32_16x16x32_bf16 v[12:15], v[236:239], v[84:87], v[12:15]\n"
    "v_mfma_f32_16x16x32_bf16 v[20:23], v[236:239], v[76:79], v[20:23]\n"
    "v_cvt_pk_bf16_f32 v28, v200, v201\n"
    "v_cvt_pk_bf16_f32 v25, v198, v199\n"
    "v_cvt_pk_bf16_f32 v29, v202, v203\n"
    "s_nop 3\n"
    "v_pk_mul_f32 v[196:197], v[12:13], v[124:125]\n"
    "v_pk_mul_f32 v[200:201], v[20:21], v[124:125]\n"
    "v_pk_mul_f32 v[198:199], v[14:15], v[126:127]\n"
    "v_pk_mul_f32 v[202:203], v[22:23], v[126:127]\n"
    "v_pk_fma_f32 v[196:197], v[20:21], v[132:133], v[196:197]\n"
    "v_pk_fma_f32 v[200:201], v[12:13], v[132:133], v[200:201] neg_lo:[1,0,0] neg_hi:[1,0,0]\n"
    "v_pk_fma_f32 v[198:199], v[22:23], v[134:135], v[198:199]\n"
    "v_pk_fma_f32 v[202:203], v[14:15], v[134:135], v[202:203] neg_lo:[1,0,0] neg_hi:[1,0,0]\n"
    "v_cvt_pk_bf16_f32 v26, v196, v197\n"
    "v_cvt_pk_bf16_f32 v30, v200, v201\n"
    "v_cvt_pk_bf16_f32 v27, v198, v199\n"
    "v_cvt_pk_bf16_f32 v31, v202, v203\n"
    "s_nop 0\n"
    "v_mfma_f32_16x16x32_bf16 v[8:11], v[96:99], v[24:27], 0\n"
    "v_mfma_f32_16x16x32_bf16 v[16:19], v[96:99], v[28:31], 0\n"
    "v_mfma_f32_16x16x32_bf16 v[8:11], v[104:107], v[28:31], v[8:11]\n"
    "v_mfma_f32_16x16x32_bf16 v[16:19], v[112:115], v[24:27], v[16:19]\n"
    "v_mfma_f32_16x16x32_bf16 v[12:15], v[100:103], v[24:27], 0\n"
    "v_mfma_f32_16x16x32_bf16 v[20:23], v[100:103], v[28:31], 0\n"
    "v_mfma_f32_16x16x32_bf16 v[12:15], v[108:111], v[28:31], v[12:15]\n"
    "v_mfma_f32_16x16x32_bf16 v[20:23], v[116:119], v[24:27], v[20:23]\n"
    "s_nop 2\n"
    "v_pk_mul_f32 v[196:197], v[8:9], v[152:153]\n"
    "v_pk_mul_f32 v[200:201], v[16:17], v[152:153]\n"
    "v_pk_mul_f32 v[198:199], v[10:11], v[154:155]\n"
    "v_pk_mul_f32 v[202:203], v[18:19], v[154:155]\n"
    "v_pk_fma_f32 v[196:197], v[16:17], v[160:161], v[196:197] neg_lo:[1,0,0] neg_hi:[1,0,0]\n"
    "v_pk_fma_f32 v[200:201], v[8:9], v[160:161], v[200:201]\n"
    "v_pk_fma_f32 v[198:199], v[18:19], v[162:163], v[198:199] neg_lo:[1,0,0] neg_hi:[1,0,0]\n"
    "v_pk_fma_f32 v[202:203], v[10:11], v[162:163], v[202:203]\n"
    "v_cvt_pk_bf16_f32 v32, v196, v197\n"
    "v_cvt_pk_bf16_f32 v36, v200, v201\n"
    "v_cvt_pk_bf16_f32 v33, v198, v199\n"
    "v_cvt_pk_bf16_f32 v37, v202, v203\n"
    "v_pk_mul_f32 v[188:189], v[12:13], v[156:157]\n"
    "v_pk_mul_f32 v[192:193], v[20:21], v[156:157]\n"
    "v_pk_mul_f32 v[190:191], v[14:15], v[158:159]\n"
    "v_pk_mul_f32 v[194:195], v[22:23], v[158:159]\n"
    "v_pk_fma_f32 v[188:189], v[20:21], v[164:165], v[188:189] neg_lo:[1,0,0] neg_hi:[1,0,0]\n"
    "v_pk_fma_f32 v[192:193], v[12:13], v[164:165], v[192:193]\n"
    "v_pk_fma_f32 v[190:191], v[22:23], v[166:167], v[190:191] neg_lo:[1,0,0] neg_hi:[1,0,0]\n"
    "v_pk_fma_f32 v[194:195], v[14:15], v[166:167], v[194:195]\n"
    "v_cvt_pk_bf16_f32 v34, v188, v189\n"
    "v_cvt_pk_bf16_f32 v38, v192, v193\n"
    "v_cvt_pk_bf16_f32 v35, v190, v191\n"
    "v_cvt_pk_bf16_f32 v39, v194, v195\n"
    "ds_read2st64_b64 v[48:51], v67 offset0:32 offset1:40\n"
    "ds_read2st64_b64 v[52:55], v67 offset0:36 offset1:44\n"
    "v_mfma_f32_16x16x32_bf16 v[8:11], v[32:35], v[96:99], 0\n"
    "v_mfma_f32_16x16x32_bf16 v[16:19], v[36:39], v[96:99], 0\n"
    "v_mfma_f32_16x16x32_bf16 v[8:11], v[36:39], v[112:115], v[8:11]\n"
    "v_mfma_f32_16x16x32_bf16 v[16:19], v[32:35], v[104:107], v[16:19]\n"
    "v_mfma_f32_16x16x32_bf16 v[12:15], v[32:35], v[100:103], 0\n"
    "v_mfma_f32_16x16x32_bf16 v[20:23], v[36:39], v[100:103], 0\n"
    "v_mfma_f32_16x16x32_bf16 v[12:15], v[36:39], v[116:119], v[12:15]\n"
    "v_mfma_f32_16x16x32_bf16 v[20:23], v[32:35], v[108:111], v[20:23]\n"
    "s_nop 2\n"
    "v_pk_mul_f32 v[196:197], v[8:9], v[136:137]\n"
    "v_pk_mul_f32 v[200:201], v[16:17], v[136:137]\n"
    "v_pk_mul_f32 v[198:199], v[10:11], v[138:139]\n"
    "v_pk_mul_f32 v[202:203], v[18:19], v[138:139]\n"
    "v_pk_fma_f32 v[196:197], v[16:17], v[144:145], v[196:197] neg_lo:[1,0,0] neg_hi:[1,0,0]\n"
    "v_pk_fma_f32 v[200:201], v[8:9], v[144:145], v[200:201]\n"
    "v_pk_fma_f32 v[198:199], v[18:19], v[146:147], v[198:199] neg_lo:[1,0,0] neg_hi:[1,0,0]\n"
    "v_pk_fma_f32 v[202:203], v[10:11], v[146:147], v[202:203]\n"
    "v_cvt_pk_bf16_f32 v24, v196, v197\n"
    "v_cvt_pk_bf16_f32 v26, v200, v201\n"
    "v_cvt_pk_bf16_f32 v25, v198, v199\n"
    "v_cvt_pk_bf16_f32 v27, v202, v203\n"
    "ds_write2st64_b64 v67, v[24:25], v[26:27] offset0:16 offset1:24\n"
    "v_pk_mul_f32 v[188:189], v[12:13], v[140:141]\n"
    "v_pk_mul_f32 v[192:193], v[20:21], v[140:141]\n"
    "v_pk_mul_f32 v[190:191], v[14:15], v[142:143]\n"
    "v_pk_mul_f32 v[194:195], v[22:23], v[142:143]\n"
    "v_pk_fma_f32 v[188:189], v[20:21], v[148:149], v[188:189] neg_lo:[1,0,0] neg_hi:[1,0,0]\n"
    "v_pk_fma_f32 v[192:193], v[12:13], v[148:149], v[192:193]\n"
    "v_pk_fma_f32 v[190:191], v[22:23], v[150:151], v[190:191] neg_lo:[1,0,0] neg_hi:[1,0,0]\n"
    "v_pk_fma_f32 v[194:195], v[14:15], v[150:151], v[194:195]\n"
    "v_cvt_pk_bf16_f32 v28, v188, v189\n"
    "v_cvt_pk_bf16_f32 v30, v192, v193\n"
    "v_cvt_pk_bf16_f32 v29, v190, v191\n"
    "v_cvt_pk_bf16_f32 v31, v194, v195\n"
    "ds_write2st64_b64 v67, v[28:29], v[30:31] offset0:20 offset1:28\n"
    "s_waitcnt lgkmcnt(2)\n"
    "v_lshlrev_b32 v240, 16, v48\n"
    "v_and_b32 v241, 0xffff0000, v48\n"
    "v_lshlrev_b32 v248, 16, v50\n"
    "v_and_b32 v249, 0xffff0000, v50\n"
    "v_lshlrev_b32 v242, 16, v49\n"
    "v_and_b32 v243, 0xffff0000, v49\n"
    "v_lshlrev_b32 v250, 16, v51\n"
    "s_waitcnt lgkmcnt(0)\n"
    "s_barrier\n"
    "ds_read_b128 v[208:211], v65 offset:8192\n"
    "ds_read_b128 v[212:215], v66 offset:8192\n"
    "ds_read_b128 v[216:219], v65 offset:12288\n"
    "ds_read_b128 v[220:223], v66 offset:12288\n"
    "ds_read_b128 v[224:227], v65 offset:10240\n"
    "ds_read_b128 v[228:231], v66 offset:10240\n"
    "ds_read_b128 v[232:235], v65 offset:14336\n"
    "ds_read_b128 v[236:239], v66 offset:14336\n"
    "v_and_b32 v251, 0xffff0000, v51\n"
    "v_lshlrev_b32 v244, 16, v52\n"
    "v_and_b32 v245, 0xffff0000, v52\n"
    "v_lshlrev_b32 v252, 16, v54\n"
    "v_and_b32 v253, 0xffff0000, v54\n"
    "v_lshlrev_b32 v246, 16, v53\n"
    "v_and_b32 v247, 0xffff0000, v53\n"
    "v_lshlrev_b32 v254, 16, v55\n"
    "v_and_b32 v255, 0xffff0000, v55\n"
    "s_add_u32 s47, %[zr], 2752512\n"
    "s_add_u32 s48, %[zi], 2752512\n"
    "s_add_u32 s45, %[zr], 1835008\n"
    "s_add_u32 s46, %[zi], 1835008\n"
    "s_waitcnt lgkmcnt(7)\n"
    "v_mfma_f32_16x16x32_bf16 v[240:243], v[72:75], v[208:211], v[240:243]\n"
    "v_mfma_f32_16x16x32_bf16 v[248:251], v[80:83], v[208:211], v[248:251]\n"
    "s_waitcnt lgkmcnt(6)\n"
    "v_mfma_f32_16x16x32_bf16 v[240:243], v[76:79], v[212:215], v[240:243]\n"
    "v_mfma_f32_16x16x32_bf16 v[248:251], v[84:87], v[212:215], v[248:251]\n"
    "s_waitcnt lgkmcnt(5)\n"
    "v_mfma_f32_16x16x32_bf16 v[240:243], v[88:91], v[216:219], v[240:243]\n"
    "v_mfma_f32_16x16x32_bf16 v[248:251], v[72:75], v[216:219], v[248:251]\n"
    "s_waitcnt lgkmcnt(4)\n"
    "v_mfma_f32_16x16x32_bf16 v[240:243], v[92:95], v[220:223], v[240:243]\n"
    "v_mfma_f32_16x16x32_bf16 v[248:251], v[76:79], v[220:223], v[248:251]\n"
    "s_waitcnt lgkmcnt(3)\n"
    "v_mfma_f32_16x16x32_bf16 v[244:247], v[72:75], v[224:227], v[244:247]\n"
    "v_nop\n"
    "s_nop 3\n"
    "v_cvt_pk_bf16_f32 v176, v240, v241\n"
    "v_mfma_f32_16x16x32_bf16 v[252:255], v[80:83], v[224:227], v[252:255]\n"
    "v_cvt_pk_bf16_f32 v177, v242, v243\n"
    "buffer_store_dwordx2 v[176:177], v59, %[ws], s45 offen sc1\n"
    "s_waitcnt lgkmcnt(2)\n"
    "v_mfma_f32_16x16x32_bf16 v[244:247], v[76:79], v[228:231], v[244:247]\n"
    "v_cvt_pk_bf16_f32 v178, v248, v249\n"
    "v_cvt_pk_bf16_f32 v179, v250, v251\n"
    "v_mfma_f32_16x16x32_bf16 v[252:255], v[84:87], v[228:231], v[252:255]\n"
    "v_bfi_b32 v178, v68, v178, v50\n"
    "v_bfi_b32 v179, v68, v179, v51\n"
    "s_waitcnt lgkmcnt(1)\n"
    "v_mfma_f32_16x16x32_bf16 v[244:247], v[88:91], v[232:235], v[244:247]\n"
    "buffer_store_dwordx2 v[178:179], v59, %[ws], s46 offen sc1\n"
    "v_mfma_f32_16x16x32_bf16 v[252:255], v[72:75], v[232:235], v[252:255]\n"
    "s_waitcnt lgkmcnt(0)\n"
    "v_mfma_f32_16x16x32_bf16 v[244:247], v[92:95], v[236:239], v[244:247]\n"
    "v_mfma_f32_16x16x32_bf16 v[252:255], v[76:79], v[236:239], v[252:255]\n"
    "s_nop 6\n"
    "v_cvt_pk_bf16_f32 v180, v244, v245\n"
    "v_cvt_pk_bf16_f32 v181, v246, v247\n"
    "buffer_store_dwordx2 v[180:181], v59, %[ws], s47 offen sc1\n"
    "v_cvt_pk_bf16_f32 v182, v252, v253\n"
    "v_cvt_pk_bf16_f32 v183, v254, v255\n"
    "v_bfi_b32 v182, v68, v182, v54\n"
    "v_bfi_b32 v183, v68, v183, v55\n"
    "buffer_store_dwordx2 v[182:183], v59, %[ws], s48 offen sc1\n"
    "v_mbcnt_lo_u32_b32 v255, -1, 0\n"
    "s_add_u32 s47, %[lds], 152064\n"
    "v_mbcnt_hi_u32_b32 v255, -1, v255\n"
    "s_lshl_b32 s46, %[w], 8\n"
    "v_lshlrev_b32 v236, 4, v255\n"
    "s_mov_b32 s40, %[xg]\n"
    "s_mov_b32 s41, %[q]\n"
    "v_lshlrev_b32 v242, 2, v255\n"
    "s_add_u32 s47, s47, s46\n"
    "s_mul_i32 s56, %[w], 8192\n"
    "v_mul_u32_u24 v130, 4096, v255\n"
    "s_add_u32 s56, s56, %[bw]\n"
    "v_min_u32 v131, 55, v255\n"
    "s_mul_i32 s57, %[w], 1024\n"
    "v_mul_u32_u24 v131, 16, v131\n"
    "s_add_u32 s57, s57, %[br]\n"
    "v_mov_b32 v132, 1\n"
    "s_mov_b32 s46, 0x10000\n"
    "s_mov_b32 s44, %[hl]\n"
    "s_mov_b32 s45, %[hs]\n"
    "s_mov_b32 s42, s57\n"
    "s_waitcnt vmcnt(0)\n"
    "s_barrier\n"
    "s_mov_b64 exec, 3\n"
    "buffer_store_dword v132, v130, %[ws], s56 offen sc1\n"
    "s_mov_b64 exec, -1\n"
    "s_mov_b32 m0, s47\n"
    "s_nop 0\n"
    "buffer_load_dword v242, %[ws], %[b1] offen lds\n"
    "buffer_load_dword v184, v242, %[ws], %[b2] offen\n"
    "s_mov_b32 m0, %[xl]\n"
    "buffer_load_dwordx4 v[64:67], v236, %[ws], s41 offen offset:0\n"
    "v_and_b32 v242, 3, v255\n"
    "v_and_b32 v243, 12, v255\n"
    "v_lshlrev_b32 v242, 3, v242\n"
    "v_lshl_or_b32 v242, v243, 4, v242\n"
    "v_and_b32 v243, 16, v255\n"
    "v_lshl_or_b32 v242, v243, 1, v242\n"
    "v_and_b32 v243, 32, v255\n"
    "v_lshl_or_b32 v242, v243, 3, v242\n"
    "v_add_u32 v237, %[lds], v242\n"
    "v_add_u32 v239, %[xl], v242\n"
    "v_add_u32 v238, 57344, v237\n"
    "v_and_b32 v243, 16, v255\n"
    "v_mul_u32_u24 v243, 62, v243\n"
    "v_add_u32 v239, v243, v239\n"
    "v_mov_b32 v244, -1\n"
    "s_mov_b32 s48, 0x22222222\n"
    "s_mov_b32 s49, 0x22222222\n"
    "s_mov_b32 s50, 0x44444444\n"
    "s_mov_b32 s51, 0x44444444\n"
    "s_mov_b32 s52, 0x88888888\n"
    "s_mov_b32 s53, 0x88888888\n"
    "s_mov_b64 exec, 0xffff\n"
    "v_mov_b32 v244, 0xffff0000\n"
    "s_mov_b64 exec, -1\n"
    "v_and_b32 v242, 31, v255\n"
    "buffer_load_dwordx4 v[68:71], v236, %[ws], s41 offen offset:1024\n"
    "s_add_u32 s56, %[lds], 151552\n"
    "v_lshlrev_b32 v245, 4, v242\n"
    "v_add_u32 v245, s56, v245\n"
    "v_mov_b32 v240, 0x409d30fc\n"
    "v_mov_b32 v241, 0x3eeff558\n"
    "s_mul_i32 s56, %[w], 9216\n"
    "v_mul_u32_u24 v248, 144, v242\n"
    "s_add_u32 s57, %[lds], 114688\n"
    "v_lshrrev_b32 v243, 5, v255\n"
    "s_add_u32 s56, s56, s57\n"
    "v_lshl_add_u32 v248, v243, 4, v248\n"
    "v_add_u32 v248, s56, v248\n"
    "s_lshl_b32 s56, %[w], 5\n"
    "v_mul_u32_u24 v249, 144, v255\n"
    "s_add_u32 s56, s56, s57\n"
    "v_add_u32 v249, s56, v249\n"
    "v_lshlrev_b32 v250, 6, v255\n"
    "s_mul_i32 s56, %[w], 14\n"
    "v_min_u32 v246, 13, v255\n"
    "v_add_u32 v246, s56, v246\n"
    "v_and_b32 v247, 3, v246\n"
    "v_lshrrev_b32 v246, 2, v246\n"
    "v_lshlrev_b32 v247, 2, v247\n"
    "v_lshl_or_b32 v246, v246, 7, v247\n"
    "buffer_load_dwordx4 v[72:75], v236, %[ws], s41 offen offset:2048\n"
    "s_nop 15\n"
    "buffer_load_dwordx4 v[76:79], v236, %[ws], s41 offen offset:3072\n"
    "s_nop 15\n"
    "s_add_u32 s41, s41, 4096\n"
    "buffer_load_dwordx4 v[80:83], v236, %[ws], s41 offen offset:0\n"
    "s_nop 15\n"
    "buffer_load_dwordx4 v[84:87], v236, %[ws], s41 offen offset:1024\n"
    "s_nop 15\n"
    "buffer_load_dwordx4 v[88:91], v236, %[ws], s41 offen offset:2048\n"
    "s_nop 15\n"
    "buffer_load_dwordx4 v[92:95], v236, %[ws], s41 offen offset:3072\n"
    "s_nop 15\n"
    "s_add_u32 s41, s41, 4096\n"
    "buffer_load_dwordx4 v[96:99], v236, %[ws], s41 offen offset:0\n"
    "s_nop 15\n"
    "buffer_load_dwordx4 v[100:103], v236, %[ws], s41 offen offset:1024\n"
    "s_nop 15\n"
    "buffer_load_dwordx4 v[104:107], v236, %[ws], s41 offen offset:2048\n"
    "s_nop 15\n"
    "buffer_load_dwordx4 v[108:111], v236, %[ws], s41 offen offset:3072\n"
    "s_nop 15\n"
    "buffer_load_dword v133, v131, %[ws], s42 offen sc1\n"
    "buffer_load_dwordx4 a[0:3], v236, %[ws], s58 offen offset:0\n"
    "s_nop 15\n"
    "buffer_load_dwordx4 a[112:115], v236, %[ws], s59 offen offset:0\n"
    "s_nop 15\n"
    "buffer_load_dwordx4 a[4:7], v236, %[ws], s58 offen offset:1024\n"
    "s_nop 15\n"
    "buffer_load_dwordx4 a[116:119], v236, %[ws], s59 offen offset:1024\n"
    "s_nop 15\n"
    "buffer_load_dwordx4 a[8:11], v236, %[ws], s58 offen offset:2048\n"
    "s_nop 15\n"
    "buffer_load_dwordx4 a[120:123], v236, %[ws], s59 offen offset:2048\n"
    "s_nop 15\n"
    "s_waitcnt vmcnt(6)\n"
    "v_cmp_gt_u32 vcc, 1, v133\n"
    "s_nop 4\n"
    "s_cbranch_vccnz LS0_%=\n"
    "LR0_%=:\n"
    "s_barrier\n"
    "buffer_load_dwordx4 v236, %[ws], s40 offen offset:0 sc1 lds\n"
    "buffer_load_dwordx4 v236, %[ws], s40 offen offset:1024 sc1 lds\n"
    "buffer_load_dwordx4 v236, %[ws], s40 offen offset:2048 sc1 lds\n"
    "buffer_load_dwordx4 v236, %[ws], s40 offen offset:3072 sc1 lds\n"
    "s_add_u32 m0, m0, 4096\n"
    "s_add_u32 s40, s40, 4096\n"
    "buffer_load_dwordx4 v236, %[ws], s40 offen offset:0 sc1 lds\n"
    "buffer_load_dwordx4 v236, %[ws], s40 offen offset:1024 sc1 lds\n"
    "s_waitcnt vmcnt(4)\n"
    "ds_read_b64_tr_b16 v[144:145], v239 offset:0\n"
    "ds_read_b64_tr_b16 v[146:147], v239 offset:512\n"
    "ds_read_b64_tr_b16 v[148:149], v239 offset:32\n"
    "ds_read_b64_tr_b16 v[150:151], v239 offset:544\n"
    "s_barrier\n"
    "ds_read_b64_tr_b16 v[112:113], v237 offset:0\n"
    "ds_read_b64_tr_b16 v[114:115], v237 offset:512\n"
    "ds_read_b64_tr_b16 v[116:117], v237 offset:28672\n"
    "ds_read_b64_tr_b16 v[118:119], v237 offset:29184\n"
    "ds_read_b64_tr_b16 v[120:121], v238 offset:0\n"
    "ds_read_b64_tr_b16 v[122:123], v238 offset:512\n"
    "ds_read_b64_tr_b16 v[124:125], v238 offset:28672\n"
    "ds_read_b64_tr_b16 v[126:127], v238 offset:29184\n"
    "s_waitcnt lgkmcnt(4)\n"
    "v_mfma_f32_32x32x16_bf16 v[0:15], v[64:67], v[112:115], 0\n"
    "v_and_b32 v144, v244, v144\n"
    "v_and_b32 v148, v244, v148\n"
    "ds_read_b64_tr_b16 v[128:129], v237 offset:1024\n"
    "ds_read_b64_tr_b16 v[130:131], v237 offset:1536\n"
    "v_mfma_f32_32x32x16_bf16 v[16:31], v[64:67], v[116:119], 0\n"
    "ds_read_b64_tr_b16 v[132:133], v237 offset:29696\n"
    "ds_read_b64_tr_b16 v[134:135], v237 offset:30208\n"
    "buffer_load_dwordx4 v236, %[ws], s40 offen offset:2048 sc1 lds\n"
    "v_mfma_f32_16x16x32_bf16 v[152:155], v[144:147], v[144:147], 0\n"
    "s_waitcnt lgkmcnt(4)\n"
    "v_mfma_f32_32x32x16_bf16 v[32:47], v[64:67], v[120:123], 0\n"
    "ds_read_b64_tr_b16 v[136:137], v238 offset:1024\n"
    "ds_read_b64_tr_b16 v[138:139], v238 offset:1536\n"
    "v_mfma_f32_32x32x16_bf16 v[48:63], v[64:67], v[124:127], 0\n"
    "ds_read_b64_tr_b16 v[140:141], v238 offset:29696\n"
    "ds_read_b64_tr_b16 v[142:143], v238 offset:30208\n"
    "s_add_u32 s41, s41, 4096\n"
    "buffer_load_dwordx4 v[64:67], v236, %[ws], s41 offen offset:0\n"
    "v_mfma_f32_16x16x32_bf16 v[156:159], v[148:151], v[148:151], 0\n"
    "s_waitcnt lgkmcnt(4)\n"
    "v_mfma_f32_32x32x16_bf16 v[0:15], v[68:71], v[128:131], v[0:15]\n"
    "s_waitcnt vmcnt(4)\n"
    "s_barrier\n"
    "ds_read_b64_tr_b16 v[112:113], v237 offset:2048\n"
    "ds_read_b64_tr_b16 v[114:115], v237 offset:2560\n"
    "v_mfma_f32_32x32x16_bf16 v[16:31], v[68:71], v[132:135], v[16:31]\n"
    "ds_read_b64_tr_b16 v[116:117], v237 offset:30720\n"
    "ds_read_b64_tr_b16 v[118:119], v237 offset:31232\n"
    "ds_read_b64_tr_b16 v[144:145], v239 offset:2048\n"
    "buffer_load_dwordx4 v236, %[ws], s40 offen offset:3072 sc1 lds\n"
    "s_waitcnt lgkmcnt(5)\n"
    "v_mfma_f32_32x32x16_bf16 v[32:47], v[68:71], v[136:139], v[32:47]\n"
    "ds_read_b64_tr_b16 v[120:121], v238 offset:2048\n"
    "ds_read_b64_tr_b16 v[122:123], v238 offset:2560\n"
    "ds_read_b64_tr_b16 v[146:147], v239 offset:2560\n"
    "v_mfma_f32_32x32x16_bf16 v[48:63], v[68:71], v[140:143], v[48:63]\n"
    "ds_read_b64_tr_b16 v[124:125], v238 offset:30720\n"
    "ds_read_b64_tr_b16 v[126:127], v238 offset:31232\n"
    "ds_read_b64_tr_b16 v[148:149], v239 offset:2080\n"
    "buffer_load_dwordx4 v[68:71], v236, %[ws], s41 offen offset:1024\n"
    "s_waitcnt lgkmcnt(7)\n"
    "v_mfma_f32_32x32x16_bf16 v[0:15], v[72:75], v[112:115], v[0:15]\n"
    "ds_read_b64_tr_b16 v[128:129], v237 offset:3072\n"
    "ds_read_b64_tr_b16 v[130:131], v237 offset:3584\n"
    "ds_read_b64_tr_b16 v[150:151], v239 offset:2592\n"
    "s_waitcnt lgkmcnt(6)\n"
    "v_mfma_f32_16x16x32_bf16 v[152:155], v[144:147], v[144:147], v[152:155]\n"
    "v_mfma_f32_32x32x16_bf16 v[16:31], v[72:75], v[116:119], v[16:31]\n"
    "ds_read_b64_tr_b16 v[132:133], v237 offset:31744\n"
    "ds_read_b64_tr_b16 v[134:135], v237 offset:32256\n"
    "s_add_u32 m0, m0, 4096\n"
    "s_add_u32 s40, s40, 4096\n"
    "buffer_load_dwordx4 v236, %[ws], s40 offen offset:0 sc1 lds\n"
    "s_waitcnt lgkmcnt(6)\n"
    "v_mfma_f32_32x32x16_bf16 v[32:47], v[72:75], v[120:123], v[32:47]\n"
    "ds_read_b64_tr_b16 v[136:137], v238 offset:3072\n"
    "ds_read_b64_tr_b16 v[138:139], v238 offset:3584\n"
    "s_waitcnt lgkmcnt(4)\n"
    "v_mfma_f32_16x16x32_bf16 v[156:159], v[148:151], v[148:151], v[156:159]\n"
    "v_mfma_f32_32x32x16_bf16 v[48:63], v[72:75], v[124:127], v[48:63]\n"
    "ds_read_b64_tr_b16 v[140:141], v238 offset:31744\n"
    "ds_read_b64_tr_b16 v[142:143], v238 offset:32256\n"
    "buffer_load_dwordx4 v[72:75], v236, %[ws], s41 offen offset:2048\n"
    "s_waitcnt lgkmcnt(4)\n"
    "v_mfma_f32_32x32x16_bf16 v[0:15], v[76:79], v[128:131], v[0:15]\n"
    "s_waitcnt vmcnt(6)\n"
    "s_barrier\n"
    "ds_read_b64_tr_b16 v[112:113], v237 offset:4096\n"
    "ds_read_b64_tr_b16 v[114:115], v237 offset:4608\n"
    "v_mfma_f32_32x32x16_bf16 v[16:31], v[76:79], v[132:135], v[16:31]\n"
    "ds_read_b64_tr_b16 v[116:117], v237 offset:32768\n"
    "ds_read_b64_tr_b16 v[118:119], v237 offset:33280\n"
    "ds_read_b64_tr_b16 v[144:145], v239 offset:4096\n"
    "buffer_load_dwordx4 v236, %[ws], s40 offen offset:1024 sc1 lds\n"
    "s_waitcnt lgkmcnt(5)\n"
    "v_mfma_f32_32x32x16_bf16 v[32:47], v[76:79], v[136:139], v[32:47]\n"
    "ds_read_b64_tr_b16 v[120:121], v238 offset:4096\n"
    "ds_read_b64_tr_b16 v[122:123], v238 offset:4608\n"
    "ds_read_b64_tr_b16 v[146:147], v239 offset:4608\n"
    "v_mfma_f32_32x32x16_bf16 v[48:63], v[76:79], v[140:143], v[48:63]\n"
    "ds_read_b64_tr_b16 v[124:125], v238 offset:32768\n"
    "ds_read_b64_tr_b16 v[126:127], v238 offset:33280\n"
    "ds_read_b64_tr_b16 v[148:149], v239 offset:4128\n"
    "buffer_load_dwordx4 v[76:79], v236, %[ws], s41 offen offset:3072\n"
    "s_waitcnt lgkmcnt(7)\n"
    "v_mfma_f32_32x32x16_bf16 v[0:15], v[80:83], v[112:115], v[0:15]\n"
    "ds_read_b64_tr_b16 v[128:129], v237 offset:5120\n"
    "ds_read_b64_tr_b16 v[130:131], v237 offset:5632\n"
    "ds_read_b64_tr_b16 v[150:151], v239 offset:4640\n"
    "s_waitcnt lgkmcnt(6)\n"
    "v_mfma_f32_16x16x32_bf16 v[152:155], v[144:147], v[144:147], v[152:155]\n"
    "v_mfma_f32_32x32x16_bf16 v[16:31], v[80:83], v[116:119], v[16:31]\n"
    "ds_read_b64_tr_b16 v[132:133], v237 offset:33792\n"
    "ds_read_b64_tr_b16 v[134:135], v237 offset:34304\n"
    "buffer_load_dwordx4 v236, %[ws], s40 offen offset:2048 sc1 lds\n"
    "s_waitcnt lgkmcnt(6)\n"
    "v_mfma_f32_32x32x16_bf16 v[32:47], v[80:83], v[120:123], v[32:47]\n"
    "ds_read_b64_tr_b16 v[136:137], v238 offset:5120\n"
    "ds_read_b64_tr_b16 v[138:139], v238 offset:5632\n"
    "s_waitcnt lgkmcnt(4)\n"
    "v_mfma_f32_16x16x32_bf16 v[156:159], v[148:151], v[148:151], v[156:159]\n"
    "v_mfma_f32_32x32x16_bf16 v[48:63], v[80:83], v[124:127], v[48:63]\n"
    "ds_read_b64_tr_b16 v[140:141], v238 offset:33792\n"
    "ds_read_b64_tr_b16 v[142:143], v238 offset:34304\n"
    "s_add_u32 s41, s41, 4096\n"
    "buffer_load_dwordx4 v[80:83], v236, %[ws], s41 offen offset:0\n"
    "s_waitcnt lgkmcnt(4)\n"
    "v_mfma_f32_32x32x16_bf16 v[0:15], v[84:87], v[128:131], v[0:15]\n"
    "s_waitcnt vmcnt(7)\n"
    "s_barrier\n"
    "ds_read_b64_tr_b16 v[112:113], v237 offset:6144\n"
    "ds_read_b64_tr_b16 v[114:115], v237 offset:6656\n"
    "v_mfma_f32_32x32x16_bf16 v[16:31], v[84:87], v[132:135], v[16:31]\n"
    "ds_read_b64_tr_b16 v[116:117], v237 offset:34816\n"
    "ds_read_b64_tr_b16 v[118:119], v237 offset:35328\n"
    "ds_read_b64_tr_b16 v[144:145], v239 offset:6144\n"
    "buffer_load_dwordx4 v236, %[ws], s40 offen offset:3072 sc1 lds\n"
    "s_waitcnt lgkmcnt(5)\n"
    "v_mfma_f32_32x32x16_bf16 v[32:47], v[84:87], v[136:139], v[32:47]\n"
    "ds_read_b64_tr_b16 v[120:121], v238 offset:6144\n"
    "ds_read_b64_tr_b16 v[122:123], v238 offset:6656\n"
    "ds_read_b64_tr_b16 v[146:147], v239 offset:6656\n"
    "v_mfma_f32_32x32x16_bf16 v[48:63], v[84:87], v[140:143], v[48:63]\n"
    "ds_read_b64_tr_b16 v[124:125], v238 offset:34816\n"
    "ds_read_b64_tr_b16 v[126:127], v238 offset:35328\n"
    "ds_read_b64_tr_b16 v[148:149], v239 offset:6176\n"
    "buffer_load_dwordx4 v[84:87], v236, %[ws], s41 offen offset:1024\n"
    "s_waitcnt lgkmcnt(7)\n"
    "v_mfma_f32_32x32x16_bf16 v[0:15], v[88:91], v[112:115], v[0:15]\n"
    "ds_read_b64_tr_b16 v[128:129], v237 offset:7168\n"
    "ds_read_b64_tr_b16 v[130:131], v237 offset:7680\n"
    "ds_read_b64_tr_b16 v[150:151], v239 offset:6688\n"
    "s_waitcnt lgkmcnt(6)\n"
    "v_mfma_f32_16x16x32_bf16 v[152:155], v[144:147], v[144:147], v[152:155]\n"
    "v_mfma_f32_32x32x16_bf16 v[16:31], v[88:91], v[116:119], v[16:31]\n"
    "ds_read_b64_tr_b16 v[132:133], v237 offset:35840\n"
    "ds_read_b64_tr_b16 v[134:135], v237 offset:36352\n"
    "s_add_u32 m0, m0, 4096\n"
    "s_add_u32 s40, s40, 4096\n"
    "buffer_load_dwordx4 v236, %[ws], s40 offen offset:0 sc1 lds\n"
    "s_waitcnt lgkmcnt(6)\n"
    "v_mfma_f32_32x32x16_bf16 v[32:47], v[88:91], v[120:123], v[32:47]\n"
    "ds_read_b64_tr_b16 v[136:137], v238 offset:7168\n"
    "ds_read_b64_tr_b16 v[138:139], v238 offset:7680\n"
    "s_waitcnt lgkmcnt(4)\n"
    "v_mfma_f32_16x16x32_bf16 v[156:159], v[148:151], v[148:151], v[156:159]\n"
    "v_mfma_f32_32x32x16_bf16 v[48:63], v[88:91], v[124:127], v[48:63]\n"
    "ds_read_b64_tr_b16 v[140:141], v238 offset:35840\n"
    "ds_read_b64_tr_b16 v[142:143], v238 offset:36352\n"
    "buffer_load_dwordx4 v[88:91], v236, %[ws], s41 offen offset:2048\n"
    "s_waitcnt lgkmcnt(4)\n"
    "v_mfma_f32_32x32x16_bf16 v[0:15], v[92:95], v[128:131], v[0:15]\n"
    "s_waitcnt vmcnt(7)\n"
    "s_barrier\n"
    "ds_read_b64_tr_b16 v[112:113], v237 offset:8192\n"
    "ds_read_b64_tr_b16 v[114:115], v237 offset:8704\n"
    "v_mfma_f32_32x32x16_bf16 v[16:31], v[92:95], v[132:135], v[16:31]\n"
    "ds_read_b64_tr_b16 v[116:117], v237 offset:36864\n"
    "ds_read_b64_tr_b16 v[118:119], v237 offset:37376\n"
    "ds_read_b64_tr_b16 v[144:145], v239 offset:8192\n"
    "buffer_load_dwordx4 v236, %[ws], s40 offen offset:1024 sc1 lds\n"
    "s_waitcnt lgkmcnt(5)\n"
    "v_mfma_f32_32x32x16_bf16 v[32:47], v[92:95], v[136:139], v[32:47]\n"
    "ds_read_b64_tr_b16 v[120:121], v238 offset:8192\n"
    "ds_read_b64_tr_b16 v[122:123], v238 offset:8704\n"
    "ds_read_b64_tr_b16 v[146:147], v239 offset:8704\n"
    "v_mfma_f32_32x32x16_bf16 v[48:63], v[92:95], v[140:143], v[48:63]\n"
    "ds_read_b64_tr_b16 v[124:125], v238 offset:36864\n"
    "ds_read_b64_tr_b16 v[126:127], v238 offset:37376\n"
    "ds_read_b64_tr_b16 v[148:149], v239 offset:8224\n"
    "buffer_load_dwordx4 v[92:95], v236, %[ws], s41 offen offset:3072\n"
    "s_waitcnt lgkmcnt(7)\n"
    "v_mfma_f32_32x32x16_bf16 v[0:15], v[96:99], v[112:115], v[0:15]\n"
    "ds_read_b64_tr_b16 v[128:129], v237 offset:9216\n"
    "ds_read_b64_tr_b16 v[130:131], v237 offset:9728\n"
    "ds_read_b64_tr_b16 v[150:151], v239 offset:8736\n"
    "s_waitcnt lgkmcnt(6)\n"
    "v_mfma_f32_16x16x32_bf16 v[152:155], v[144:147], v[144:147], v[152:155]\n"
    "v_mfma_f32_32x32x16_bf16 v[16:31], v[96:99], v[116:119], v[16:31]\n"
    "ds_read_b64_tr_b16 v[132:133], v237 offset:37888\n"
    "ds_read_b64_tr_b16 v[134:135], v237 offset:38400\n"
    "buffer_load_dwordx4 v236, %[ws], s40 offen offset:2048 sc1 lds\n"
    "s_waitcnt lgkmcnt(6)\n"
    "v_mfma_f32_32x32x16_bf16 v[32:47], v[96:99], v[120:123], v[32:47]\n"
    "ds_read_b64_tr_b16 v[136:137], v238 offset:9216\n"
    "ds_read_b64_tr_b16 v[138:139], v238 offset:9728\n"
    "s_waitcnt lgkmcnt(4)\n"
    "v_mfma_f32_16x16x32_bf16 v[156:159], v[148:151], v[148:151], v[156:159]\n"
    "v_mfma_f32_32x32x16_bf16 v[48:63], v[96:99], v[124:127], v[48:63]\n"
    "ds_read_b64_tr_b16 v[140:141], v238 offset:37888\n"
    "ds_read_b64_tr_b16 v[142:143], v238 offset:38400\n"
    "s_add_u32 s41, s41, 4096\n"
    "buffer_load_dwordx4 v[96:99], v236, %[ws], s41 offen offset:0\n"
    "s_waitcnt lgkmcnt(4)\n"
    "v_mfma_f32_32x32x16_bf16 v[0:15], v[100:103], v[128:131], v[0:15]\n"
    "s_waitcnt vmcnt(7)\n"
    "s_barrier\n"
    "ds_read_b64_tr_b16 v[112:113], v237 offset:10240\n"
    "ds_read_b64_tr_b16 v[114:115], v237 offset:10752\n"
    "v_mfma_f32_32x32x16_bf16 v[16:31], v[100:103], v[132:135], v[16:31]\n"
    "ds_read_b64_tr_b16 v[116:117], v237 offset:38912\n"
    "ds_read_b64_tr_b16 v[118:119], v237 offset:39424\n"
    "ds_read_b64_tr_b16 v[144:145], v239 offset:10240\n"
    "buffer_load_dwordx4 v236, %[ws], s40 offen offset:3072 sc1 lds\n"
    "s_waitcnt lgkmcnt(5)\n"
    "v_mfma_f32_32x32x16_bf16 v[32:47], v[100:103], v[136:139], v[32:47]\n"
    "ds_read_b64_tr_b16 v[120:121], v238 offset:10240\n"
    "ds_read_b64_tr_b16 v[122:123], v238 offset:10752\n"
    "ds_read_b64_tr_b16 v[146:147], v239 offset:10752\n"
    "v_mfma_f32_32x32x16_bf16 v[48:63], v[100:103], v[140:143], v[48:63]\n"
    "ds_read_b64_tr_b16 v[124:125], v238 offset:38912\n"
    "ds_read_b64_tr_b16 v[126:127], v238 offset:39424\n"
    "ds_read_b64_tr_b16 v[148:149], v239 offset:10272\n"
    "buffer_load_dwordx4 v[100:103], v236, %[ws], s41 offen offset:1024\n"
    "s_waitcnt lgkmcnt(7)\n"
    "v_mfma_f32_32x32x16_bf16 v[0:15], v[104:107], v[112:115], v[0:15]\n"
    "ds_read_b64_tr_b16 v[128:129], v237 offset:11264\n"
    "ds_read_b64_tr_b16 v[130:131], v237 offset:11776\n"
    "ds_read_b64_tr_b16 v[150:151], v239 offset:10784\n"
    "s_waitcnt lgkmcnt(6)\n"
    "v_mfma_f32_16x16x32_bf16 v[152:155], v[144:147], v[144:147], v[152:155]\n"
    "v_mfma_f32_32x32x16_bf16 v[16:31], v[104:107], v[116:119], v[16:31]\n"
    "ds_read_b64_tr_b16 v[132:133], v237 offset:39936\n"
    "ds_read_b64_tr_b16 v[134:135], v237 offset:40448\n"
    "s_add_u32 m0, m0, 4096\n"
    "s_add_u32 s40, s40, 4096\n"
    "buffer_load_dwordx4 v236, %[ws], s40 offen offset:0 sc1 lds\n"
    "s_waitcnt lgkmcnt(6)\n"
    "v_mfma_f32_32x32x16_bf16 v[32:47], v[104:107], v[120:123], v[32:47]\n"
    "ds_read_b64_tr_b16 v[136:137], v238 offset:11264\n"
    "ds_read_b64_tr_b16 v[138:139], v238 offset:11776\n"
    "s_waitcnt lgkmcnt(4)\n"
    "v_mfma_f32_16x16x32_bf16 v[156:159], v[148:151], v[148:151], v[156:159]\n"
    "v_mfma_f32_32x32x16_bf16 v[48:63], v[104:107], v[124:127], v[48:63]\n"
    "ds_read_b64_tr_b16 v[140:141], v238 offset:39936\n"
    "ds_read_b64_tr_b16 v[142:143], v238 offset:40448\n"
    "buffer_load_dwordx4 v[104:107], v236, %[ws], s41 offen offset:2048\n"
    "s_waitcnt lgkmcnt(4)\n"
    "v_mfma_f32_32x32x16_bf16 v[0:15], v[108:111], v[128:131], v[0:15]\n"
    "s_waitcnt vmcnt(7)\n"
    "s_barrier\n"
    "ds_read_b64_tr_b16 v[112:113], v237 offset:12288\n"
    "ds_read_b64_tr_b16 v[114:115], v237 offset:12800\n"
    "v_mfma_f32_32x32x16_bf16 v[16:31], v[108:111], v[132:135], v[16:31]\n"
    "ds_read_b64_tr_b16 v[116:117], v237 offset:40960\n"
    "ds_read_b64_tr_b16 v[118:119], v237 offset:41472\n"
    "ds_read_b64_tr_b16 v[144:145], v239 offset:12288\n"
    "buffer_load_dwordx4 v236, %[ws], s40 offen offset:1024 sc1 lds\n"
    "s_waitcnt lgkmcnt(5)\n"
    "v_mfma_f32_32x32x16_bf16 v[32:47], v[108:111], v[136:139], v[32:47]\n"
    "ds_read_b64_tr_b16 v[120:121], v238 offset:12288\n"
    "ds_read_b64_tr_b16 v[122:123], v238 offset:12800\n"
    "ds_read_b64_tr_b16 v[146:147], v239 offset:12800\n"
    "v_mfma_f32_32x32x16_bf16 v[48:63], v[108:111], v[140:143], v[48:63]\n"
    "ds_read_b64_tr_b16 v[124:125], v238 offset:40960\n"
    "ds_read_b64_tr_b16 v[126:127], v238 offset:41472\n"
    "ds_read_b64_tr_b16 v[148:149], v239 offset:12320\n"
    "buffer_load_dwordx4 v[108:111], v236, %[ws], s41 offen offset:3072\n"
    "s_waitcnt lgkmcnt(7)\n"
    "v_mfma_f32_32x32x16_bf16 v[0:15], v[64:67], v[112:115], v[0:15]\n"
    "ds_read_b64_tr_b16 v[128:129], v237 offset:13312\n"
    "ds_read_b64_tr_b16 v[130:131], v237 offset:13824\n"
    "ds_read_b64_tr_b16 v[150:151], v239 offset:12832\n"
    "s_waitcnt lgkmcnt(6)\n"
    "v_mfma_f32_16x16x32_bf16 v[152:155], v[144:147], v[144:147], v[152:155]\n"
    "v_mfma_f32_32x32x16_bf16 v[16:31], v[64:67], v[116:119], v[16:31]\n"
    "ds_read_b64_tr_b16 v[132:133], v237 offset:41984\n"
    "ds_read_b64_tr_b16 v[134:135], v237 offset:42496\n"
    "buffer_load_dwordx4 v236, %[ws], s40 offen offset:2048 sc1 lds\n"
    "s_waitcnt lgkmcnt(6)\n"
    "v_mfma_f32_32x32x16_bf16 v[32:47], v[64:67], v[120:123], v[32:47]\n"
    "ds_read_b64_tr_b16 v[136:137], v238 offset:13312\n"
    "ds_read_b64_tr_b16 v[138:139], v238 offset:13824\n"
    "s_waitcnt lgkmcnt(4)\n"
    "v_mfma_f32_16x16x32_bf16 v[156:159], v[148:151], v[148:151], v[156:159]\n"
    "v_mfma_f32_32x32x16_bf16 v[48:63], v[64:67], v[124:127], v[48:63]\n"
    "ds_read_b64_tr_b16 v[140:141], v238 offset:41984\n"
    "ds_read_b64_tr_b16 v[142:143], v238 offset:42496\n"
    "s_add_u32 s41, s41, 4096\n"
    "buffer_load_dwordx4 v[64:67], v236, %[ws], s41 offen offset:0\n"
    "s_waitcnt lgkmcnt(4)\n"
    "v_mfma_f32_32x32x16_bf16 v[0:15], v[68:71], v[128:131], v[0:15]\n"
    "s_waitcnt vmcnt(7)\n"
    "s_barrier\n"
    "ds_read_b64_tr_b16 v[112:113], v237 offset:14336\n"
    "ds_read_b64_tr_b16 v[114:115], v237 offset:14848\n"
    "v_mfma_f32_32x32x16_bf16 v[16:31], v[68:71], v[132:135], v[16:31]\n"
    "ds_read_b64_tr_b16 v[116:117], v237 offset:43008\n"
    "ds_read_b64_tr_b16 v[118:119], v237 offset:43520\n"
    "ds_read_b64_tr_b16 v[144:145], v239 offset:14336\n"
    "buffer_load_dwordx4 v236, %[ws], s40 offen offset:3072 sc1 lds\n"
    "s_waitcnt lgkmcnt(5)\n"
    "v_mfma_f32_32x32x16_bf16 v[32:47], v[68:71], v[136:139], v[32:47]\n"
    "ds_read_b64_tr_b16 v[120:121], v238 offset:14336\n"
    "ds_read_b64_tr_b16 v[122:123], v238 offset:14848\n"
    "ds_read_b64_tr_b16 v[146:147], v239 offset:14848\n"
    "v_mfma_f32_32x32x16_bf16 v[48:63], v[68:71], v[140:143], v[48:63]\n"
    "ds_read_b64_tr_b16 v[124:125], v238 offset:43008\n"
    "ds_read_b64_tr_b16 v[126:127], v238 offset:43520\n"
    "ds_read_b64_tr_b16 v[148:149], v239 offset:14368\n"
    "buffer_load_dwordx4 v[68:71], v236, %[ws], s41 offen offset:1024\n"
    "s_waitcnt lgkmcnt(7)\n"
    "v_mfma_f32_32x32x16_bf16 v[0:15], v[72:75], v[112:115], v[0:15]\n"
    "ds_read_b64_tr_b16 v[128:129], v237 offset:15360\n"
    "ds_read_b64_tr_b16 v[130:131], v237 offset:15872\n"
    "ds_read_b64_tr_b16 v[150:151], v239 offset:14880\n"
    "s_waitcnt lgkmcnt(6)\n"
    "v_mfma_f32_16x16x32_bf16 v[152:155], v[144:147], v[144:147], v[152:155]\n"
    "v_mfma_f32_32x32x16_bf16 v[16:31], v[72:75], v[116:119], v[16:31]\n"
    "ds_read_b64_tr_b16 v[132:133], v237 offset:44032\n"
    "ds_read_b64_tr_b16 v[134:135], v237 offset:44544\n"
    "s_add_u32 m0, m0, 4096\n"
    "s_add_u32 s40, s40, 4096\n"
    "buffer_load_dwordx4 v236, %[ws], s40 offen offset:0 sc1 lds\n"
    "s_waitcnt lgkmcnt(6)\n"
    "v_mfma_f32_32x32x16_bf16 v[32:47], v[72:75], v[120:123], v[32:47]\n"
    "ds_read_b64_tr_b16 v[136:137], v238 offset:15360\n"
    "ds_read_b64_tr_b16 v[138:139], v238 offset:15872\n"
    "s_waitcnt lgkmcnt(4)\n"
    "v_mfma_f32_16x16x32_bf16 v[156:159], v[148:151], v[148:151], v[156:159]\n"
    "v_mfma_f32_32x32x16_bf16 v[48:63], v[72:75], v[124:127], v[48:63]\n"
    "ds_read_b64_tr_b16 v[140:141], v238 offset:44032\n"
    "ds_read_b64_tr_b16 v[142:143], v238 offset:44544\n"
    "buffer_load_dwordx4 v[72:75], v236, %[ws], s41 offen offset:2048\n"
    "s_waitcnt lgkmcnt(4)\n"
    "v_mfma_f32_32x32x16_bf16 v[0:15], v[76:79], v[128:131], v[0:15]\n"
    "s_waitcnt vmcnt(7)\n"
    "s_barrier\n"
    "ds_read_b64_tr_b16 v[112:113], v237 offset:16384\n"
    "ds_read_b64_tr_b16 v[114:115], v237 offset:16896\n"
    "v_mfma_f32_32x32x16_bf16 v[16:31], v[76:79], v[132:135], v[16:31]\n"
    "ds_read_b64_tr_b16 v[116:117], v237 offset:45056\n"
    "ds_read_b64_tr_b16 v[118:119], v237 offset:45568\n"
    "ds_read_b64_tr_b16 v[144:145], v239 offset:16384\n"
    "buffer_load_dwordx4 v236, %[ws], s40 offen offset:1024 sc1 lds\n"
    "s_waitcnt lgkmcnt(5)\n"
    "v_mfma_f32_32x32x16_bf16 v[32:47], v[76:79], v[136:139], v[32:47]\n"
    "ds_read_b64_tr_b16 v[120:121], v238 offset:16384\n"
    "ds_read_b64_tr_b16 v[122:123], v238 offset:16896\n"
    "ds_read_b64_tr_b16 v[146:147], v239 offset:16896\n"
    "v_mfma_f32_32x32x16_bf16 v[48:63], v[76:79], v[140:143], v[48:63]\n"
    "ds_read_b64_tr_b16 v[124:125], v238 offset:45056\n"
    "ds_read_b64_tr_b16 v[126:127], v238 offset:45568\n"
    "ds_read_b64_tr_b16 v[148:149], v239 offset:16416\n"
    "buffer_load_dwordx4 v[76:79], v236, %[ws], s41 offen offset:3072\n"
    "s_waitcnt lgkmcnt(7)\n"
    "v_mfma_f32_32x32x16_bf16 v[0:15], v[80:83], v[112:115], v[0:15]\n"
    "ds_read_b64_tr_b16 v[128:129], v237 offset:17408\n"
    "ds_read_b64_tr_b16 v[130:131], v237 offset:17920\n"
    "ds_read_b64_tr_b16 v[150:151], v239 offset:16928\n"
    "s_waitcnt lgkmcnt(6)\n"
    "v_mfma_f32_16x16x32_bf16 v[152:155], v[144:147], v[144:147], v[152:155]\n"
    "v_mfma_f32_32x32x16_bf16 v[16:31], v[80:83], v[116:119], v[16:31]\n"
    "ds_read_b64_tr_b16 v[132:133], v237 offset:46080\n"
    "ds_read_b64_tr_b16 v[134:135], v237 offset:46592\n"
    "buffer_load_dwordx4 v236, %[ws], s40 offen offset:2048 sc1 lds\n"
    "s_waitcnt lgkmcnt(6)\n"
    "v_mfma_f32_32x32x16_bf16 v[32:47], v[80:83], v[120:123], v[32:47]\n"
    "ds_read_b64_tr_b16 v[136:137], v238 offset:17408\n"
    "ds_read_b64_tr_b16 v[138:139], v238 offset:17920\n"
    "s_waitcnt lgkmcnt(4)\n"
    "v_mfma_f32_16x16x32_bf16 v[156:159], v[148:151], v[148:151], v[156:159]\n"
    "v_mfma_f32_32x32x16_bf16 v[48:63], v[80:83], v[124:127], v[48:63]\n"
    "ds_read_b64_tr_b16 v[140:141], v238 offset:46080\n"
    "ds_read_b64_tr_b16 v[142:143], v238 offset:46592\n"
    "buffer_load_dwordx4 v236, %[ws], s40 offen offset:3072 sc1 lds\n"
    "s_waitcnt lgkmcnt(4)\n"
    "v_mfma_f32_32x32x16_bf16 v[0:15], v[84:87], v[128:131], v[0:15]\n"
    "s_waitcnt vmcnt(7)\n"
    "s_barrier\n"
    "ds_read_b64_tr_b16 v[112:113], v237 offset:18432\n"
    "ds_read_b64_tr_b16 v[114:115], v237 offset:18944\n"
    "v_mfma_f32_32x32x16_bf16 v[16:31], v[84:87], v[132:135], v[16:31]\n"
    "ds_read_b64_tr_b16 v[116:117], v237 offset:47104\n"
    "ds_read_b64_tr_b16 v[118:119], v237 offset:47616\n"
    "ds_read_b64_tr_b16 v[144:145], v239 offset:18432\n"
    "s_add_u32 m0, m0, 4096\n"
    "s_add_u32 s40, s40, 4096\n"
    "buffer_load_dwordx4 v236, %[ws], s40 offen offset:0 sc1 lds\n"
    "s_waitcnt lgkmcnt(5)\n"
    "v_mfma_f32_32x32x16_bf16 v[32:47], v[84:87], v[136:139], v[32:47]\n"
    "ds_read_b64_tr_b16 v[120:121], v238 offset:18432\n"
    "ds_read_b64_tr_b16 v[122:123], v238 offset:18944\n"
    "ds_read_b64_tr_b16 v[146:147], v239 offset:18944\n"
    "v_mfma_f32_32x32x16_bf16 v[48:63], v[84:87], v[140:143], v[48:63]\n"
    "ds_read_b64_tr_b16 v[124:125], v238 offset:47104\n"
    "ds_read_b64_tr_b16 v[126:127], v238 offset:47616\n"
    "ds_read_b64_tr_b16 v[148:149], v239 offset:18464\n"
    "buffer_load_dwordx4 v236, %[ws], s40 offen offset:1024 sc1 lds\n"
    "s_waitcnt lgkmcnt(7)\n"
    "v_mfma_f32_32x32x16_bf16 v[0:15], v[88:91], v[112:115], v[0:15]\n"
    "ds_read_b64_tr_b16 v[128:129], v237 offset:19456\n"
    "ds_read_b64_tr_b16 v[130:131], v237 offset:19968\n"
    "ds_read_b64_tr_b16 v[150:151], v239 offset:18976\n"
    "s_waitcnt lgkmcnt(6)\n"
    "v_mfma_f32_16x16x32_bf16 v[152:155], v[144:147], v[144:147], v[152:155]\n"
    "v_mfma_f32_32x32x16_bf16 v[16:31], v[88:91], v[116:119], v[16:31]\n"
    "ds_read_b64_tr_b16 v[132:133], v237 offset:48128\n"
    "ds_read_b64_tr_b16 v[134:135], v237 offset:48640\n"
    "buffer_load_dwordx4 v236, %[ws], s40 offen offset:2048 sc1 lds\n"
    "s_waitcnt lgkmcnt(6)\n"
    "v_mfma_f32_32x32x16_bf16 v[32:47], v[88:91], v[120:123], v[32:47]\n"
    "ds_read_b64_tr_b16 v[136:137], v238 offset:19456\n"
    "ds_read_b64_tr_b16 v[138:139], v238 offset:19968\n"
    "s_waitcnt lgkmcnt(4)\n"
    "v_mfma_f32_16x16x32_bf16 v[156:159], v[148:151], v[148:151], v[156:159]\n"
    "v_mfma_f32_32x32x16_bf16 v[48:63], v[88:91], v[124:127], v[48:63]\n"
    "ds_read_b64_tr_b16 v[140:141], v238 offset:48128\n"
    "ds_read_b64_tr_b16 v[142:143], v238 offset:48640\n"
    "buffer_load_dwordx4 v236, %[ws], s40 offen offset:3072 sc1 lds\n"
    "s_waitcnt lgkmcnt(4)\n"
    "v_mfma_f32_32x32x16_bf16 v[0:15], v[92:95], v[128:131], v[0:15]\n"
    "s_waitcnt vmcnt(7)\n"
    "s_barrier\n"
    "ds_read_b64_tr_b16 v[112:113], v237 offset:20480\n"
    "ds_read_b64_tr_b16 v[114:115], v237 offset:20992\n"
    "v_mfma_f32_32x32x16_bf16 v[16:31], v[92:95], v[132:135], v[16:31]\n"
    "ds_read_b64_tr_b16 v[116:117], v237 offset:49152\n"
    "ds_read_b64_tr_b16 v[118:119], v237 offset:49664\n"
    "ds_read_b64_tr_b16 v[144:145], v239 offset:20480\n"
    "buffer_load_dwordx4 a[12:15], v236, %[ws], s58 offen offset:3072\n"
    "s_waitcnt lgkmcnt(5)\n"
    "v_mfma_f32_32x32x16_bf16 v[32:47], v[92:95], v[136:139], v[32:47]\n"
    "ds_read_b64_tr_b16 v[120:121], v238 offset:20480\n"
    "ds_read_b64_tr_b16 v[122:123], v238 offset:20992\n"
    "ds_read_b64_tr_b16 v[146:147], v239 offset:20992\n"
    "v_mfma_f32_32x32x16_bf16 v[48:63], v[92:95], v[140:143], v[48:63]\n"
    "ds_read_b64_tr_b16 v[124:125], v238 offset:49152\n"
    "ds_read_b64_tr_b16 v[126:127], v238 offset:49664\n"
    "ds_read_b64_tr_b16 v[148:149], v239 offset:20512\n"
    "buffer_load_dwordx4 a[124:127], v236, %[ws], s59 offen offset:3072\n"
    "s_waitcnt lgkmcnt(7)\n"
    "v_mfma_f32_32x32x16_bf16 v[0:15], v[96:99], v[112:115], v[0:15]\n"
    "ds_read_b64_tr_b16 v[128:129], v237 offset:21504\n"
    "ds_read_b64_tr_b16 v[130:131], v237 offset:22016\n"
    "ds_read_b64_tr_b16 v[150:151], v239 offset:21024\n"
    "s_waitcnt lgkmcnt(6)\n"
    "v_mfma_f32_16x16x32_bf16 v[152:155], v[144:147], v[144:147], v[152:155]\n"
    "v_mfma_f32_32x32x16_bf16 v[16:31], v[96:99], v[116:119], v[16:31]\n"
    "ds_read_b64_tr_b16 v[132:133], v237 offset:50176\n"
    "ds_read_b64_tr_b16 v[134:135], v237 offset:50688\n"
    "s_add_u32 s58, s58, 4096\n"
    "buffer_load_dwordx4 a[16:19], v236, %[ws], s58 offen offset:0\n"
    "s_waitcnt lgkmcnt(6)\n"
    "v_mfma_f32_32x32x16_bf16 v[32:47], v[96:99], v[120:123], v[32:47]\n"
    "ds_read_b64_tr_b16 v[136:137], v238 offset:21504\n"
    "ds_read_b64_tr_b16 v[138:139], v238 offset:22016\n"
    "s_waitcnt lgkmcnt(4)\n"
    "v_mfma_f32_16x16x32_bf16 v[156:159], v[148:151], v[148:151], v[156:159]\n"
    "v_mfma_f32_32x32x16_bf16 v[48:63], v[96:99], v[124:127], v[48:63]\n"
    "ds_read_b64_tr_b16 v[140:141], v238 offset:50176\n"
    "ds_read_b64_tr_b16 v[142:143], v238 offset:50688\n"
    "s_add_u32 s59, s59, 4096\n"
    "buffer_load_dwordx4 a[128:131], v236, %[ws], s59 offen offset:0\n"
    "s_waitcnt lgkmcnt(4)\n"
    "v_mfma_f32_32x32x16_bf16 v[0:15], v[100:103], v[128:131], v[0:15]\n"
    "s_waitcnt vmcnt(8)\n"
    "s_barrier\n"
    "ds_read_b64_tr_b16 v[112:113], v237 offset:22528\n"
    "ds_read_b64_tr_b16 v[114:115], v237 offset:23040\n"
    "v_mfma_f32_32x32x16_bf16 v[16:31], v[100:103], v[132:135], v[16:31]\n"
    "ds_read_b64_tr_b16 v[116:117], v237 offset:51200\n"
    "ds_read_b64_tr_b16 v[118:119], v237 offset:51712\n"
    "ds_read_b64_tr_b16 v[144:145], v239 offset:22528\n"
    "buffer_load_dwordx4 a[20:23], v236, %[ws], s58 offen offset:1024\n"
    "s_waitcnt lgkmcnt(5)\n"
    "v_mfma_f32_32x32x16_bf16 v[32:47], v[100:103], v[136:139], v[32:47]\n"
    "ds_read_b64_tr_b16 v[120:121], v238 offset:22528\n"
    "ds_read_b64_tr_b16 v[122:123], v238 offset:23040\n"
    "ds_read_b64_tr_b16 v[146:147], v239 offset:23040\n"
    "v_mfma_f32_32x32x16_bf16 v[48:63], v[100:103], v[140:143], v[48:63]\n"
    "ds_read_b64_tr_b16 v[124:125], v238 offset:51200\n"
    "ds_read_b64_tr_b16 v[126:127], v238 offset:51712\n"
    "ds_read_b64_tr_b16 v[148:149], v239 offset:22560\n"
    "buffer_load_dwordx4 a[132:135], v236, %[ws], s59 offen offset:1024\n"
    "s_waitcnt lgkmcnt(7)\n"
    "v_mfma_f32_32x32x16_bf16 v[0:15], v[104:107], v[112:115], v[0:15]\n"
    "ds_read_b64_tr_b16 v[128:129], v237 offset:23552\n"
    "ds_read_b64_tr_b16 v[130:131], v237 offset:24064\n"
    "ds_read_b64_tr_b16 v[150:151], v239 offset:23072\n"
    "s_waitcnt lgkmcnt(6)\n"
    "v_mfma_f32_16x16x32_bf16 v[152:155], v[144:147], v[144:147], v[152:155]\n"
    "v_mfma_f32_32x32x16_bf16 v[16:31], v[104:107], v[116:119], v[16:31]\n"
    "ds_read_b64_tr_b16 v[132:133], v237 offset:52224\n"
    "ds_read_b64_tr_b16 v[134:135], v237 offset:52736\n"
    "buffer_load_dwordx4 a[24:27], v236, %[ws], s58 offen offset:2048\n"
    "s_waitcnt lgkmcnt(6)\n"
    "v_mfma_f32_32x32x16_bf16 v[32:47], v[104:107], v[120:123], v[32:47]\n"
    "ds_read_b64_tr_b16 v[136:137], v238 offset:23552\n"
    "ds_read_b64_tr_b16 v[138:139], v238 offset:24064\n"
    "s_waitcnt lgkmcnt(4)\n"
    "v_mfma_f32_16x16x32_bf16 v[156:159], v[148:151], v[148:151], v[156:159]\n"
    "v_mfma_f32_32x32x16_bf16 v[48:63], v[104:107], v[124:127], v[48:63]\n"
    "ds_read_b64_tr_b16 v[140:141], v238 offset:52224\n"
    "ds_read_b64_tr_b16 v[142:143], v238 offset:52736\n"
    "buffer_load_dwordx4 a[136:139], v236, %[ws], s59 offen offset:2048\n"
    "s_waitcnt lgkmcnt(4)\n"
    "v_mfma_f32_32x32x16_bf16 v[0:15], v[108:111], v[128:131], v[0:15]\n"
    "s_waitcnt vmcnt(10)\n"
    "s_barrier\n"
    "ds_read_b64_tr_b16 v[112:113], v237 offset:24576\n"
    "ds_read_b64_tr_b16 v[114:115], v237 offset:25088\n"
    "v_mfma_f32_32x32x16_bf16 v[16:31], v[108:111], v[132:135], v[16:31]\n"
    "ds_read_b64_tr_b16 v[116:117], v237 offset:53248\n"
    "ds_read_b64_tr_b16 v[118:119], v237 offset:53760\n"
    "ds_read_b64_tr_b16 v[144:145], v239 offset:24576\n"
    "buffer_load_dwordx4 a[28:31], v236, %[ws], s58 offen offset:3072\n"
    "s_waitcnt lgkmcnt(5)\n"
    "v_mfma_f32_32x32x16_bf16 v[32:47], v[108:111], v[136:139], v[32:47]\n"
    "ds_read_b64_tr_b16 v[120:121], v238 offset:24576\n"
    "ds_read_b64_tr_b16 v[122:123], v238 offset:25088\n"
    "ds_read_b64_tr_b16 v[146:147], v239 offset:25088\n"
    "v_mfma_f32_32x32x16_bf16 v[48:63], v[108:111], v[140:143], v[48:63]\n"
    "ds_read_b64_tr_b16 v[124:125], v238 offset:53248\n"
    "ds_read_b64_tr_b16 v[126:127], v238 offset:53760\n"
    "ds_read_b64_tr_b16 v[148:149], v239 offset:24608\n"
    "buffer_load_dwordx4 a[140:143], v236, %[ws], s59 offen offset:3072\n"
    "s_waitcnt lgkmcnt(7)\n"
    "v_mfma_f32_32x32x16_bf16 v[0:15], v[64:67], v[112:115], v[0:15]\n"
    "ds_read_b64_tr_b16 v[128:129], v237 offset:25600\n"
    "ds_read_b64_tr_b16 v[130:131], v237 offset:26112\n"
    "ds_read_b64_tr_b16 v[150:151], v239 offset:25120\n"
    "s_waitcnt lgkmcnt(6)\n"
    "v_mfma_f32_16x16x32_bf16 v[152:155], v[144:147], v[144:147], v[152:155]\n"
    "v_mfma_f32_32x32x16_bf16 v[16:31], v[64:67], v[116:119], v[16:31]\n"
    "ds_read_b64_tr_b16 v[132:133], v237 offset:54272\n"
    "ds_read_b64_tr_b16 v[134:135], v237 offset:54784\n"
    "s_add_u32 s58, s58, 4096\n"
    "buffer_load_dwordx4 a[32:35], v236, %[ws], s58 offen offset:0\n"
    "s_waitcnt lgkmcnt(6)\n"
    "v_mfma_f32_32x32x16_bf16 v[32:47], v[64:67], v[120:123], v[32:47]\n"
    "ds_read_b64_tr_b16 v[136:137], v238 offset:25600\n"
    "ds_read_b64_tr_b16 v[138:139], v238 offset:26112\n"
    "s_waitcnt lgkmcnt(4)\n"
    "v_mfma_f32_16x16x32_bf16 v[156:159], v[148:151], v[148:151], v[156:159]\n"
    "v_mfma_f32_32x32x16_bf16 v[48:63], v[64:67], v[124:127], v[48:63]\n"
    "ds_read_b64_tr_b16 v[140:141], v238 offset:54272\n"
    "ds_read_b64_tr_b16 v[142:143], v238 offset:54784\n"
    "s_add_u32 s59, s59, 4096\n"
    "buffer_load_dwordx4 a[144:147], v236, %[ws], s59 offen offset:0\n"
    "s_waitcnt lgkmcnt(4)\n"
    "v_mfma_f32_32x32x16_bf16 v[0:15], v[68:71], v[128:131], v[0:15]\n"
    "s_waitcnt vmcnt(12)\n"
    "s_barrier\n"
    "ds_read_b64_tr_b16 v[112:113], v237 offset:26624\n"
    "ds_read_b64_tr_b16 v[114:115], v237 offset:27136\n"
    "v_mfma_f32_32x32x16_bf16 v[16:31], v[68:71], v[132:135], v[16:31]\n"
    "ds_read_b64_tr_b16 v[116:117], v237 offset:55296\n"
    "ds_read_b64_tr_b16 v[118:119], v237 offset:55808\n"
    "ds_read_b64_tr_b16 v[144:145], v239 offset:26624\n"
    "buffer_load_dwordx4 a[36:39], v236, %[ws], s58 offen offset:1024\n"
    "s_waitcnt lgkmcnt(5)\n"
    "v_mfma_f32_32x32x16_bf16 v[32:47], v[68:71], v[136:139], v[32:47]\n"
    "ds_read_b64_tr_b16 v[120:121], v238 offset:26624\n"
    "ds_read_b64_tr_b16 v[122:123], v238 offset:27136\n"
    "ds_read_b64_tr_b16 v[146:147], v239 offset:27136\n"
    "v_mfma_f32_32x32x16_bf16 v[48:63], v[68:71], v[140:143], v[48:63]\n"
    "ds_read_b64_tr_b16 v[124:125], v238 offset:55296\n"
    "ds_read_b64_tr_b16 v[126:127], v238 offset:55808\n"
    "ds_read_b64_tr_b16 v[148:149], v239 offset:26656\n"
    "buffer_load_dwordx4 a[148:151], v236, %[ws], s59 offen offset:1024\n"
    "s_waitcnt lgkmcnt(7)\n"
    "v_mfma_f32_32x32x16_bf16 v[0:15], v[72:75], v[112:115], v[0:15]\n"
    "ds_read_b64_tr_b16 v[128:129], v237 offset:27648\n"
    "ds_read_b64_tr_b16 v[130:131], v237 offset:28160\n"
    "ds_read_b64_tr_b16 v[150:151], v239 offset:27168\n"
    "s_waitcnt lgkmcnt(6)\n"
    "v_mfma_f32_16x16x32_bf16 v[152:155], v[144:147], v[144:147], v[152:155]\n"
    "v_mfma_f32_32x32x16_bf16 v[16:31], v[72:75], v[116:119], v[16:31]\n"
    "ds_read_b64_tr_b16 v[132:133], v237 offset:56320\n"
    "ds_read_b64_tr_b16 v[134:135], v237 offset:56832\n"
    "buffer_load_dwordx4 a[40:43], v236, %[ws], s58 offen offset:2048\n"
    "s_waitcnt lgkmcnt(6)\n"
    "v_mfma_f32_32x32x16_bf16 v[32:47], v[72:75], v[120:123], v[32:47]\n"
    "ds_read_b64_tr_b16 v[136:137], v238 offset:27648\n"
    "ds_read_b64_tr_b16 v[138:139], v238 offset:28160\n"
    "s_waitcnt lgkmcnt(4)\n"
    "v_mfma_f32_16x16x32_bf16 v[156:159], v[148:151], v[148:151], v[156:159]\n"
    "v_mfma_f32_32x32x16_bf16 v[48:63], v[72:75], v[124:127], v[48:63]\n"
    "ds_read_b64_tr_b16 v[140:141], v238 offset:56320\n"
    "ds_read_b64_tr_b16 v[142:143], v238 offset:56832\n"
    "buffer_load_dwordx4 a[152:155], v236, %[ws], s59 offen offset:2048\n"
    "v_lshrrev_b32 v243, 5, v255\n"
    "v_lshlrev_b32 v243, 4, v243\n"
    "v_add_u32 v243, s47, v243\n"
    "s_waitcnt lgkmcnt(4)\n"
    "v_mfma_f32_32x32x16_bf16 v[0:15], v[76:79], v[128:131], v[0:15]\n"
    "ds_read_b128 v[216:219], v243 offset:0\n"
    "ds_read_b128 v[220:223], v243 offset:32\n"
    "ds_read_b128 v[224:227], v243 offset:64\n"
    "ds_read_b128 v[228:231], v243 offset:96\n"
    "v_mov_b32 v242, 0x3b124984\n"
    "s_lshl_b32 s56, %[w], 2\n"
    "s_or_b64 s[50:51], s[50:51], s[52:53]\n"
    "v_cndmask_b32_e64 v152, v152, v153, s[48:49]\n"
    "v_cndmask_b32_e64 v154, v154, v155, s[52:53]\n"
    "v_cndmask_b32_e64 v156, v156, v157, s[48:49]\n"
    "v_cndmask_b32_e64 v158, v158, v159, s[52:53]\n"
    "v_cndmask_b32_e64 v152, v152, v154, s[50:51]\n"
    "v_cndmask_b32_e64 v156, v156, v158, s[50:51]\n"
    "v_and_b32 v243, 15, v255\n"
    "s_add_u32 s56, s56, %[lds]\n"
    "v_lshl_add_u32 v243, v243, 4, s56\n"
    "v_mfma_f32_32x32x16_bf16 v[16:31], v[76:79], v[132:135], v[16:31]\n"
    "buffer_load_dwordx4 a[44:47], v236, %[ws], s58 offen offset:3072\n"
    "v_fmaak_f32 v152, v242, v152, 0x2edbe6ff\n"
    "v_fmaak_f32 v156, v242, v156, 0x2edbe6ff\n"
    "v_add_u32 v243, 151552, v243\n"
    "v_rsq_f32 v152, v152\n"
    "v_rsq_f32 v156, v156\n"
    "v_mul_f32 v152, 0x3eeff558, v152\n"
    "v_mul_f32 v156, 0x3eeff558, v156\n"
    "s_mov_b32 exec_lo, 0x00f0000f\n"
    "s_mov_b32 exec_hi, 0xf0000f00\n"
    "ds_write_b32 v243, v152\n"
    "ds_write_b32 v243, v156 offset:256\n"
    "s_mov_b64 exec, -1\n"
    "s_waitcnt lgkmcnt(6)\n"
    "s_nop 2\n"
    "v_mfma_f32_32x32x16_bf16 v[32:47], v[76:79], v[136:139], v[32:47]\n"
    "s_waitcnt lgkmcnt(0)\n"
    "s_barrier\n"
    "ds_read_b128 v[232:235], v245\n"
    "v_mfma_f32_32x32x16_bf16 v[48:63], v[76:79], v[140:143], v[48:63]\n"
    "buffer_load_dwordx4 a[156:159], v236, %[ws], s59 offen offset:3072\n"
    "s_waitcnt lgkmcnt(0)\n"
    "v_pk_fma_f32 v[96:97], v[0:1], v[232:233], v[216:217] op_sel:[0,0,0] op_sel_hi:[1,0,1]\n"
    "v_pk_fma_f32 v[98:99], v[2:3], v[232:233], v[218:219] op_sel:[0,0,0] op_sel_hi:[1,0,1]\n"
    "v_pk_fma_f32 v[108:109], v[96:97], v[96:97], v[240:241] op_sel:[0,0,0] op_sel_hi:[1,1,0]\n"
    "s_add_u32 s58, s58, 4096\n"
    "buffer_load_dwordx4 a[48:51], v236, %[ws], s58 offen offset:0\n"
    "v_pk_fma_f32 v[110:111], v[98:99], v[98:99], v[240:241] op_sel:[0,0,0] op_sel_hi:[1,1,0]\n"
    "v_pk_mul_f32 v[108:109], v[96:97], v[108:109] neg_lo:[1,0] neg_hi:[1,0]\n"
    "v_pk_mul_f32 v[110:111], v[98:99], v[110:111] neg_lo:[1,0] neg_hi:[1,0]\n"
    "v_exp_f32 v108, v108\n"
    "v_pk_fma_f32 v[100:101], v[4:5], v[232:233], v[220:221] op_sel:[0,0,0] op_sel_hi:[1,0,1]\n"
    "v_exp_f32 v109, v109\n"
    "v_pk_fma_f32 v[102:103], v[6:7], v[232:233], v[222:223] op_sel:[0,0,0] op_sel_hi:[1,0,1]\n"
    "v_exp_f32 v110, v110\n"
    "v_pk_fma_f32 v[112:113], v[100:101], v[100:101], v[240:241] op_sel:[0,0,0] op_sel_hi:[1,1,0]\n"
    "s_add_u32 s59, s59, 4096\n"
    "buffer_load_dwordx4 a[160:163], v236, %[ws], s59 offen offset:0\n"
    "v_exp_f32 v111, v111\n"
    "v_pk_fma_f32 v[114:115], v[102:103], v[102:103], v[240:241] op_sel:[0,0,0] op_sel_hi:[1,1,0]\n"
    "v_pk_mul_f32 v[112:113], v[100:101], v[112:113] neg_lo:[1,0] neg_hi:[1,0]\n"
    "v_pk_mul_f32 v[114:115], v[102:103], v[114:115] neg_lo:[1,0] neg_hi:[1,0]\n"
    "v_pk_fma_f32 v[108:109], v[108:109], v[240:241], v[240:241] op_sel:[0,1,1] op_sel_hi:[1,1,1]\n"
    "v_pk_fma_f32 v[110:111], v[110:111], v[240:241], v[240:241] op_sel:[0,1,1] op_sel_hi:[1,1,1]\n"
    "v_exp_f32 v112, v112\n"
    "v_pk_fma_f32 v[104:105], v[8:9], v[232:233], v[224:225] op_sel:[0,0,0] op_sel_hi:[1,0,1]\n"
    "v_exp_f32 v113, v113\n"
    "v_pk_fma_f32 v[106:107], v[10:11], v[232:233], v[226:227] op_sel:[0,0,0] op_sel_hi:[1,0,1]\n"
    "v_exp_f32 v114, v114\n"
    "v_pk_fma_f32 v[116:117], v[104:105], v[104:105], v[240:241] op_sel:[0,0,0] op_sel_hi:[1,1,0]\n"
    "v_exp_f32 v115, v115\n"
    "v_pk_fma_f32 v[118:119], v[106:107], v[106:107], v[240:241] op_sel:[0,0,0] op_sel_hi:[1,1,0]\n"
    "v_rcp_f32 v108, v108\n"
    "v_pk_mul_f32 v[116:117], v[104:105], v[116:117] neg_lo:[1,0] neg_hi:[1,0]\n"
    "v_rcp_f32 v109, v109\n"
    "v_pk_mul_f32 v[118:119], v[106:107], v[118:119] neg_lo:[1,0] neg_hi:[1,0]\n"
    "v_rcp_f32 v110, v110\n"
    "v_pk_fma_f32 v[112:113], v[112:113], v[240:241], v[240:241] op_sel:[0,1,1] op_sel_hi:[1,1,1]\n"
    "v_rcp_f32 v111, v111\n"
    "v_pk_mul_f32 v[108:109], v[96:97], v[108:109]\n"
    "v_pk_fma_f32 v[114:115], v[114:115], v[240:241], v[240:241] op_sel:[0,1,1] op_sel_hi:[1,1,1]\n"
    "v_pk_mul_f32 v[110:111], v[98:99], v[110:111]\n"
    "v_cvt_pk_bf16_f32 v120, v108, v109\n"
    "v_cvt_pk_bf16_f32 v121, v110, v111\n"
    "v_exp_f32 v116, v116\n"
    "v_pk_fma_f32 v[96:97], v[12:13], v[232:233], v[228:229] op_sel:[0,0,0] op_sel_hi:[1,0,1]\n"
    "v_exp_f32 v117, v117\n"
    "v_pk_fma_f32 v[98:99], v[14:15], v[232:233], v[230:231] op_sel:[0,0,0] op_sel_hi:[1,0,1]\n"
    "v_exp_f32 v118, v118\n"
    "v_pk_fma_f32 v[108:109], v[96:97], v[96:97], v[240:241] op_sel:[0,0,0] op_sel_hi:[1,1,0]\n"
    "v_exp_f32 v119, v119\n"
    "v_pk_fma_f32 v[110:111], v[98:99], v[98:99], v[240:241] op_sel:[0,0,0] op_sel_hi:[1,1,0]\n"
    "v_rcp_f32 v112, v112\n"
    "v_pk_mul_f32 v[108:109], v[96:97], v[108:109] neg_lo:[1,0] neg_hi:[1,0]\n"
    "v_rcp_f32 v113, v113\n"
    "v_pk_mul_f32 v[110:111], v[98:99], v[110:111] neg_lo:[1,0] neg_hi:[1,0]\n"
    "v_rcp_f32 v114, v114\n"
    "v_pk_fma_f32 v[116:117], v[116:117], v[240:241], v[240:241] op_sel:[0,1,1] op_sel_hi:[1,1,1]\n"
    "v_rcp_f32 v115, v115\n"
    "v_pk_mul_f32 v[112:113], v[100:101], v[112:113]\n"
    "v_pk_fma_f32 v[118:119], v[118:119], v[240:241], v[240:241] op_sel:[0,1,1] op_sel_hi:[1,1,1]\n"
    "v_pk_mul_f32 v[114:115], v[102:103], v[114:115]\n"
    "v_cvt_pk_bf16_f32 v122, v112, v113\n"
    "v_cvt_pk_bf16_f32 v123, v114, v115\n"
    "buffer_store_dwordx4 v[120:123], v236, %[ws], s45 offen offset:0 sc1\n"
    "v_exp_f32 v108, v108\n"
    "v_pk_fma_f32 v[100:101], v[16:17], v[232:233], v[216:217] op_sel:[0,1,0] op_sel_hi:[1,1,1]\n"
    "v_exp_f32 v109, v109\n"
    "v_pk_fma_f32 v[102:103], v[18:19], v[232:233], v[218:219] op_sel:[0,1,0] op_sel_hi:[1,1,1]\n"
    "v_exp_f32 v110, v110\n"
    "v_pk_fma_f32 v[112:113], v[100:101], v[100:101], v[240:241] op_sel:[0,0,0] op_sel_hi:[1,1,0]\n"
    "v_exp_f32 v111, v111\n"
    "v_pk_fma_f32 v[114:115], v[102:103], v[102:103], v[240:241] op_sel:[0,0,0] op_sel_hi:[1,1,0]\n"
    "v_rcp_f32 v116, v116\n"
    "v_pk_mul_f32 v[112:113], v[100:101], v[112:113] neg_lo:[1,0] neg_hi:[1,0]\n"
    "v_rcp_f32 v117, v117\n"
    "v_pk_mul_f32 v[114:115], v[102:103], v[114:115] neg_lo:[1,0] neg_hi:[1,0]\n"
    "v_rcp_f32 v118, v118\n"
    "v_pk_fma_f32 v[108:109], v[108:109], v[240:241], v[240:241] op_sel:[0,1,1] op_sel_hi:[1,1,1]\n"
    "v_rcp_f32 v119, v119\n"
    "v_pk_mul_f32 v[116:117], v[104:105], v[116:117]\n"
    "v_pk_fma_f32 v[110:111], v[110:111], v[240:241], v[240:241] op_sel:[0,1,1] op_sel_hi:[1,1,1]\n"
    "v_pk_mul_f32 v[118:119], v[106:107], v[118:119]\n"
    "v_cvt_pk_bf16_f32 v124, v116, v117\n"
    "v_cvt_pk_bf16_f32 v125, v118, v119\n"
    "v_exp_f32 v112, v112\n"
    "v_pk_fma_f32 v[104:105], v[20:21], v[232:233], v[220:221] op_sel:[0,1,0] op_sel_hi:[1,1,1]\n"
    "v_exp_f32 v113, v113\n"
    "v_pk_fma_f32 v[106:107], v[22:23], v[232:233], v[222:223] op_sel:[0,1,0] op_sel_hi:[1,1,1]\n"
    "v_exp_f32 v114, v114\n"
    "v_pk_fma_f32 v[116:117], v[104:105], v[104:105], v[240:241] op_sel:[0,0,0] op_sel_hi:[1,1,0]\n"
    "v_exp_f32 v115, v115\n"
    "v_pk_fma_f32 v[118:119], v[106:107], v[106:107], v[240:241] op_sel:[0,0,0] op_sel_hi:[1,1,0]\n"
    "v_rcp_f32 v108, v108\n"
    "v_pk_mul_f32 v[116:117], v[104:105], v[116:117] neg_lo:[1,0] neg_hi:[1,0]\n"
    "v_rcp_f32 v109, v109\n"
    "v_pk_mul_f32 v[118:119], v[106:107], v[118:119] neg_lo:[1,0] neg_hi:[1,0]\n"
    "v_rcp_f32 v110, v110\n"
    "v_pk_fma_f32 v[112:113], v[112:113], v[240:241], v[240:241] op_sel:[0,1,1] op_sel_hi:[1,1,1]\n"
    "v_rcp_f32 v111, v111\n"
    "v_pk_mul_f32 v[108:109], v[96:97], v[108:109]\n"
    "v_pk_fma_f32 v[114:115], v[114:115], v[240:241], v[240:241] op_sel:[0,1,1] op_sel_hi:[1,1,1]\n"
    "v_pk_mul_f32 v[110:111], v[98:99], v[110:111]\n"
    "v_cvt_pk_bf16_f32 v126, v108, v109\n"
    "v_cvt_pk_bf16_f32 v127, v110, v111\n"
    "buffer_store_dwordx4 v[124:127], v236, %[ws], s45 offen offset:1024 sc1\n"
    "s_add_u32 s45, s45, 114688\n"
    "v_exp_f32 v116, v116\n"
    "v_pk_fma_f32 v[96:97], v[24:25], v[232:233], v[224:225] op_sel:[0,1,0] op_sel_hi:[1,1,1]\n"
    "v_exp_f32 v117, v117\n"
    "v_pk_fma_f32 v[98:99], v[26:27], v[232:233], v[226:227] op_sel:[0,1,0] op_sel_hi:[1,1,1]\n"
    "v_exp_f32 v118, v118\n"
    "v_pk_fma_f32 v[108:109], v[96:97], v[96:97], v[240:241] op_sel:[0,0,0] op_sel_hi:[1,1,0]\n"
    "v_exp_f32 v119, v119\n"
    "v_pk_fma_f32 v[110:111], v[98:99], v[98:99], v[240:241] op_sel:[0,0,0] op_sel_hi:[1,1,0]\n"
    "v_rcp_f32 v112, v112\n"
    "v_pk_mul_f32 v[108:109], v[96:97], v[108:109] neg_lo:[1,0] neg_hi:[1,0]\n"
    "v_rcp_f32 v113, v113\n"
    "v_pk_mul_f32 v[110:111], v[98:99], v[110:111] neg_lo:[1,0] neg_hi:[1,0]\n"
    "v_rcp_f32 v114, v114\n"
    "v_pk_fma_f32 v[116:117], v[116:117], v[240:241], v[240:241] op_sel:[0,1,1] op_sel_hi:[1,1,1]\n"
    "v_rcp_f32 v115, v115\n"
    "v_pk_mul_f32 v[112:113], v[100:101], v[112:113]\n"
    "v_pk_fma_f32 v[118:119], v[118:119], v[240:241], v[240:241] op_sel:[0,1,1] op_sel_hi:[1,1,1]\n"
    "v_pk_mul_f32 v[114:115], v[102:103], v[114:115]\n"
    "v_cvt_pk_bf16_f32 v120, v112, v113\n"
    "v_cvt_pk_bf16_f32 v121, v114, v115\n"
    "v_exp_f32 v108, v108\n"
    "v_pk_fma_f32 v[100:101], v[28:29], v[232:233], v[228:229] op_sel:[0,1,0] op_sel_hi:[1,1,1]\n"
    "v_exp_f32 v109, v109\n"
    "v_pk_fma_f32 v[102:103], v[30:31], v[232:233], v[230:231] op_sel:[0,1,0] op_sel_hi:[1,1,1]\n"
    "v_exp_f32 v110, v110\n"
    "v_pk_fma_f32 v[112:113], v[100:101], v[100:101], v[240:241] op_sel:[0,0,0] op_sel_hi:[1,1,0]\n"
    "v_exp_f32 v111, v111\n"
    "v_pk_fma_f32 v[114:115], v[102:103], v[102:103], v[240:241] op_sel:[0,0,0] op_sel_hi:[1,1,0]\n"
    "v_rcp_f32 v116, v116\n"
    "v_pk_mul_f32 v[112:113], v[100:101], v[112:113] neg_lo:[1,0] neg_hi:[1,0]\n"
    "v_rcp_f32 v117, v117\n"
    "v_pk_mul_f32 v[114:115], v[102:103], v[114:115] neg_lo:[1,0] neg_hi:[1,0]\n"
    "v_rcp_f32 v118, v118\n"
    "v_pk_fma_f32 v[108:109], v[108:109], v[240:241], v[240:241] op_sel:[0,1,1] op_sel_hi:[1,1,1]\n"
    "v_rcp_f32 v119, v119\n"
    "v_pk_mul_f32 v[116:117], v[104:105], v[116:117]\n"
    "v_pk_fma_f32 v[110:111], v[110:111], v[240:241], v[240:241] op_sel:[0,1,1] op_sel_hi:[1,1,1]\n"
    "v_pk_mul_f32 v[118:119], v[106:107], v[118:119]\n"
    "v_cvt_pk_bf16_f32 v122, v116, v117\n"
    "v_cvt_pk_bf16_f32 v123, v118, v119\n"
    "buffer_store_dwordx4 v[120:123], v236, %[ws], s45 offen offset:0 sc1\n"
    "v_exp_f32 v112, v112\n"
    "v_pk_fma_f32 v[104:105], v[32:33], v[234:235], v[216:217] op_sel:[0,0,0] op_sel_hi:[1,0,1]\n"
    "v_exp_f32 v113, v113\n"
    "v_pk_fma_f32 v[106:107], v[34:35], v[234:235], v[218:219] op_sel:[0,0,0] op_sel_hi:[1,0,1]\n"
    "v_exp_f32 v114, v114\n"
    "v_pk_fma_f32 v[116:117], v[104:105], v[104:105], v[240:241] op_sel:[0,0,0] op_sel_hi:[1,1,0]\n"
    "v_exp_f32 v115, v115\n"
    "v_pk_fma_f32 v[118:119], v[106:107], v[106:107], v[240:241] op_sel:[0,0,0] op_sel_hi:[1,1,0]\n"
    "v_rcp_f32 v108, v108\n"
    "v_pk_mul_f32 v[116:117], v[104:105], v[116:117] neg_lo:[1,0] neg_hi:[1,0]\n"
    "v_rcp_f32 v109, v109\n"
    "v_pk_mul_f32 v[118:119], v[106:107], v[118:119] neg_lo:[1,0] neg_hi:[1,0]\n"
    "v_rcp_f32 v110, v110\n"
    "v_pk_fma_f32 v[112:113], v[112:113], v[240:241], v[240:241] op_sel:[0,1,1] op_sel_hi:[1,1,1]\n"
    "v_rcp_f32 v111, v111\n"
    "v_pk_mul_f32 v[108:109], v[96:97], v[108:109]\n"
    "v_pk_fma_f32 v[114:115], v[114:115], v[240:241], v[240:241] op_sel:[0,1,1] op_sel_hi:[1,1,1]\n"
    "v_pk_mul_f32 v[110:111], v[98:99], v[110:111]\n"
    "v_cvt_pk_bf16_f32 v124, v108, v109\n"
    "v_cvt_pk_bf16_f32 v125, v110, v111\n"
    "v_exp_f32 v116, v116\n"
    "v_pk_fma_f32 v[96:97], v[36:37], v[234:235], v[220:221] op_sel:[0,0,0] op_sel_hi:[1,0,1]\n"
    "v_exp_f32 v117, v117\n"
    "v_pk_fma_f32 v[98:99], v[38:39], v[234:235], v[222:223] op_sel:[0,0,0] op_sel_hi:[1,0,1]\n"
    "v_exp_f32 v118, v118\n"
    "v_pk_fma_f32 v[108:109], v[96:97], v[96:97], v[240:241] op_sel:[0,0,0] op_sel_hi:[1,1,0]\n"
    "buffer_load_dwordx4 a[52:55], v236, %[ws], s58 offen offset:1024\n"
    "v_exp_f32 v119, v119\n"
    "v_pk_fma_f32 v[110:111], v[98:99], v[98:99], v[240:241] op_sel:[0,0,0] op_sel_hi:[1,1,0]\n"
    "v_rcp_f32 v112, v112\n"
    "v_pk_mul_f32 v[108:109], v[96:97], v[108:109] neg_lo:[1,0] neg_hi:[1,0]\n"
    "v_rcp_f32 v113, v113\n"
    "v_pk_mul_f32 v[110:111], v[98:99], v[110:111] neg_lo:[1,0] neg_hi:[1,0]\n"
    "v_rcp_f32 v114, v114\n"
    "v_pk_fma_f32 v[116:117], v[116:117], v[240:241], v[240:241] op_sel:[0,1,1] op_sel_hi:[1,1,1]\n"
    "v_rcp_f32 v115, v115\n"
    "v_pk_mul_f32 v[112:113], v[100:101], v[112:113]\n"
    "v_pk_fma_f32 v[118:119], v[118:119], v[240:241], v[240:241] op_sel:[0,1,1] op_sel_hi:[1,1,1]\n"
    "v_pk_mul_f32 v[114:115], v[102:103], v[114:115]\n"
    "v_cvt_pk_bf16_f32 v126, v112, v113\n"
    "v_cvt_pk_bf16_f32 v127, v114, v115\n"
    "buffer_store_dwordx4 v[124:127], v236, %[ws], s45 offen offset:1024 sc1\n"
    "s_add_u32 s45, s45, 114688\n"
    "v_exp_f32 v108, v108\n"
    "v_pk_fma_f32 v[100:101], v[40:41], v[234:235], v[224:225] op_sel:[0,0,0] op_sel_hi:[1,0,1]\n"
    "v_exp_f32 v109, v109\n"
    "v_pk_fma_f32 v[102:103], v[42:43], v[234:235], v[226:227] op_sel:[0,0,0] op_sel_hi:[1,0,1]\n"
    "v_exp_f32 v110, v110\n"
    "v_pk_fma_f32 v[112:113], v[100:101], v[100:101], v[240:241] op_sel:[0,0,0] op_sel_hi:[1,1,0]\n"
    "buffer_load_dwordx4 a[164:167], v236, %[ws], s59 offen offset:1024\n"
    "v_exp_f32 v111, v111\n"
    "v_pk_fma_f32 v[114:115], v[102:103], v[102:103], v[240:241] op_sel:[0,0,0] op_sel_hi:[1,1,0]\n"
    "v_rcp_f32 v116, v116\n"
    "v_pk_mul_f32 v[112:113], v[100:101], v[112:113] neg_lo:[1,0] neg_hi:[1,0]\n"
    "v_rcp_f32 v117, v117\n"
    "v_pk_mul_f32 v[114:115], v[102:103], v[114:115] neg_lo:[1,0] neg_hi:[1,0]\n"
    "v_rcp_f32 v118, v118\n"
    "v_pk_fma_f32 v[108:109], v[108:109], v[240:241], v[240:241] op_sel:[0,1,1] op_sel_hi:[1,1,1]\n"
    "v_rcp_f32 v119, v119\n"
    "v_pk_mul_f32 v[116:117], v[104:105], v[116:117]\n"
    "v_pk_fma_f32 v[110:111], v[110:111], v[240:241], v[240:241] op_sel:[0,1,1] op_sel_hi:[1,1,1]\n"
    "v_pk_mul_f32 v[118:119], v[106:107], v[118:119]\n"
    "v_cvt_pk_bf16_f32 v120, v116, v117\n"
    "v_cvt_pk_bf16_f32 v121, v118, v119\n"
    "v_exp_f32 v112, v112\n"
    "v_pk_fma_f32 v[104:105], v[44:45], v[234:235], v[228:229] op_sel:[0,0,0] op_sel_hi:[1,0,1]\n"
    "v_exp_f32 v113, v113\n"
    "v_pk_fma_f32 v[106:107], v[46:47], v[234:235], v[230:231] op_sel:[0,0,0] op_sel_hi:[1,0,1]\n"
    "v_exp_f32 v114, v114\n"
    "v_pk_fma_f32 v[116:117], v[104:105], v[104:105], v[240:241] op_sel:[0,0,0] op_sel_hi:[1,1,0]\n"
    "buffer_load_dwordx4 a[56:59], v236, %[ws], s58 offen offset:2048\n"
    "v_exp_f32 v115, v115\n"
    "v_pk_fma_f32 v[118:119], v[106:107], v[106:107], v[240:241] op_sel:[0,0,0] op_sel_hi:[1,1,0]\n"
    "v_rcp_f32 v108, v108\n"
    "v_pk_mul_f32 v[116:117], v[104:105], v[116:117] neg_lo:[1,0] neg_hi:[1,0]\n"
    "v_rcp_f32 v109, v109\n"
    "v_pk_mul_f32 v[118:119], v[106:107], v[118:119] neg_lo:[1,0] neg_hi:[1,0]\n"
    "v_rcp_f32 v110, v110\n"
    "v_pk_fma_f32 v[112:113], v[112:113], v[240:241], v[240:241] op_sel:[0,1,1] op_sel_hi:[1,1,1]\n"
    "v_rcp_f32 v111, v111\n"
    "v_pk_mul_f32 v[108:109], v[96:97], v[108:109]\n"
    "v_pk_fma_f32 v[114:115], v[114:115], v[240:241], v[240:241] op_sel:[0,1,1] op_sel_hi:[1,1,1]\n"
    "v_pk_mul_f32 v[110:111], v[98:99], v[110:111]\n"
    "v_cvt_pk_bf16_f32 v122, v108, v109\n"
    "v_cvt_pk_bf16_f32 v123, v110, v111\n"
    "buffer_store_dwordx4 v[120:123], v236, %[ws], s45 offen offset:0 sc1\n"
    "v_exp_f32 v116, v116\n"
    "v_pk_fma_f32 v[96:97], v[48:49], v[234:235], v[216:217] op_sel:[0,1,0] op_sel_hi:[1,1,1]\n"
    "v_exp_f32 v117, v117\n"
    "v_pk_fma_f32 v[98:99], v[50:51], v[234:235], v[218:219] op_sel:[0,1,0] op_sel_hi:[1,1,1]\n"
    "v_exp_f32 v118, v118\n"
    "v_pk_fma_f32 v[108:109], v[96:97], v[96:97], v[240:241] op_sel:[0,0,0] op_sel_hi:[1,1,0]\n"
    "buffer_load_dwordx4 a[168:171], v236, %[ws], s59 offen offset:2048\n"
    "v_exp_f32 v119, v119\n"
    "v_pk_fma_f32 v[110:111], v[98:99], v[98:99], v[240:241] op_sel:[0,0,0] op_sel_hi:[1,1,0]\n"
    "v_rcp_f32 v112, v112\n"
    "v_pk_mul_f32 v[108:109], v[96:97], v[108:109] neg_lo:[1,0] neg_hi:[1,0]\n"
    "v_rcp_f32 v113, v113\n"
    "v_pk_mul_f32 v[110:111], v[98:99], v[110:111] neg_lo:[1,0] neg_hi:[1,0]\n"
    "v_rcp_f32 v114, v114\n"
    "v_pk_fma_f32 v[116:117], v[116:117], v[240:241], v[240:241] op_sel:[0,1,1] op_sel_hi:[1,1,1]\n"
    "v_rcp_f32 v115, v115\n"
    "v_pk_mul_f32 v[112:113], v[100:101], v[112:113]\n"
    "v_pk_fma_f32 v[118:119], v[118:119], v[240:241], v[240:241] op_sel:[0,1,1] op_sel_hi:[1,1,1]\n"
    "v_pk_mul_f32 v[114:115], v[102:103], v[114:115]\n"
    "v_cvt_pk_bf16_f32 v124, v112, v113\n"
    "v_cvt_pk_bf16_f32 v125, v114, v115\n"
    "v_exp_f32 v108, v108\n"
    "v_pk_fma_f32 v[100:101], v[52:53], v[234:235], v[220:221] op_sel:[0,1,0] op_sel_hi:[1,1,1]\n"
    "v_exp_f32 v109, v109\n"
    "v_pk_fma_f32 v[102:103], v[54:55], v[234:235], v[222:223] op_sel:[0,1,0] op_sel_hi:[1,1,1]\n"
    "v_exp_f32 v110, v110\n"
    "v_pk_fma_f32 v[112:113], v[100:101], v[100:101], v[240:241] op_sel:[0,0,0] op_sel_hi:[1,1,0]\n"
    "buffer_load_dwordx4 a[60:63], v236, %[ws], s58 offen offset:3072\n"
    "v_exp_f32 v111, v111\n"
    "v_pk_fma_f32 v[114:115], v[102:103], v[102:103], v[240:241] op_sel:[0,0,0] op_sel_hi:[1,1,0]\n"
    "v_rcp_f32 v116, v116\n"
    "v_pk_mul_f32 v[112:113], v[100:101], v[112:113] neg_lo:[1,0] neg_hi:[1,0]\n"
    "v_rcp_f32 v117, v117\n"
    "v_pk_mul_f32 v[114:115], v[102:103], v[114:115] neg_lo:[1,0] neg_hi:[1,0]\n"
    "v_rcp_f32 v118, v118\n"
    "v_pk_fma_f32 v[108:109], v[108:109], v[240:241], v[240:241] op_sel:[0,1,1] op_sel_hi:[1,1,1]\n"
    "v_rcp_f32 v119, v119\n"
    "v_pk_mul_f32 v[116:117], v[104:105], v[116:117]\n"
    "v_pk_fma_f32 v[110:111], v[110:111], v[240:241], v[240:241] op_sel:[0,1,1] op_sel_hi:[1,1,1]\n"
    "v_pk_mul_f32 v[118:119], v[106:107], v[118:119]\n"
    "v_cvt_pk_bf16_f32 v126, v116, v117\n"
    "v_cvt_pk_bf16_f32 v127, v118, v119\n"
    "buffer_store_dwordx4 v[124:127], v236, %[ws], s45 offen offset:1024 sc1\n"
    "s_add_u32 s45, s45, 114688\n"
    "v_exp_f32 v112, v112\n"
    "v_pk_fma_f32 v[104:105], v[56:57], v[234:235], v[224:225] op_sel:[0,1,0] op_sel_hi:[1,1,1]\n"
    "v_exp_f32 v113, v113\n"
    "v_pk_fma_f32 v[106:107], v[58:59], v[234:235], v[226:227] op_sel:[0,1,0] op_sel_hi:[1,1,1]\n"
    "v_exp_f32 v114, v114\n"
    "v_pk_fma_f32 v[116:117], v[104:105], v[104:105], v[240:241] op_sel:[0,0,0] op_sel_hi:[1,1,0]\n"
    "buffer_load_dwordx4 a[172:175], v236, %[ws], s59 offen offset:3072\n"
    "v_exp_f32 v115, v115\n"
    "v_pk_fma_f32 v[118:119], v[106:107], v[106:107], v[240:241] op_sel:[0,0,0] op_sel_hi:[1,1,0]\n"
    "v_rcp_f32 v108, v108\n"
    "v_pk_mul_f32 v[116:117], v[104:105], v[116:117] neg_lo:[1,0] neg_hi:[1,0]\n"
    "v_rcp_f32 v109, v109\n"
    "v_pk_mul_f32 v[118:119], v[106:107], v[118:119] neg_lo:[1,0] neg_hi:[1,0]\n"
    "v_rcp_f32 v110, v110\n"
    "v_pk_fma_f32 v[112:113], v[112:113], v[240:241], v[240:241] op_sel:[0,1,1] op_sel_hi:[1,1,1]\n"
    "v_rcp_f32 v111, v111\n"
    "v_pk_mul_f32 v[108:109], v[96:97], v[108:109]\n"
    "v_pk_fma_f32 v[114:115], v[114:115], v[240:241], v[240:241] op_sel:[0,1,1] op_sel_hi:[1,1,1]\n"
    "v_pk_mul_f32 v[110:111], v[98:99], v[110:111]\n"
    "v_cvt_pk_bf16_f32 v120, v108, v109\n"
    "v_cvt_pk_bf16_f32 v121, v110, v111\n"
    "v_exp_f32 v116, v116\n"
    "v_pk_fma_f32 v[96:97], v[60:61], v[234:235], v[228:229] op_sel:[0,1,0] op_sel_hi:[1,1,1]\n"
    "v_exp_f32 v117, v117\n"
    "v_pk_fma_f32 v[98:99], v[62:63], v[234:235], v[230:231] op_sel:[0,1,0] op_sel_hi:[1,1,1]\n"
    "v_exp_f32 v118, v118\n"
    "v_pk_fma_f32 v[108:109], v[96:97], v[96:97], v[240:241] op_sel:[0,0,0] op_sel_hi:[1,1,0]\n"
    "s_add_u32 s58, s58, 4096\n"
    "buffer_load_dwordx4 a[64:67], v236, %[ws], s58 offen offset:0\n"
    "v_exp_f32 v119, v119\n"
    "v_pk_fma_f32 v[110:111], v[98:99], v[98:99], v[240:241] op_sel:[0,0,0] op_sel_hi:[1,1,0]\n"
    "v_rcp_f32 v112, v112\n"
    "v_pk_mul_f32 v[108:109], v[96:97], v[108:109] neg_lo:[1,0] neg_hi:[1,0]\n"
    "v_rcp_f32 v113, v113\n"
    "v_pk_mul_f32 v[110:111], v[98:99], v[110:111] neg_lo:[1,0] neg_hi:[1,0]\n"
    "v_rcp_f32 v114, v114\n"
    "v_pk_fma_f32 v[116:117], v[116:117], v[240:241], v[240:241] op_sel:[0,1,1] op_sel_hi:[1,1,1]\n"
    "v_rcp_f32 v115, v115\n"
    "v_pk_mul_f32 v[112:113], v[100:101], v[112:113]\n"
    "v_pk_fma_f32 v[118:119], v[118:119], v[240:241], v[240:241] op_sel:[0,1,1] op_sel_hi:[1,1,1]\n"
    "v_pk_mul_f32 v[114:115], v[102:103], v[114:115]\n"
    "v_cvt_pk_bf16_f32 v122, v112, v113\n"
    "v_cvt_pk_bf16_f32 v123, v114, v115\n"
    "buffer_store_dwordx4 v[120:123], v236, %[ws], s45 offen offset:0 sc1\n"
    "s_waitcnt vmcnt(9)\n"
    "v_mov_b32 v242, 1\n"
    "s_mov_b64 exec, 1\n"
    "buffer_store_dword v242, off, %[ws], %[fw] sc1\n"
    "s_mov_b64 exec, -1\n"
    "v_exp_f32 v108, v108\n"
    "v_exp_f32 v109, v109\n"
    "v_exp_f32 v110, v110\n"
    "s_add_u32 s59, s59, 4096\n"
    "buffer_load_dwordx4 a[176:179], v236, %[ws], s59 offen offset:0\n"
    "v_exp_f32 v111, v111\n"
    "v_rcp_f32 v116, v116\n"
    "v_rcp_f32 v117, v117\n"
    "v_rcp_f32 v118, v118\n"
    "v_pk_fma_f32 v[108:109], v[108:109], v[240:241], v[240:241] op_sel:[0,1,1] op_sel_hi:[1,1,1]\n"
    "v_rcp_f32 v119, v119\n"
    "v_pk_mul_f32 v[116:117], v[104:105], v[116:117]\n"
    "v_pk_fma_f32 v[110:111], v[110:111], v[240:241], v[240:241] op_sel:[0,1,1] op_sel_hi:[1,1,1]\n"
    "v_pk_mul_f32 v[118:119], v[106:107], v[118:119]\n"
    "v_cvt_pk_bf16_f32 v124, v116, v117\n"
    "v_cvt_pk_bf16_f32 v125, v118, v119\n"
    "buffer_load_dwordx4 a[68:71], v236, %[ws], s58 offen offset:1024\n"
    "v_rcp_f32 v108, v108\n"
    "v_rcp_f32 v109, v109\n"
    "v_rcp_f32 v110, v110\n"
    "v_rcp_f32 v111, v111\n"
    "v_pk_mul_f32 v[108:109], v[96:97], v[108:109]\n"
    "v_pk_mul_f32 v[110:111], v[98:99], v[110:111]\n"
    "v_cvt_pk_bf16_f32 v126, v108, v109\n"
    "v_cvt_pk_bf16_f32 v127, v110, v111\n"
    "buffer_store_dwordx4 v[124:127], v236, %[ws], s45 offen offset:1024 sc1\n"
    "s_add_u32 s45, s45, 114688\n"
    "buffer_load_dwordx4 a[180:183], v236, %[ws], s59 offen offset:1024\n"
    "s_sleep 0\n"
    "buffer_load_dwordx4 a[72:75], v236, %[ws], s58 offen offset:2048\n"
    "s_sleep 0\n"
    "buffer_load_dwordx4 a[184:187], v236, %[ws], s59 offen offset:2048\n"
    "s_sleep 0\n"
    "buffer_load_dwordx4 a[76:79], v236, %[ws], s58 offen offset:3072\n"
    "s_sleep 0\n"
    "buffer_load_dwordx4 a[188:191], v236, %[ws], s59 offen offset:3072\n"
    "s_sleep 0\n"
    "s_add_u32 s58, s58, 4096\n"
    "buffer_load_dwordx4 a[80:83], v236, %[ws], s58 offen offset:0\n"
    "s_sleep 0\n"
    "s_add_u32 s59, s59, 4096\n"
    "buffer_load_dwordx4 a[192:195], v236, %[ws], s59 offen offset:0\n"
    "s_sleep 0\n"
    "buffer_load_dwordx4 a[84:87], v236, %[ws], s58 offen offset:1024\n"
    "s_sleep 0\n"
    "buffer_load_dwordx4 a[196:199], v236, %[ws], s59 offen offset:1024\n"
    "s_sleep 0\n"
    "buffer_load_dwordx4 a[88:91], v236, %[ws], s58 offen offset:2048\n"
    "s_sleep 0\n"
    "buffer_load_dwordx4 a[200:203], v236, %[ws], s59 offen offset:2048\n"
    "s_sleep 0\n"
    "buffer_load_dwordx4 a[92:95], v236, %[ws], s58 offen offset:3072\n"
    "s_sleep 0\n"
    "buffer_load_dword v247, v246, %[ws], %[fr] offen sc1\n"
    "v_add_u32 v243, %[rl], v250\n"
    "ds_read_b128 v[176:179], v243\n"
    "ds_read_b128 v[180:183], v243 offset:57344\n"
    "buffer_load_dwordx4 a[204:207], v236, %[ws], s59 offen offset:3072\n"
    "s_add_u32 s58, s58, 4096\n"
    "buffer_load_dwordx4 a[96:99], v236, %[ws], s58 offen offset:0\n"
    "s_waitcnt lgkmcnt(0)\n"
    "v_lshlrev_b32 v160, 16, v176\n"
    "v_and_b32 v161, 0xffff0000, v176\n"
    "v_lshlrev_b32 v162, 16, v177\n"
    "v_and_b32 v163, 0xffff0000, v177\n"
    "v_lshlrev_b32 v164, 16, v178\n"
    "v_and_b32 v165, 0xffff0000, v178\n"
    "v_lshlrev_b32 v166, 16, v179\n"
    "v_and_b32 v167, 0xffff0000, v179\n"
    "s_add_u32 s59, s59, 4096\n"
    "buffer_load_dwordx4 a[208:211], v236, %[ws], s59 offen offset:0\n"
    "v_pk_add_f32 v[160:161], v[160:161], v[184:185] op_sel:[0,0] op_sel_hi:[1,0]\n"
    "v_pk_add_f32 v[162:163], v[162:163], v[184:185] op_sel:[0,0] op_sel_hi:[1,0]\n"
    "v_pk_add_f32 v[164:165], v[164:165], v[184:185] op_sel:[0,0] op_sel_hi:[1,0]\n"
    "v_pk_add_f32 v[166:167], v[166:167], v[184:185] op_sel:[0,0] op_sel_hi:[1,0]\n"
    "buffer_load_dwordx4 a[100:103], v236, %[ws], s58 offen offset:1024\n"
    "v_lshlrev_b32 v168, 16, v180\n"
    "v_and_b32 v169, 0xffff0000, v180\n"
    "v_lshlrev_b32 v170, 16, v181\n"
    "v_and_b32 v171, 0xffff0000, v181\n"
    "v_lshlrev_b32 v172, 16, v182\n"
    "v_and_b32 v173, 0xffff0000, v182\n"
    "v_lshlrev_b32 v174, 16, v183\n"
    "v_and_b32 v175, 0xffff0000, v183\n"
    "buffer_load_dwordx4 a[212:215], v236, %[ws], s59 offen offset:1024\n"
    "v_pk_add_f32 v[168:169], v[168:169], v[184:185] op_sel:[0,0] op_sel_hi:[1,0]\n"
    "v_pk_add_f32 v[170:171], v[170:171], v[184:185] op_sel:[0,0] op_sel_hi:[1,0]\n"
    "v_pk_add_f32 v[172:173], v[172:173], v[184:185] op_sel:[0,0] op_sel_hi:[1,0]\n"
    "v_pk_add_f32 v[174:175], v[174:175], v[184:185] op_sel:[0,0] op_sel_hi:[1,0]\n"
    "buffer_load_dwordx4 a[104:107], v236, %[ws], s58 offen offset:2048\n"
    "v_mov_b32 v186, 1.0\n"
    "s_mov_b64 exec, 1\n"
    "v_mov_b32 v186, %[mk]\n"
    "s_mov_b64 exec, -1\n"
    "v_lshrrev_b32 v187, 3, v255\n"
    "v_lshlrev_b32 v188, 10, v187\n"
    "v_lshlrev_b32 v187, 2, v187\n"
    "v_or_b32 v187, v187, v188\n"
    "v_and_b32 v187, 0x100c, v187\n"
    "s_mov_b32 s48, 0xff00ff00\n"
    "s_mov_b32 s49, 0xff00ff00\n"
    "buffer_load_dwordx4 a[216:219], v236, %[ws], s59 offen offset:2048\n"
    "buffer_load_dwordx4 a[108:111], v236, %[ws], s58 offen offset:3072\n"
    "buffer_load_dwordx4 a[220:223], v236, %[ws], s59 offen offset:3072\n"
    "s_add_u32 s58, %[hl], 28672\n"
    "s_waitcnt vmcnt(9)\n"
    "v_cmp_gt_u32 vcc, 1, v247\n"
    "s_nop 4\n"
    "s_cbranch_vccnz LS1_%=\n"
    "LR1_%=:\n"
    "v_mov_b32 v242, 2\n"
    "s_mov_b64 exec, 1\n"
    "buffer_store_dword v242, off, %[ws], %[fw] sc1\n"
    "s_mov_b64 exec, -1\n"
    "buffer_load_dwordx4 v[0:3], v236, %[ws], s44 offen offset:0 sc1\n"
    "buffer_load_dwordx4 v[4:7], v236, %[ws], s44 offen offset:1024 sc1\n"
    "buffer_load_dwordx4 v[8:11], v236, %[ws], s44 offen offset:2048 sc1\n"
    "buffer_load_dwordx4 v[12:15], v236, %[ws], s44 offen offset:3072 sc1\n"
    "s_add_u32 s44, s44, 4096\n"
    "buffer_load_dwordx4 v[16:19], v236, %[ws], s44 offen offset:0 sc1\n"
    "buffer_load_dwordx4 v[20:23], v236, %[ws], s44 offen offset:1024 sc1\n"
    "buffer_load_dwordx4 v[24:27], v236, %[ws], s44 offen offset:2048 sc1\n"
    "buffer_load_dwordx4 v[28:31], v236, %[ws], s44 offen offset:3072 sc1\n"
    "s_add_u32 s44, s44, 4096\n"
    "buffer_load_dwordx4 v[32:35], v236, %[ws], s44 offen offset:0 sc1\n"
    "buffer_load_dwordx4 v[36:39], v236, %[ws], s44 offen offset:1024 sc1\n"
    "buffer_load_dwordx4 v[40:43], v236, %[ws], s44 offen offset:2048 sc1\n"
    "buffer_load_dwordx4 v[44:47], v236, %[ws], s44 offen offset:3072 sc1\n"
    "s_add_u32 s44, s44, 4096\n"
    "buffer_load_dwordx4 v[48:51], v236, %[ws], s44 offen offset:0 sc1\n"
    "s_waitcnt vmcnt(12)\n"
    "v_mfma_f32_32x32x16_bf16 a[224:239], v[0:3], a[0:3], 0\n"
    "v_mfma_f32_32x32x16_bf16 a[240:255], v[0:3], a[112:115], 0\n"
    "buffer_load_dwordx4 v[52:55], v236, %[ws], s44 offen offset:1024 sc1\n"
    "s_waitcnt vmcnt(12)\n"
    "v_mfma_f32_32x32x16_bf16 a[224:239], v[4:7], a[4:7], a[224:239]\n"
    "v_mfma_f32_32x32x16_bf16 a[240:255], v[4:7], a[116:119], a[240:255]\n"
    "buffer_load_dwordx4 v[56:59], v236, %[ws], s44 offen offset:2048 sc1\n"
    "s_waitcnt vmcnt(12)\n"
    "v_mfma_f32_32x32x16_bf16 a[224:239], v[8:11], a[8:11], a[224:239]\n"
    "v_mfma_f32_32x32x16_bf16 a[240:255], v[8:11], a[120:123], a[240:255]\n"
    "buffer_load_dwordx4 v[60:63], v236, %[ws], s44 offen offset:3072 sc1\n"
    "s_waitcnt vmcnt(12)\n"
    "v_mfma_f32_32x32x16_bf16 a[224:239], v[12:15], a[12:15], a[224:239]\n"
    "v_mfma_f32_32x32x16_bf16 a[240:255], v[12:15], a[124:127], a[240:255]\n"
    "s_add_u32 s44, s44, 4096\n"
    "buffer_load_dwordx4 v[0:3], v236, %[ws], s44 offen offset:0 sc1\n"
    "s_waitcnt vmcnt(12)\n"
    "v_mfma_f32_32x32x16_bf16 a[224:239], v[16:19], a[16:19], a[224:239]\n"
    "v_mfma_f32_32x32x16_bf16 a[240:255], v[16:19], a[128:131], a[240:255]\n"
    "buffer_load_dwordx4 v[4:7], v236, %[ws], s44 offen offset:1024 sc1\n"
    "s_waitcnt vmcnt(12)\n"
    "v_mfma_f32_32x32x16_bf16 a[224:239], v[20:23], a[20:23], a[224:239]\n"
    "v_mfma_f32_32x32x16_bf16 a[240:255], v[20:23], a[132:135], a[240:255]\n"
    "buffer_load_dwordx4 v[8:11], v236, %[ws], s44 offen offset:2048 sc1\n"
    "s_waitcnt vmcnt(12)\n"
    "v_mfma_f32_32x32x16_bf16 a[224:239], v[24:27], a[24:27], a[224:239]\n"
    "v_mfma_f32_32x32x16_bf16 a[240:255], v[24:27], a[136:139], a[240:255]\n"
    "buffer_load_dwordx4 v[12:15], v236, %[ws], s44 offen offset:3072 sc1\n"
    "s_waitcnt vmcnt(12)\n"
    "v_mfma_f32_32x32x16_bf16 a[224:239], v[28:31], a[28:31], a[224:239]\n"
    "v_mfma_f32_32x32x16_bf16 a[240:255], v[28:31], a[140:143], a[240:255]\n"
    "s_add_u32 s44, s44, 4096\n"
    "buffer_load_dwordx4 v[16:19], v236, %[ws], s44 offen offset:0 sc1\n"
    "s_waitcnt vmcnt(12)\n"
    "v_mfma_f32_32x32x16_bf16 a[224:239], v[32:35], a[32:35], a[224:239]\n"
    "v_mfma_f32_32x32x16_bf16 a[240:255], v[32:35], a[144:147], a[240:255]\n"
    "buffer_load_dwordx4 v[20:23], v236, %[ws], s44 offen offset:1024 sc1\n"
    "s_waitcnt vmcnt(12)\n"
    "v_mfma_f32_32x32x16_bf16 a[224:239], v[36:39], a[36:39], a[224:239]\n"
    "v_mfma_f32_32x32x16_bf16 a[240:255], v[36:39], a[148:151], a[240:255]\n"
    "buffer_load_dwordx4 v[24:27], v236, %[ws], s44 offen offset:2048 sc1\n"
    "s_waitcnt vmcnt(12)\n"
    "v_mfma_f32_32x32x16_bf16 a[224:239], v[40:43], a[40:43], a[224:239]\n"
    "v_mfma_f32_32x32x16_bf16 a[240:255], v[40:43], a[152:155], a[240:255]\n"
    "buffer_load_dwordx4 v[28:31], v236, %[ws], s44 offen offset:3072 sc1\n"
    "s_waitcnt vmcnt(12)\n"
    "v_mfma_f32_32x32x16_bf16 a[224:239], v[44:47], a[44:47], a[224:239]\n"
    "v_mfma_f32_32x32x16_bf16 a[240:255], v[44:47], a[156:159], a[240:255]\n"
    "s_add_u32 s44, s44, 4096\n"
    "buffer_load_dwordx4 v[32:35], v236, %[ws], s44 offen offset:0 sc1\n"
    "buffer_load_dword v247, v246, %[ws], %[fr] offen sc1\n"
    "s_waitcnt vmcnt(13)\n"
    "v_mfma_f32_32x32x16_bf16 a[224:239], v[48:51], a[48:51], a[224:239]\n"
    "v_mfma_f32_32x32x16_bf16 a[240:255], v[48:51], a[160:163], a[240:255]\n"
    "buffer_load_dwordx4 v[36:39], v236, %[ws], s44 offen offset:1024 sc1\n"
    "s_waitcnt vmcnt(13)\n"
    "v_mfma_f32_32x32x16_bf16 a[224:239], v[52:55], a[52:55], a[224:239]\n"
    "v_mfma_f32_32x32x16_bf16 a[240:255], v[52:55], a[164:167], a[240:255]\n"
    "buffer_load_dwordx4 v[40:43], v236, %[ws], s44 offen offset:2048 sc1\n"
    "s_waitcnt vmcnt(13)\n"
    "v_mfma_f32_32x32x16_bf16 a[224:239], v[56:59], a[56:59], a[224:239]\n"
    "v_mfma_f32_32x32x16_bf16 a[240:255], v[56:59], a[168:171], a[240:255]\n"
    "buffer_load_dwordx4 v[44:47], v236, %[ws], s44 offen offset:3072 sc1\n"
    "s_waitcnt vmcnt(13)\n"
    "v_mfma_f32_32x32x16_bf16 a[224:239], v[60:63], a[60:63], a[224:239]\n"
    "v_mfma_f32_32x32x16_bf16 a[240:255], v[60:63], a[172:175], a[240:255]\n"
    "s_waitcnt vmcnt(3)\n"
    "v_cmp_gt_u32 vcc, 2, v247\n"
    "s_nop 4\n"
    "s_cbranch_vccnz LS2_%=\n"
    "LR2_%=:\n"
    "s_add_u32 s59, %[hl], 229376\n"
    "s_add_u32 s58, s59, 28672\n"
    "s_mov_b32 s44, s59\n"
    "buffer_load_dwordx4 v[48:51], v236, %[ws], s44 offen offset:0 sc1\n"
    "v_mfma_f32_32x32x16_bf16 a[224:239], v[0:3], a[64:67], a[224:239]\n"
    "v_mfma_f32_32x32x16_bf16 a[240:255], v[0:3], a[176:179], a[240:255]\n"
    "buffer_load_dwordx4 v[52:55], v236, %[ws], s44 offen offset:1024 sc1\n"
    "v_mfma_f32_32x32x16_bf16 a[224:239], v[4:7], a[68:71], a[224:239]\n"
    "v_mfma_f32_32x32x16_bf16 a[240:255], v[4:7], a[180:183], a[240:255]\n"
    "buffer_load_dwordx4 v[56:59], v236, %[ws], s44 offen offset:2048 sc1\n"
    "v_mfma_f32_32x32x16_bf16 a[224:239], v[8:11], a[72:75], a[224:239]\n"
    "v_mfma_f32_32x32x16_bf16 a[240:255], v[8:11], a[184:187], a[240:255]\n"
    "buffer_load_dwordx4 v[60:63], v236, %[ws], s44 offen offset:3072 sc1\n"
    "v_mfma_f32_32x32x16_bf16 a[224:239], v[12:15], a[76:79], a[224:239]\n"
    "v_mfma_f32_32x32x16_bf16 a[240:255], v[12:15], a[188:191], a[240:255]\n"
    "s_add_u32 s44, s44, 4096\n"
    "buffer_load_dwordx4 v[0:3], v236, %[ws], s44 offen offset:0 sc1\n"
    "v_mfma_f32_32x32x16_bf16 a[224:239], v[16:19], a[80:83], a[224:239]\n"
    "v_mfma_f32_32x32x16_bf16 a[240:255], v[16:19], a[192:195], a[240:255]\n"
    "buffer_load_dwordx4 v[4:7], v236, %[ws], s44 offen offset:1024 sc1\n"
    "v_mfma_f32_32x32x16_bf16 a[224:239], v[20:23], a[84:87], a[224:239]\n"
    "v_mfma_f32_32x32x16_bf16 a[240:255], v[20:23], a[196:199], a[240:255]\n"
    "buffer_load_dwordx4 v[8:11], v236, %[ws], s44 offen offset:2048 sc1\n"
    "v_mfma_f32_32x32x16_bf16 a[224:239], v[24:27], a[88:91], a[224:239]\n"
    "v_mfma_f32_32x32x16_bf16 a[240:255], v[24:27], a[200:203], a[240:255]\n"
    "buffer_load_dwordx4 v[12:15], v236, %[ws], s44 offen offset:3072 sc1\n"
    "v_mfma_f32_32x32x16_bf16 a[224:239], v[28:31], a[92:95], a[224:239]\n"
    "v_mfma_f32_32x32x16_bf16 a[240:255], v[28:31], a[204:207], a[240:255]\n"
    "s_add_u32 s44, s44, 4096\n"
    "buffer_load_dwordx4 v[16:19], v236, %[ws], s44 offen offset:0 sc1\n"
    "v_mfma_f32_32x32x16_bf16 a[224:239], v[32:35], a[96:99], a[224:239]\n"
    "v_mfma_f32_32x32x16_bf16 a[240:255], v[32:35], a[208:211], a[240:255]\n"
    "buffer_load_dwordx4 v[20:23], v236, %[ws], s44 offen offset:1024 sc1\n"
    "s_waitcnt vmcnt(12)\n"
    "v_mfma_f32_32x32x16_bf16 a[224:239], v[36:39], a[100:103], a[224:239]\n"
    "v_mfma_f32_32x32x16_bf16 a[240:255], v[36:39], a[212:215], a[240:255]\n"
    "buffer_load_dwordx4 v[24:27], v236, %[ws], s44 offen offset:2048 sc1\n"
    "s_waitcnt vmcnt(12)\n"
    "v_mfma_f32_32x32x16_bf16 a[224:239], v[40:43], a[104:107], a[224:239]\n"
    "v_mfma_f32_32x32x16_bf16 a[240:255], v[40:43], a[216:219], a[240:255]\n"
    "buffer_load_dwordx4 v[28:31], v236, %[ws], s44 offen offset:3072 sc1\n"
    "s_waitcnt vmcnt(12)\n"
    "v_mfma_f32_32x32x16_bf16 a[224:239], v[44:47], a[108:111], a[224:239]\n"
    "v_mfma_f32_32x32x16_bf16 a[240:255], v[44:47], a[220:223], a[240:255]\n"
    "s_add_u32 s44, s44, 4096\n"
    "buffer_load_dwordx4 v[32:35], v236, %[ws], s44 offen offset:0 sc1\n"
    "s_waitcnt vmcnt(12)\n"
    "v_mfma_f32_32x32x16_bf16 v[64:79], v[48:51], a[0:3], 0\n"
    "v_mfma_f32_32x32x16_bf16 v[80:95], v[48:51], a[112:115], 0\n"
    "buffer_load_dwordx4 v[36:39], v236, %[ws], s44 offen offset:1024 sc1\n"
    "s_waitcnt vmcnt(12)\n"
    "v_mfma_f32_32x32x16_bf16 v[64:79], v[52:55], a[4:7], v[64:79]\n"
    "v_mfma_f32_32x32x16_bf16 v[80:95], v[52:55], a[116:119], v[80:95]\n"
    "buffer_load_dwordx4 v[40:43], v236, %[ws], s44 offen offset:2048 sc1\n"
    "s_waitcnt vmcnt(12)\n"
    "v_mfma_f32_32x32x16_bf16 v[64:79], v[56:59], a[8:11], v[64:79]\n"
    "ds_write_b128 v248, a[224:227] offset:0\n"
    "v_mfma_f32_32x32x16_bf16 v[80:95], v[56:59], a[120:123], v[80:95]\n"
    "ds_write_b128 v248, a[228:231] offset:32\n"
    "buffer_load_dwordx4 v[44:47], v236, %[ws], s44 offen offset:3072 sc1\n"
    "s_waitcnt vmcnt(12)\n"
    "v_mfma_f32_32x32x16_bf16 v[64:79], v[60:63], a[12:15], v[64:79]\n"
    "ds_write_b128 v248, a[232:235] offset:64\n"
    "v_mfma_f32_32x32x16_bf16 v[80:95], v[60:63], a[124:127], v[80:95]\n"
    "ds_write_b128 v248, a[236:239] offset:96\n"
    "s_add_u32 s44, s44, 4096\n"
    "buffer_load_dwordx4 v[48:51], v236, %[ws], s44 offen offset:0 sc1\n"
    "s_waitcnt vmcnt(12)\n"
    "v_mfma_f32_32x32x16_bf16 v[64:79], v[0:3], a[16:19], v[64:79]\n"
    "ds_write_b128 v248, a[240:243] offset:4608\n"
    "v_mfma_f32_32x32x16_bf16 v[80:95], v[0:3], a[128:131], v[80:95]\n"
    "ds_write_b128 v248, a[244:247] offset:4640\n"
    "buffer_load_dwordx4 v[52:55], v236, %[ws], s44 offen offset:1024 sc1\n"
    "s_waitcnt vmcnt(12)\n"
    "v_mfma_f32_32x32x16_bf16 v[64:79], v[4:7], a[20:23], v[64:79]\n"
    "ds_write_b128 v248, a[248:251] offset:4672\n"
    "v_mfma_f32_32x32x16_bf16 v[80:95], v[4:7], a[132:135], v[80:95]\n"
    "ds_write_b128 v248, a[252:255] offset:4704\n"
    "buffer_load_dwordx4 v[56:59], v236, %[ws], s44 offen offset:2048 sc1\n"
    "s_waitcnt vmcnt(12)\n"
    "v_mfma_f32_32x32x16_bf16 v[64:79], v[8:11], a[24:27], v[64:79]\n"
    "v_mfma_f32_32x32x16_bf16 v[80:95], v[8:11], a[136:139], v[80:95]\n"
    "buffer_load_dwordx4 v[60:63], v236, %[ws], s44 offen offset:3072 sc1\n"
    "s_waitcnt vmcnt(12)\n"
    "v_mfma_f32_32x32x16_bf16 v[64:79], v[12:15], a[28:31], v[64:79]\n"
    "v_mfma_f32_32x32x16_bf16 v[80:95], v[12:15], a[140:143], v[80:95]\n"
    "s_add_u32 s44, s44, 4096\n"
    "buffer_load_dwordx4 v[0:3], v236, %[ws], s44 offen offset:0 sc1\n"
    "s_waitcnt vmcnt(12)\n"
    "v_mfma_f32_32x32x16_bf16 v[64:79], v[16:19], a[32:35], v[64:79]\n"
    "v_mfma_f32_32x32x16_bf16 v[80:95], v[16:19], a[144:147], v[80:95]\n"
    "buffer_load_dwordx4 v[4:7], v236, %[ws], s44 offen offset:1024 sc1\n"
    "s_waitcnt vmcnt(12)\n"
    "v_mfma_f32_32x32x16_bf16 v[64:79], v[20:23], a[36:39], v[64:79]\n"
    "v_mfma_f32_32x32x16_bf16 v[80:95], v[20:23], a[148:151], v[80:95]\n"
    "buffer_load_dwordx4 v[8:11], v236, %[ws], s44 offen offset:2048 sc1\n"
    "s_waitcnt vmcnt(12)\n"
    "v_mfma_f32_32x32x16_bf16 v[64:79], v[24:27], a[40:43], v[64:79]\n"
    "v_mfma_f32_32x32x16_bf16 v[80:95], v[24:27], a[152:155], v[80:95]\n"
    "buffer_load_dwordx4 v[12:15], v236, %[ws], s44 offen offset:3072 sc1\n"
    "s_waitcnt vmcnt(12)\n"
    "v_mfma_f32_32x32x16_bf16 v[64:79], v[28:31], a[44:47], v[64:79]\n"
    "v_mfma_f32_32x32x16_bf16 v[80:95], v[28:31], a[156:159], v[80:95]\n"
    "s_add_u32 s44, s44, 4096\n"
    "buffer_load_dwordx4 v[16:19], v236, %[ws], s44 offen offset:0 sc1\n"
    "s_waitcnt vmcnt(12)\n"
    "v_mfma_f32_32x32x16_bf16 v[64:79], v[32:35], a[48:51], v[64:79]\n"
    "v_mfma_f32_32x32x16_bf16 v[80:95], v[32:35], a[160:163], v[80:95]\n"
    "buffer_load_dwordx4 v[20:23], v236, %[ws], s44 offen offset:1024 sc1\n"
    "s_waitcnt vmcnt(12)\n"
    "v_mfma_f32_32x32x16_bf16 v[64:79], v[36:39], a[52:55], v[64:79]\n"
    "v_mfma_f32_32x32x16_bf16 v[80:95], v[36:39], a[164:167], v[80:95]\n"
    "buffer_load_dwordx4 v[24:27], v236, %[ws], s44 offen offset:2048 sc1\n"
    "s_waitcnt vmcnt(12)\n"
    "v_mfma_f32_32x32x16_bf16 v[64:79], v[40:43], a[56:59], v[64:79]\n"
    "v_mfma_f32_32x32x16_bf16 v[80:95], v[40:43], a[168:171], v[80:95]\n"
    "buffer_load_dwordx4 v[28:31], v236, %[ws], s44 offen offset:3072 sc1\n"
    "v_lshlrev_b32 v252, 2, v236\n"
    "s_mov_b64 exec, 7\n"
    "global_load_dword v253, v252, %[ct]\n"
    "s_mov_b64 exec, -1\n"
    "s_waitcnt vmcnt(13)\n"
    "s_nop 2\n"
    "v_mfma_f32_32x32x16_bf16 v[64:79], v[44:47], a[60:63], v[64:79]\n"
    "v_mfma_f32_32x32x16_bf16 v[80:95], v[44:47], a[172:175], v[80:95]\n"
    "s_waitcnt lgkmcnt(0)\n"
    "s_barrier\n"
    "s_waitcnt vmcnt(12)\n"
    "v_mfma_f32_32x32x16_bf16 v[64:79], v[48:51], a[64:67], v[64:79]\n"
    "ds_read_b128 v[96:99], v249 offset:0\n"
    "ds_read_b128 v[100:103], v249 offset:16\n"
    "ds_read_b128 v[104:107], v249 offset:9216\n"
    "ds_read_b128 v[108:111], v249 offset:9232\n"
    "ds_read_b128 v[112:115], v249 offset:18432\n"
    "v_mfma_f32_32x32x16_bf16 v[80:95], v[48:51], a[176:179], v[80:95]\n"
    "ds_read_b128 v[116:119], v249 offset:18448\n"
    "ds_read_b128 v[120:123], v249 offset:27648\n"
    "ds_read_b128 v[124:127], v249 offset:27664\n"
    "s_waitcnt lgkmcnt(6)\n"
    "v_add_f32 v96, v96, v160\n"
    "s_waitcnt vmcnt(11)\n"
    "v_mfma_f32_32x32x16_bf16 v[64:79], v[52:55], a[68:71], v[64:79]\n"
    "v_add_f32 v97, v97, v161\n"
    "v_add_f32 v98, v98, v162\n"
    "v_add_f32 v99, v99, v163\n"
    "v_add_f32 v100, v100, v164\n"
    "v_add_f32 v101, v101, v165\n"
    "v_mfma_f32_32x32x16_bf16 v[80:95], v[52:55], a[180:183], v[80:95]\n"
    "v_add_f32 v102, v102, v166\n"
    "v_add_f32 v103, v103, v167\n"
    "s_waitcnt lgkmcnt(2)\n"
    "v_add_f32 v104, v104, v112\n"
    "v_add_f32 v105, v105, v113\n"
    "s_waitcnt vmcnt(10)\n"
    "v_mfma_f32_32x32x16_bf16 v[64:79], v[56:59], a[72:75], v[64:79]\n"
    "v_add_f32 v106, v106, v114\n"
    "v_add_f32 v107, v107, v115\n"
    "v_add_f32 v108, v108, v116\n"
    "v_add_f32 v109, v109, v117\n"
    "v_add_f32 v110, v110, v118\n"
    "v_mfma_f32_32x32x16_bf16 v[80:95], v[56:59], a[184:187], v[80:95]\n"
    "v_add_f32 v111, v111, v119\n"
    "s_waitcnt lgkmcnt(0)\n"
    "v_add_f32 v96, v96, v120\n"
    "v_add_f32 v97, v97, v121\n"
    "v_add_f32 v98, v98, v122\n"
    "s_waitcnt vmcnt(9)\n"
    "v_mfma_f32_32x32x16_bf16 v[64:79], v[60:63], a[76:79], v[64:79]\n"
    "v_add_f32 v99, v99, v123\n"
    "v_add_f32 v100, v100, v124\n"
    "v_add_f32 v101, v101, v125\n"
    "v_add_f32 v102, v102, v126\n"
    "v_add_f32 v103, v103, v127\n"
    "v_mfma_f32_32x32x16_bf16 v[80:95], v[60:63], a[188:191], v[80:95]\n"
    "v_add_f32 v96, v96, v104\n"
    "v_add_f32 v97, v97, v105\n"
    "v_add_f32 v98, v98, v106\n"
    "v_add_f32 v99, v99, v107\n"
    "v_add_f32 v100, v100, v108\n"
    "s_waitcnt vmcnt(8)\n"
    "v_mfma_f32_32x32x16_bf16 v[64:79], v[0:3], a[80:83], v[64:79]\n"
    "v_add_f32 v101, v101, v109\n"
    "v_add_f32 v102, v102, v110\n"
    "v_add_f32 v103, v103, v111\n"
    "v_cvt_pk_bf16_f32 v104, v96, v97\n"
    "v_cvt_pk_bf16_f32 v105, v98, v99\n"
    "v_mfma_f32_32x32x16_bf16 v[80:95], v[0:3], a[192:195], v[80:95]\n"
    "v_cvt_pk_bf16_f32 v106, v100, v101\n"
    "v_cvt_pk_bf16_f32 v107, v102, v103\n"
    "buffer_store_dwordx4 v[104:107], v250, %[ws], %[xo] offen\n"
    "v_mul_f32 v112, v96, v186\n"
    "v_mul_f32 v113, v97, v186\n"
    "s_waitcnt vmcnt(8)\n"
    "v_mfma_f32_32x32x16_bf16 v[64:79], v[4:7], a[84:87], v[64:79]\n"
    "v_mul_f32 v114, v98, v186\n"
    "v_mul_f32 v115, v99, v186\n"
    "v_mul_f32 v116, v100, v186\n"
    "v_mul_f32 v117, v101, v186\n"
    "v_mul_f32 v118, v102, v186\n"
    "v_mfma_f32_32x32x16_bf16 v[80:95], v[4:7], a[196:199], v[80:95]\n"
    "v_mul_f32 v119, v103, v186\n"
    "v_mul_f32 v112, v112, v96\n"
    "v_mul_f32 v113, v113, v97\n"
    "v_mul_f32 v114, v114, v98\n"
    "v_mul_f32 v115, v115, v99\n"
    "s_waitcnt vmcnt(7)\n"
    "v_mfma_f32_32x32x16_bf16 v[64:79], v[8:11], a[88:91], v[64:79]\n"
    "v_mul_f32 v116, v116, v100\n"
    "v_mul_f32 v117, v117, v101\n"
    "v_mul_f32 v118, v118, v102\n"
    "v_mul_f32 v119, v119, v103\n"
    "v_permlane32_swap_b32 v112, v116\n"
    "v_mfma_f32_32x32x16_bf16 v[80:95], v[8:11], a[200:203], v[80:95]\n"
    "v_permlane32_swap_b32 v113, v117\n"
    "v_permlane32_swap_b32 v114, v118\n"
    "v_permlane32_swap_b32 v115, v119\n"
    "v_add_f32 v112, v112, v116\n"
    "v_add_f32 v113, v113, v117\n"
    "s_waitcnt vmcnt(6)\n"
    "v_mfma_f32_32x32x16_bf16 v[64:79], v[12:15], a[92:95], v[64:79]\n"
    "v_add_f32 v114, v114, v118\n"
    "v_add_f32 v115, v115, v119\n"
    "s_nop 0\n"
    "v_permlane16_swap_b32 v112, v114\n"
    "v_permlane16_swap_b32 v113, v115\n"
    "v_mfma_f32_32x32x16_bf16 v[80:95], v[12:15], a[204:207], v[80:95]\n"
    "v_add_f32 v112, v112, v114\n"
    "v_add_f32 v113, v113, v115\n"
    "v_cndmask_b32_e64 v114, v112, v113, s[48:49]\n"
    "v_cndmask_b32_e64 v115, v113, v112, s[48:49]\n"
    "s_nop 1\n"
    "v_add_f32_dpp v114, v115, v114 row_ror:8 row_mask:0xf bank_mask:0xf\n"
    "s_waitcnt vmcnt(5)\n"
    "v_mfma_f32_32x32x16_bf16 v[64:79], v[16:19], a[96:99], v[64:79]\n"
    "v_add_f32_dpp v114, v114, v114 row_half_mirror row_mask:0xf bank_mask:0xf\n"
    "s_nop 1\n"
    "v_add_f32_dpp v114, v114, v114 quad_perm:[1,0,3,2] row_mask:0xf bank_mask:0xf\n"
    "s_nop 1\n"
    "v_add_f32_dpp v114, v114, v114 quad_perm:[2,3,0,1] row_mask:0xf bank_mask:0xf\n"
    "v_mfma_f32_32x32x16_bf16 v[80:95], v[16:19], a[208:211], v[80:95]\n"
    "v_cvt_pk_bf16_f32 v114, v114, v114\n"
    "buffer_store_short v114, v187, %[ws], %[so] offen offset:0\n"
    "s_waitcnt vmcnt(5)\n"
    "v_mfma_f32_32x32x16_bf16 v[64:79], v[20:23], a[100:103], v[64:79]\n"
    "v_mfma_f32_32x32x16_bf16 v[80:95], v[20:23], a[212:215], v[80:95]\n"
    "s_waitcnt vmcnt(4)\n"
    "v_mfma_f32_32x32x16_bf16 v[64:79], v[24:27], a[104:107], v[64:79]\n"
    "v_mfma_f32_32x32x16_bf16 v[80:95], v[24:27], a[216:219], v[80:95]\n"
    "s_waitcnt vmcnt(3)\n"
    "v_mfma_f32_32x32x16_bf16 v[64:79], v[28:31], a[108:111], v[64:79]\n"
    "v_mfma_f32_32x32x16_bf16 v[80:95], v[28:31], a[220:223], v[80:95]\n"
    "v_subrev_u32 v248, 114688, v248\n"
    "v_subrev_u32 v249, 114688, v249\n"
    "s_nop 8\n"
    "ds_write_b128 v248, v[64:67] offset:0\n"
    "ds_write_b128 v248, v[68:71] offset:32\n"
    "ds_write_b128 v248, v[72:75] offset:64\n"
    "ds_write_b128 v248, v[76:79] offset:96\n"
    "ds_write_b128 v248, v[80:83] offset:4608\n"
    "ds_write_b128 v248, v[84:87] offset:4640\n"
    "ds_write_b128 v248, v[88:91] offset:4672\n"
    "ds_write_b128 v248, v[92:95] offset:4704\n"
    "s_waitcnt lgkmcnt(0)\n"
    "s_barrier\n"
    "ds_read_b128 v[96:99], v249 offset:0\n"
    "ds_read_b128 v[100:103], v249 offset:16\n"
    "ds_read_b128 v[104:107], v249 offset:9216\n"
    "ds_read_b128 v[108:111], v249 offset:9232\n"
    "ds_read_b128 v[112:115], v249 offset:18432\n"
    "ds_read_b128 v[116:119], v249 offset:18448\n"
    "ds_read_b128 v[120:123], v249 offset:27648\n"
    "ds_read_b128 v[124:127], v249 offset:27664\n"
    "s_waitcnt lgkmcnt(6)\n"
    "v_pk_add_f32 v[96:97], v[96:97], v[168:169]\n"
    "v_pk_add_f32 v[98:99], v[98:99], v[170:171]\n"
    "v_pk_add_f32 v[100:101], v[100:101], v[172:173]\n"
    "v_pk_add_f32 v[102:103], v[102:103], v[174:175]\n"
    "s_waitcnt lgkmcnt(2)\n"
    "v_pk_add_f32 v[104:105], v[104:105], v[112:113]\n"
    "v_pk_add_f32 v[106:107], v[106:107], v[114:115]\n"
    "v_pk_add_f32 v[108:109], v[108:109], v[116:117]\n"
    "v_pk_add_f32 v[110:111], v[110:111], v[118:119]\n"
    "s_waitcnt lgkmcnt(0)\n"
    "v_pk_add_f32 v[96:97], v[96:97], v[120:121]\n"
    "v_pk_add_f32 v[98:99], v[98:99], v[122:123]\n"
    "v_pk_add_f32 v[100:101], v[100:101], v[124:125]\n"
    "v_pk_add_f32 v[102:103], v[102:103], v[126:127]\n"
    "v_pk_add_f32 v[96:97], v[96:97], v[104:105]\n"
    "v_pk_add_f32 v[98:99], v[98:99], v[106:107]\n"
    "v_pk_add_f32 v[100:101], v[100:101], v[108:109]\n"
    "v_pk_add_f32 v[102:103], v[102:103], v[110:111]\n"
    "v_cvt_pk_bf16_f32 v104, v96, v97\n"
    "v_cvt_pk_bf16_f32 v105, v98, v99\n"
    "v_cvt_pk_bf16_f32 v106, v100, v101\n"
    "v_cvt_pk_bf16_f32 v107, v102, v103\n"
    "s_add_u32 s56, %[xo], 57344\n"
    "buffer_store_dwordx4 v[104:107], v250, %[ws], s56 offen\n"
    "v_pk_mul_f32 v[112:113], v[96:97], v[186:187] op_sel:[0,0] op_sel_hi:[1,0]\n"
    "v_pk_mul_f32 v[114:115], v[98:99], v[186:187] op_sel:[0,0] op_sel_hi:[1,0]\n"
    "v_pk_mul_f32 v[116:117], v[100:101], v[186:187] op_sel:[0,0] op_sel_hi:[1,0]\n"
    "v_pk_mul_f32 v[118:119], v[102:103], v[186:187] op_sel:[0,0] op_sel_hi:[1,0]\n"
    "v_pk_mul_f32 v[112:113], v[112:113], v[96:97]\n"
    "v_pk_mul_f32 v[114:115], v[114:115], v[98:99]\n"
    "v_pk_mul_f32 v[116:117], v[116:117], v[100:101]\n"
    "v_pk_mul_f32 v[118:119], v[118:119], v[102:103]\n"
    "s_nop 0\n"
    "v_permlane32_swap_b32 v112, v116\n"
    "v_permlane32_swap_b32 v113, v117\n"
    "v_permlane32_swap_b32 v114, v118\n"
    "v_permlane32_swap_b32 v115, v119\n"
    "v_add_f32 v112, v112, v116\n"
    "v_add_f32 v113, v113, v117\n"
    "v_add_f32 v114, v114, v118\n"
    "v_add_f32 v115, v115, v119\n"
    "s_nop 0\n"
    "v_permlane16_swap_b32 v112, v114\n"
    "v_permlane16_swap_b32 v113, v115\n"
    "v_add_f32 v112, v112, v114\n"
    "v_add_f32 v113, v113, v115\n"
    "v_cndmask_b32_e64 v114, v112, v113, s[48:49]\n"
    "v_cndmask_b32_e64 v115, v113, v112, s[48:49]\n"
    "s_nop 1\n"
    "v_add_f32_dpp v114, v115, v114 row_ror:8 row_mask:0xf bank_mask:0xf\n"
    "s_nop 1\n"
    "v_add_f32_dpp v114, v114, v114 row_half_mirror row_mask:0xf bank_mask:0xf\n"
    "s_nop 1\n"
    "v_add_f32_dpp v114, v114, v114 quad_perm:[1,0,3,2] row_mask:0xf bank_mask:0xf\n"
    "s_nop 1\n"
    "v_add_f32_dpp v114, v114, v114 quad_perm:[2,3,0,1] row_mask:0xf bank_mask:0xf\n"
    "v_cvt_pk_bf16_f32 v114, v114, v114\n"
    "buffer_store_short v114, v187, %[ws], %[so] offen offset:128\n"
    "s_branch LE_%=\n"
    "LS0_%=:\n"
    "s_sleep 4\n"
    "buffer_load_dword v133, v131, %[ws], s42 offen sc1\n"
    "s_waitcnt vmcnt(0)\n"
    "v_cmp_gt_u32 vcc, 1, v133\n"
    "s_sub_u32 s46, s46, 1\n"
    "s_cbranch_scc1 LR0_%=\n"
    "s_nop 4\n"
    "s_cbranch_vccnz LS0_%=\n"
    "s_branch LR0_%=\n"
    "LS1_%=:\n"
    "s_sleep 4\n"
    "buffer_load_dword v247, v246, %[ws], %[fr] offen sc1\n"
    "s_waitcnt vmcnt(0)\n"
    "v_cmp_gt_u32 vcc, 1, v247\n"
    "s_sub_u32 s46, s46, 1\n"
    "s_cbranch_scc1 LR1_%=\n"
    "s_nop 4\n"
    "s_cbranch_vccnz LS1_%=\n"
    "s_branch LR1_%=\n"
    "LS2_%=:\n"
    "s_sleep 4\n"
    "buffer_load_dword v247, v246, %[ws], %[fr] offen sc1\n"
    "s_waitcnt vmcnt(0)\n"
    "v_cmp_gt_u32 vcc, 2, v247\n"
    "s_sub_u32 s46, s46, 1\n"
    "s_cbranch_scc1 LR2_%=\n"
    "s_nop 4\n"
    "s_cbranch_vccnz LS2_%=\n"
    "s_branch LR2_%=\n"
    "LE_%=:\n"
    "ilg_end0:\n"
            :: G_OPS, [xo] "s"(xo), [rl] "s"(rl), [so] "s"(so), [mk] "s"(mk), [ct] "s"(ct) : G_CLOB);
    } else {
        const u32x4 out = {(unsigned)(size_t)p.out, (unsigned)((size_t)p.out >> 32) & 0xffffu, (unsigned)(S_ * D_ * 2), 0x00020000u};
        const int oo = (32 * (32 * th + 8 * w) + 2 * pn) * (D_ * 2) + 128 * nt, el = lds + th * TILE_B + 112 * 64 * w, d0 = 64 * nt;
        asm volatile(
    "s_nop 4\n"
    "s_mov_b32 s58, %[p]\n"
    "s_add_u32 s59, %[p], 114688\n"
    "v_mbcnt_lo_u32_b32 v56, -1, 0\n"
    "s_lshl_b32 s40, %[w], 6\n"
    "v_mbcnt_hi_u32_b32 v56, -1, v56\n"
    "s_add_u32 s41, %[sq], 8192\n"
    "v_add_u32 v70, s40, v56\n"
    "v_lshlrev_b32 v58, 4, v70\n"
    "s_add_u32 s42, %[sq], 16384\n"
    "s_add_u32 s43, %[sq], 24576\n"
    "buffer_load_dwordx4 v[8:11], v58, %[ws], %[sq] offen\n"
    "v_add_u32 v60, 4096, v58\n"
    "v_lshrrev_b32 v57, 2, v70\n"
    "buffer_load_dwordx4 v[12:15], v60, %[ws], %[sq] offen\n"
    "v_mul_u32_u24 v57, 28672, v57\n"
    "v_and_b32 v61, 48, v58\n"
    "v_add_u32 v57, v57, v61\n"
    "buffer_load_dwordx4 v[0:3], v57, %[ws], %[zr] offen\n"
    "v_and_b32 v61, 15, v56\n"
    "s_lshl_b32 s44, %[w], 4\n"
    "v_lshrrev_b32 v62, 4, v56\n"
    "v_add_u32 v176, s44, v61\n"
    "v_cvt_f32_ubyte0 v177, v62\n"
    "v_cvt_f32_ubyte0 v176, v176\n"
    "v_lshlrev_b32 v63, 31, v56\n"
    "buffer_load_dwordx4 v[16:19], v58, %[ws], s41 offen\n"
    "v_mul_f32 v178, 0x3c800000, v176\n"
    "v_mul_f32 v179, 0x3e000000, v176\n"
    "v_lshrrev_b32 v71, 16, v63\n"
    "v_mul_f32 v180, v179, v177\n"
    "v_or_b32 v63, v63, v71\n"
    "v_fmamk_f32 v181, v178, 0x3f800000, v180\n"
    "v_fmamk_f32 v182, v178, 0x40000000, v180\n"
    "buffer_load_dwordx4 v[20:23], v60, %[ws], s41 offen\n"
    "v_fmamk_f32 v183, v178, 0x40400000, v180\n"
    "v_fmamk_f32 v184, v178, 0x40800000, v180\n"
    "v_fmamk_f32 v185, v178, 0x40a00000, v180\n"
    "v_fmamk_f32 v186, v178, 0x40c00000, v180\n"
    "v_fmamk_f32 v187, v178, 0x40e00000, v180\n"
    "v_cos_f32 v188, v180\n"
    "buffer_load_dwordx4 v[4:7], v57, %[ws], %[zi] offen\n"
    "v_cos_f32 v189, v181\n"
    "v_sin_f32 v190, v180\n"
    "v_sin_f32 v191, v181\n"
    "v_cvt_pk_bf16_f32 v72, v188, v189\n"
    "buffer_load_dwordx4 v[24:27], v58, %[ws], s42 offen\n"
    "v_cvt_pk_bf16_f32 v80, v190, v191\n"
    "v_cvt_pk_bf16_f32 v88, -v190, -v191\n"
    "v_cos_f32 v188, v182\n"
    "v_cos_f32 v189, v183\n"
    "v_sin_f32 v190, v182\n"
    "buffer_load_dwordx4 v[28:31], v60, %[ws], s42 offen\n"
    "v_sin_f32 v191, v183\n"
    "v_cvt_pk_bf16_f32 v73, v188, v189\n"
    "v_cvt_pk_bf16_f32 v81, v190, v191\n"
    "v_cvt_pk_bf16_f32 v89, -v190, -v191\n"
    "v_cos_f32 v188, v184\n"
    "buffer_load_dwordx4 v[32:35], v58, %[ws], s43 offen\n"
    "v_cos_f32 v189, v185\n"
    "v_sin_f32 v190, v184\n"
    "v_sin_f32 v191, v185\n"
    "v_cvt_pk_bf16_f32 v74, v188, v189\n"
    "buffer_load_dwordx4 v[36:39], v60, %[ws], s43 offen\n"
    "s_lshr_b32 s45, %[w], 1\n"
    "v_lshlrev_b32 v60, 4, v56\n"
    "s_and_b32 s46, %[w], 1\n"
    "v_lshl_add_u32 v59, v61, 1, s45\n"
    "s_lshl_b32 s46, s46, 5\n"
    "v_mul_u32_u24 v59, 28672, v59\n"
    "v_lshl_add_u32 v70, v62, 3, s46\n"
    "v_add_u32 v59, v59, v70\n"
    "v_cvt_pk_bf16_f32 v82, v190, v191\n"
    "v_cvt_pk_bf16_f32 v90, -v190, -v191\n"
    "v_cos_f32 v188, v186\n"
    "v_cos_f32 v189, v187\n"
    "v_sin_f32 v190, v186\n"
    "buffer_load_dwordx4 v[40:43], v60, %[ws], %[wt] offen\n"
    "v_sin_f32 v191, v187\n"
    "v_cvt_pk_bf16_f32 v75, v188, v189\n"
    "v_cvt_pk_bf16_f32 v83, v190, v191\n"
    "v_cvt_pk_bf16_f32 v91, -v190, -v191\n"
    "v_lshrrev_b32 v64, 7, v58\n"
    "v_lshrrev_b32 v70, 4, v58\n"
    "buffer_load_dwordx4 v[44:47], v60, %[ws], %[wt] offen offset:1024\n"
    "v_lshrrev_b32 v65, 8, v58\n"
    "v_xor_b32 v70, v70, v65\n"
    "v_and_b32 v70, 7, v70\n"
    "v_lshlrev_b32 v64, 7, v64\n"
    "v_lshl_add_u32 v64, v70, 4, v64\n"
    "v_add_u32 v64, %[lds], v64\n"
    "v_lshrrev_b32 v70, 1, v61\n"
    "v_lshlrev_b32 v66, 7, v61\n"
    "v_xor_b32 v65, v62, v70\n"
    "v_add_u32 v66, %[lds], v66\n"
    "s_lshl_b32 s49, %[w], 1\n"
    "v_lshrrev_b32 v67, 1, v62\n"
    "v_lshl_add_u32 v65, v65, 4, v66\n"
    "v_add_u32 v67, s49, v67\n"
    "v_xor_b32 v67, v67, v70\n"
    "v_lshl_add_u32 v67, v67, 4, v66\n"
    "v_and_b32 v70, 1, v62\n"
    "v_xor_b32 v66, 64, v65\n"
    "v_lshl_add_u32 v67, v70, 3, v67\n"
    "v_mov_b32 v69, 0x3f803f80\n"
    "s_mov_b32 s50, 0x3b92ccf7\n"
    "v_xor_b32 v76, v63, v72\n"
    "v_xor_b32 v84, v63, v80\n"
    "v_xor_b32 v92, v63, v88\n"
    "v_xor_b32 v77, v63, v73\n"
    "v_xor_b32 v85, v63, v81\n"
    "v_xor_b32 v93, v63, v89\n"
    "v_xor_b32 v78, v63, v74\n"
    "v_xor_b32 v86, v63, v82\n"
    "v_xor_b32 v94, v63, v90\n"
    "v_xor_b32 v79, v63, v75\n"
    "v_xor_b32 v87, v63, v83\n"
    "v_xor_b32 v95, v63, v91\n"
    "v_mul_f32 v178, 0x3a000000, v176\n"
    "v_mul_f32 v179, 0x3b000000, v176\n"
    "v_mul_f32 v180, v179, v177\n"
    "v_fmamk_f32 v181, v178, 0x3f800000, v180\n"
    "v_fmamk_f32 v182, v178, 0x40000000, v180\n"
    "v_fmamk_f32 v183, v178, 0x40400000, v180\n"
    "v_fmamk_f32 v184, v178, 0x41800000, v180\n"
    "v_fmamk_f32 v185, v178, 0x41880000, v180\n"
    "v_fmamk_f32 v186, v178, 0x41900000, v180\n"
    "v_fmamk_f32 v187, v178, 0x41980000, v180\n"
    "v_cos_f32 v120, v180\n"
    "v_sin_f32 v128, v180\n"
    "v_cos_f32 v121, v181\n"
    "v_sin_f32 v129, v181\n"
    "v_cos_f32 v122, v182\n"
    "v_sin_f32 v130, v182\n"
    "v_cos_f32 v123, v183\n"
    "v_sin_f32 v131, v183\n"
    "v_cos_f32 v124, v184\n"
    "v_sin_f32 v132, v184\n"
    "v_cos_f32 v125, v185\n"
    "v_sin_f32 v133, v185\n"
    "v_cos_f32 v126, v186\n"
    "v_sin_f32 v134, v186\n"
    "v_cos_f32 v127, v187\n"
    "v_sin_f32 v135, v187\n"
    "v_cvt_f32_ubyte0 v178, v61\n"
    "v_xor_b32 v71, 0x80000000, v63\n"
    "v_mul_f32 v179, 0x3e000000, v178\n"
    "v_mul_f32 v192, 0x3d000000, v178\n"
    "v_mul_f32 v180, v179, v177\n"
    "v_fmamk_f32 v181, v192, 0x3f800000, v180\n"
    "v_fmamk_f32 v182, v192, 0x40000000, v180\n"
    "v_fmamk_f32 v183, v192, 0x40400000, v180\n"
    "v_cos_f32 v188, v180\n"
    "v_cos_f32 v189, v181\n"
    "v_sin_f32 v190, v180\n"
    "v_sin_f32 v191, v181\n"
    "v_cvt_pk_bf16_f32 v96, v188, v189\n"
    "v_cvt_pk_bf16_f32 v104, v190, v191\n"
    "v_cvt_pk_bf16_f32 v112, -v190, -v191\n"
    "v_cos_f32 v188, v182\n"
    "v_cos_f32 v189, v183\n"
    "v_sin_f32 v190, v182\n"
    "v_sin_f32 v191, v183\n"
    "v_cvt_pk_bf16_f32 v97, v188, v189\n"
    "v_cvt_pk_bf16_f32 v105, v190, v191\n"
    "v_cvt_pk_bf16_f32 v113, -v190, -v191\n"
    "v_xor_b32 v98, v63, v96\n"
    "v_xor_b32 v100, 0x80000000, v96\n"
    "v_xor_b32 v102, v71, v96\n"
    "v_xor_b32 v99, v63, v97\n"
    "v_xor_b32 v101, 0x80000000, v97\n"
    "v_xor_b32 v103, v71, v97\n"
    "v_xor_b32 v106, v63, v104\n"
    "v_xor_b32 v108, 0x80000000, v104\n"
    "v_xor_b32 v110, v71, v104\n"
    "v_xor_b32 v107, v63, v105\n"
    "v_xor_b32 v109, 0x80000000, v105\n"
    "v_xor_b32 v111, v71, v105\n"
    "v_xor_b32 v114, v63, v112\n"
    "v_xor_b32 v116, 0x80000000, v112\n"
    "v_xor_b32 v118, v71, v112\n"
    "v_xor_b32 v115, v63, v113\n"
    "v_xor_b32 v117, 0x80000000, v113\n"
    "v_xor_b32 v119, v71, v113\n"
    "v_cvt_f32_u32 v179, s44\n"
    "v_mul_f32 v192, 0x3a000000, v178\n"
    "v_fmamk_f32 v179, v177, 0x40800000, v179\n"
    "v_add_f32 v193, 0x3c000000, v192\n"
    "v_mov_b32 v194, 0x3c2953fd\n"
    "v_mul_f32 v180, v179, v192\n"
    "v_mul_f32 v184, v179, v193\n"
    "v_fmamk_f32 v181, v192, 0x3f800000, v180\n"
    "v_fmamk_f32 v182, v192, 0x40000000, v180\n"
    "v_fmamk_f32 v183, v192, 0x40400000, v180\n"
    "v_fmamk_f32 v185, v193, 0x3f800000, v184\n"
    "v_fmamk_f32 v186, v193, 0x40000000, v184\n"
    "v_fmamk_f32 v187, v193, 0x40400000, v184\n"
    "v_cos_f32 v136, v180\n"
    "v_sin_f32 v144, v180\n"
    "v_cos_f32 v137, v181\n"
    "v_sin_f32 v145, v181\n"
    "v_cos_f32 v138, v182\n"
    "v_sin_f32 v146, v182\n"
    "v_cos_f32 v139, v183\n"
    "v_sin_f32 v147, v183\n"
    "v_cos_f32 v140, v184\n"
    "v_sin_f32 v148, v184\n"
    "v_cos_f32 v141, v185\n"
    "v_sin_f32 v149, v185\n"
    "v_cos_f32 v142, v186\n"
    "v_sin_f32 v150, v186\n"
    "v_cos_f32 v143, v187\n"
    "v_sin_f32 v151, v187\n"
    "v_pk_mul_f32 v[136:137], v[136:137], v[194:195] op_sel_hi:[1,0]\n"
    "v_pk_mul_f32 v[138:139], v[138:139], v[194:195] op_sel_hi:[1,0]\n"
    "v_pk_mul_f32 v[140:141], v[140:141], v[194:195] op_sel_hi:[1,0]\n"
    "v_pk_mul_f32 v[142:143], v[142:143], v[194:195] op_sel_hi:[1,0]\n"
    "v_pk_mul_f32 v[144:145], v[144:145], v[194:195] op_sel_hi:[1,0]\n"
    "v_pk_mul_f32 v[146:147], v[146:147], v[194:195] op_sel_hi:[1,0]\n"
    "v_pk_mul_f32 v[148:149], v[148:149], v[194:195] op_sel_hi:[1,0]\n"
    "v_pk_mul_f32 v[150:151], v[150:151], v[194:195] op_sel_hi:[1,0]\n"
    "s_waitcnt vmcnt(10)\n"
    "v_dot2_f32_bf16 v168, v8, v69, s50\n"
    "v_dot2_f32_bf16 v169, v9, v69, s50\n"
    "v_dot2_f32_bf16 v170, v10, v69, s50\n"
    "v_dot2_f32_bf16 v171, v11, v69, s50\n"
    "v_dot2_f32_bf16 v172, v12, v69, s50\n"
    "v_dot2_f32_bf16 v173, v13, v69, s50\n"
    "v_dot2_f32_bf16 v174, v14, v69, s50\n"
    "v_dot2_f32_bf16 v175, v15, v69, s50\n"
    "s_waitcnt vmcnt(9)\n"
    "ds_write_b128 v64, v[0:3] offset:16384\n"
    "v_lshlrev_b32 v192, 16, v0\n"
    "v_and_b32 v193, 0xffff0000, v0\n"
    "v_lshlrev_b32 v194, 16, v1\n"
    "v_and_b32 v195, 0xffff0000, v1\n"
    "v_lshlrev_b32 v196, 16, v2\n"
    "v_and_b32 v197, 0xffff0000, v2\n"
    "v_lshlrev_b32 v198, 16, v3\n"
    "v_and_b32 v199, 0xffff0000, v3\n"
    "s_waitcnt vmcnt(7)\n"
    "v_dot2c_f32_bf16 v168, v16, v69\n"
    "v_dot2c_f32_bf16 v169, v17, v69\n"
    "v_dot2c_f32_bf16 v170, v18, v69\n"
    "v_dot2c_f32_bf16 v171, v19, v69\n"
    "v_dot2c_f32_bf16 v172, v20, v69\n"
    "v_dot2c_f32_bf16 v173, v21, v69\n"
    "v_dot2c_f32_bf16 v174, v22, v69\n"
    "v_dot2c_f32_bf16 v175, v23, v69\n"
    "s_waitcnt vmcnt(6)\n"
    "ds_write_b128 v64, v[4:7] offset:20480\n"
    "v_lshlrev_b32 v200, 16, v4\n"
    "v_and_b32 v201, 0xffff0000, v4\n"
    "v_lshlrev_b32 v202, 16, v5\n"
    "v_and_b32 v203, 0xffff0000, v5\n"
    "v_lshlrev_b32 v204, 16, v6\n"
    "v_and_b32 v205, 0xffff0000, v6\n"
    "v_lshlrev_b32 v206, 16, v7\n"
    "v_and_b32 v207, 0xffff0000, v7\n"
    "s_waitcnt vmcnt(4)\n"
    "v_dot2c_f32_bf16 v168, v24, v69\n"
    "v_dot2c_f32_bf16 v169, v25, v69\n"
    "v_dot2c_f32_bf16 v170, v26, v69\n"
    "v_dot2c_f32_bf16 v171, v27, v69\n"
    "v_dot2c_f32_bf16 v172, v28, v69\n"
    "v_dot2c_f32_bf16 v173, v29, v69\n"
    "v_dot2c_f32_bf16 v174, v30, v69\n"
    "v_dot2c_f32_bf16 v175, v31, v69\n"
    "s_waitcnt vmcnt(2)\n"
    "v_dot2c_f32_bf16 v168, v32, v69\n"
    "v_dot2c_f32_bf16 v169, v33, v69\n"
    "v_dot2c_f32_bf16 v170, v34, v69\n"
    "v_dot2c_f32_bf16 v171, v35, v69\n"
    "v_dot2c_f32_bf16 v172, v36, v69\n"
    "v_dot2c_f32_bf16 v173, v37, v69\n"
    "v_dot2c_f32_bf16 v174, v38, v69\n"
    "v_dot2c_f32_bf16 v175, v39, v69\n"
    "v_mov_b32 v68, %[keep]\n"
    "v_rsq_f32 v168, v168\n"
    "v_rsq_f32 v169, v169\n"
    "v_rsq_f32 v170, v170\n"
    "v_rsq_f32 v171, v171\n"
    "v_rsq_f32 v172, v172\n"
    "v_rsq_f32 v173, v173\n"
    "v_rsq_f32 v174, v174\n"
    "v_rsq_f32 v175, v175\n"
    "v_pk_mul_f32 v[192:193], v[192:193], v[168:169]\n"
    "v_pk_mul_f32 v[194:195], v[194:195], v[170:171]\n"
    "v_pk_mul_f32 v[196:197], v[196:197], v[172:173]\n"
    "v_pk_mul_f32 v[198:199], v[198:199], v[174:175]\n"
    "v_cvt_pk_bf16_f32 v208, v192, v193\n"
    "v_cvt_pk_bf16_f32 v209, v194, v195\n"
    "v_cvt_pk_bf16_f32 v210, v196, v197\n"
    "v_cvt_pk_bf16_f32 v211, v198, v199\n"
    "ds_write_b128 v64, v[208:211]\n"
    "v_pk_mul_f32 v[200:201], v[200:201], v[168:169]\n"
    "v_pk_mul_f32 v[202:203], v[202:203], v[170:171]\n"
    "v_pk_mul_f32 v[204:205], v[204:205], v[172:173]\n"
    "v_pk_mul_f32 v[206:207], v[206:207], v[174:175]\n"
    "v_cvt_pk_bf16_f32 v212, v200, v201\n"
    "v_cvt_pk_bf16_f32 v213, v202, v203\n"
    "v_cvt_pk_bf16_f32 v214, v204, v205\n"
    "v_cvt_pk_bf16_f32 v215, v206, v207\n"
    "v_and_b32 v212, v68, v212\n"
    "v_and_b32 v213, v68, v213\n"
    "v_and_b32 v214, v68, v214\n"
    "v_and_b32 v215, v68, v215\n"
    "ds_write_b128 v64, v[212:215] offset:4096\n"
    "s_waitcnt lgkmcnt(0)\n"
    "s_barrier\n"
    "ds_read_b128 v[208:211], v65 offset:0\n"
    "ds_read_b128 v[212:215], v66 offset:0\n"
    "ds_read_b128 v[216:219], v65 offset:4096\n"
    "ds_read_b128 v[220:223], v66 offset:4096\n"
    "ds_read_b128 v[224:227], v65 offset:2048\n"
    "ds_read_b128 v[228:231], v66 offset:2048\n"
    "ds_read_b128 v[232:235], v65 offset:6144\n"
    "ds_read_b128 v[236:239], v66 offset:6144\n"
    "s_waitcnt vmcnt(0)\n"
    "v_mul_u32_u24 v70, 128, v56\n"
    "buffer_load_dword a224, v70, %[ws], %[pq] offen\n"
    "v_lshlrev_b32 v152, 16, v40\n"
    "v_and_b32 v160, 0xffff0000, v40\n"
    "v_lshlrev_b32 v153, 16, v41\n"
    "v_and_b32 v161, 0xffff0000, v41\n"
    "v_lshlrev_b32 v154, 16, v42\n"
    "v_and_b32 v162, 0xffff0000, v42\n"
    "v_lshlrev_b32 v155, 16, v43\n"
    "v_and_b32 v163, 0xffff0000, v43\n"
    "v_lshlrev_b32 v156, 16, v44\n"
    "v_and_b32 v164, 0xffff0000, v44\n"
    "v_lshlrev_b32 v157, 16, v45\n"
    "v_and_b32 v165, 0xffff0000, v45\n"
    "v_lshlrev_b32 v158, 16, v46\n"
    "v_and_b32 v166, 0xffff0000, v46\n"
    "v_lshlrev_b32 v159, 16, v47\n"
    "v_and_b32 v167, 0xffff0000, v47\n"
    "s_waitcnt lgkmcnt(7)\n"
    "v_mfma_f32_16x16x32_bf16 v[8:11], v[208:211], v[72:75], 0\n"
    "v_mfma_f32_16x16x32_bf16 v[16:19], v[208:211], v[88:91], 0\n"
    "s_waitcnt lgkmcnt(6)\n"
    "v_mfma_f32_16x16x32_bf16 v[8:11], v[212:215], v[76:79], v[8:11]\n"
    "v_mfma_f32_16x16x32_bf16 v[16:19], v[212:215], v[92:95], v[16:19]\n"
    "s_waitcnt lgkmcnt(5)\n"
    "v_mfma_f32_16x16x32_bf16 v[8:11], v[216:219], v[80:83], v[8:11]\n"
    "v_mfma_f32_16x16x32_bf16 v[16:19], v[216:219], v[72:75], v[16:19]\n"
    "s_waitcnt lgkmcnt(4)\n"
    "v_mfma_f32_16x16x32_bf16 v[8:11], v[220:223], v[84:87], v[8:11]\n"
    "v_mfma_f32_16x16x32_bf16 v[16:19], v[220:223], v[76:79], v[16:19]\n"
    "s_waitcnt lgkmcnt(3)\n"
    "v_mfma_f32_16x16x32_bf16 v[12:15], v[224:227], v[72:75], 0\n"
    "v_mfma_f32_16x16x32_bf16 v[20:23], v[224:227], v[88:91], 0\n"
    "s_nop 3\n"
    "v_pk_mul_f32 v[196:197], v[8:9], v[120:121]\n"
    "s_waitcnt lgkmcnt(2)\n"
    "s_nop 0\n"
    "v_mfma_f32_16x16x32_bf16 v[12:15], v[228:231], v[76:79], v[12:15]\n"
    "v_mfma_f32_16x16x32_bf16 v[20:23], v[228:231], v[92:95], v[20:23]\n"
    "v_pk_mul_f32 v[200:201], v[16:17], v[120:121]\n"
    "v_pk_mul_f32 v[198:199], v[10:11], v[122:123]\n"
    "v_pk_mul_f32 v[202:203], v[18:19], v[122:123]\n"
    "v_pk_fma_f32 v[196:197], v[16:17], v[128:129], v[196:197]\n"
    "s_waitcnt lgkmcnt(1)\n"
    "s_nop 0\n"
    "v_mfma_f32_16x16x32_bf16 v[12:15], v[232:235], v[80:83], v[12:15]\n"
    "v_mfma_f32_16x16x32_bf16 v[20:23], v[232:235], v[72:75], v[20:23]\n"
    "v_pk_fma_f32 v[200:201], v[8:9], v[128:129], v[200:201] neg_lo:[1,0,0] neg_hi:[1,0,0]\n"
    "v_pk_fma_f32 v[198:199], v[18:19], v[130:131], v[198:199]\n"
    "v_pk_fma_f32 v[202:203], v[10:11], v[130:131], v[202:203] neg_lo:[1,0,0] neg_hi:[1,0,0]\n"
    "v_cvt_pk_bf16_f32 v24, v196, v197\n"
    "s_waitcnt lgkmcnt(0)\n"
    "v_mfma_f32_16x16x32_bf16 v[12:15], v[236:239], v[84:87], v[12:15]\n"
    "v_mfma_f32_16x16x32_bf16 v[20:23], v[236:239], v[76:79], v[20:23]\n"
    "v_cvt_pk_bf16_f32 v28, v200, v201\n"
    "v_cvt_pk_bf16_f32 v25, v198, v199\n"
    "v_cvt_pk_bf16_f32 v29, v202, v203\n"
    "s_nop 3\n"
    "v_pk_mul_f32 v[196:197], v[12:13], v[124:125]\n"
    "v_pk_mul_f32 v[200:201], v[20:21], v[124:125]\n"
    "v_pk_mul_f32 v[198:199], v[14:15], v[126:127]\n"
    "v_pk_mul_f32 v[202:203], v[22:23], v[126:127]\n"
    "v_pk_fma_f32 v[196:197], v[20:21], v[132:133], v[196:197]\n"
    "v_pk_fma_f32 v[200:201], v[12:13], v[132:133], v[200:201] neg_lo:[1,0,0] neg_hi:[1,0,0]\n"
    "v_pk_fma_f32 v[198:199], v[22:23], v[134:135], v[198:199]\n"
    "v_pk_fma_f32 v[202:203], v[14:15], v[134:135], v[202:203] neg_lo:[1,0,0] neg_hi:[1,0,0]\n"
    "v_cvt_pk_bf16_f32 v26, v196, v197\n"
    "v_cvt_pk_bf16_f32 v30, v200, v201\n"
    "v_cvt_pk_bf16_f32 v27, v198, v199\n"
    "v_cvt_pk_bf16_f32 v31, v202, v203\n"
    "s_nop 0\n"
    "v_mfma_f32_16x16x32_bf16 v[8:11], v[96:99], v[24:27], 0\n"
    "v_mfma_f32_16x16x32_bf16 v[16:19], v[96:99], v[28:31], 0\n"
    "v_mfma_f32_16x16x32_bf16 v[8:11], v[104:107], v[28:31], v[8:11]\n"
    "v_mfma_f32_16x16x32_bf16 v[16:19], v[112:115], v[24:27], v[16:19]\n"
    "v_mfma_f32_16x16x32_bf16 v[12:15], v[100:103], v[24:27], 0\n"
    "v_mfma_f32_16x16x32_bf16 v[20:23], v[100:103], v[28:31], 0\n"
    "v_mfma_f32_16x16x32_bf16 v[12:15], v[108:111], v[28:31], v[12:15]\n"
    "v_mfma_f32_16x16x32_bf16 v[20:23], v[116:119], v[24:27], v[20:23]\n"
    "s_nop 2\n"
    "v_pk_mul_f32 v[196:197], v[8:9], v[152:153]\n"
    "v_pk_mul_f32 v[200:201], v[16:17], v[152:153]\n"
    "v_pk_mul_f32 v[198:199], v[10:11], v[154:155]\n"
    "v_pk_mul_f32 v[202:203], v[18:19], v[154:155]\n"
    "v_pk_fma_f32 v[196:197], v[16:17], v[160:161], v[196:197] neg_lo:[1,0,0] neg_hi:[1,0,0]\n"
    "v_pk_fma_f32 v[200:201], v[8:9], v[160:161], v[200:201]\n"
    "v_pk_fma_f32 v[198:199], v[18:19], v[162:163], v[198:199] neg_lo:[1,0,0] neg_hi:[1,0,0]\n"
    "v_pk_fma_f32 v[202:203], v[10:11], v[162:163], v[202:203]\n"
    "v_cvt_pk_bf16_f32 v32, v196, v197\n"
    "v_cvt_pk_bf16_f32 v36, v200, v201\n"
    "v_cvt_pk_bf16_f32 v33, v198, v199\n"
    "v_cvt_pk_bf16_f32 v37, v202, v203\n"
    "v_pk_mul_f32 v[188:189], v[12:13], v[156:157]\n"
    "v_pk_mul_f32 v[192:193], v[20:21], v[156:157]\n"
    "v_pk_mul_f32 v[190:191], v[14:15], v[158:159]\n"
    "v_pk_mul_f32 v[194:195], v[22:23], v[158:159]\n"
    "v_pk_fma_f32 v[188:189], v[20:21], v[164:165], v[188:189] neg_lo:[1,0,0] neg_hi:[1,0,0]\n"
    "v_pk_fma_f32 v[192:193], v[12:13], v[164:165], v[192:193]\n"
    "v_pk_fma_f32 v[190:191], v[22:23], v[166:167], v[190:191] neg_lo:[1,0,0] neg_hi:[1,0,0]\n"
    "v_pk_fma_f32 v[194:195], v[14:15], v[166:167], v[194:195]\n"
    "v_cvt_pk_bf16_f32 v34, v188, v189\n"
    "v_cvt_pk_bf16_f32 v38, v192, v193\n"
    "v_cvt_pk_bf16_f32 v35, v190, v191\n"
    "v_cvt_pk_bf16_f32 v39, v194, v195\n"
    "ds_read2st64_b64 v[48:51], v67 offset0:32 offset1:40\n"
    "ds_read2st64_b64 v[52:55], v67 offset0:36 offset1:44\n"
    "v_mfma_f32_16x16x32_bf16 v[8:11], v[32:35], v[96:99], 0\n"
    "v_mfma_f32_16x16x32_bf16 v[16:19], v[36:39], v[96:99], 0\n"
    "v_mfma_f32_16x16x32_bf16 v[8:11], v[36:39], v[112:115], v[8:11]\n"
    "v_mfma_f32_16x16x32_bf16 v[16:19], v[32:35], v[104:107], v[16:19]\n"
    "v_mfma_f32_16x16x32_bf16 v[12:15], v[32:35], v[100:103], 0\n"
    "v_mfma_f32_16x16x32_bf16 v[20:23], v[36:39], v[100:103], 0\n"
    "v_mfma_f32_16x16x32_bf16 v[12:15], v[36:39], v[116:119], v[12:15]\n"
    "v_mfma_f32_16x16x32_bf16 v[20:23], v[32:35], v[108:111], v[20:23]\n"
    "s_nop 2\n"
    "v_pk_mul_f32 v[196:197], v[8:9], v[136:137]\n"
    "v_pk_mul_f32 v[200:201], v[16:17], v[136:137]\n"
    "v_pk_mul_f32 v[198:199], v[10:11], v[138:139]\n"
    "v_pk_mul_f32 v[202:203], v[18:19], v[138:139]\n"
    "v_pk_fma_f32 v[196:197], v[16:17], v[144:145], v[196:197] neg_lo:[1,0,0] neg_hi:[1,0,0]\n"
    "v_pk_fma_f32 v[200:201], v[8:9], v[144:145], v[200:201]\n"
    "v_pk_fma_f32 v[198:199], v[18:19], v[146:147], v[198:199] neg_lo:[1,0,0] neg_hi:[1,0,0]\n"
    "v_pk_fma_f32 v[202:203], v[10:11], v[146:147], v[202:203]\n"
    "v_cvt_pk_bf16_f32 v24, v196, v197\n"
    "v_cvt_pk_bf16_f32 v26, v200, v201\n"
    "v_cvt_pk_bf16_f32 v25, v198, v199\n"
    "v_cvt_pk_bf16_f32 v27, v202, v203\n"
    "ds_write2st64_b64 v67, v[24:25], v[26:27] offset0:16 offset1:24\n"
    "v_pk_mul_f32 v[188:189], v[12:13], v[140:141]\n"
    "v_pk_mul_f32 v[192:193], v[20:21], v[140:141]\n"
    "v_pk_mul_f32 v[190:191], v[14:15], v[142:143]\n"
    "v_pk_mul_f32 v[194:195], v[22:23], v[142:143]\n"
    "v_pk_fma_f32 v[188:189], v[20:21], v[148:149], v[188:189] neg_lo:[1,0,0] neg_hi:[1,0,0]\n"
    "v_pk_fma_f32 v[192:193], v[12:13], v[148:149], v[192:193]\n"
    "v_pk_fma_f32 v[190:191], v[22:23], v[150:151], v[190:191] neg_lo:[1,0,0] neg_hi:[1,0,0]\n"
    "v_pk_fma_f32 v[194:195], v[14:15], v[150:151], v[194:195]\n"
    "v_cvt_pk_bf16_f32 v28, v188, v189\n"
    "v_cvt_pk_bf16_f32 v30, v192, v193\n"
    "v_cvt_pk_bf16_f32 v29, v190, v191\n"
    "v_cvt_pk_bf16_f32 v31, v194, v195\n"
    "ds_write2st64_b64 v67, v[28:29], v[30:31] offset0:20 offset1:28\n"
    "s_waitcnt lgkmcnt(2)\n"
    "v_lshlrev_b32 v240, 16, v48\n"
    "v_and_b32 v241, 0xffff0000, v48\n"
    "v_lshlrev_b32 v248, 16, v50\n"
    "v_and_b32 v249, 0xffff0000, v50\n"
    "v_lshlrev_b32 v242, 16, v49\n"
    "v_and_b32 v243, 0xffff0000, v49\n"
    "v_lshlrev_b32 v250, 16, v51\n"
    "s_waitcnt lgkmcnt(0)\n"
    "s_barrier\n"
    "ds_read_b128 v[208:211], v65 offset:8192\n"
    "ds_read_b128 v[212:215], v66 offset:8192\n"
    "ds_read_b128 v[216:219], v65 offset:12288\n"
    "ds_read_b128 v[220:223], v66 offset:12288\n"
    "ds_read_b128 v[224:227], v65 offset:10240\n"
    "ds_read_b128 v[228:231], v66 offset:10240\n"
    "ds_read_b128 v[232:235], v65 offset:14336\n"
    "ds_read_b128 v[236:239], v66 offset:14336\n"
    "v_and_b32 v251, 0xffff0000, v51\n"
    "v_lshlrev_b32 v244, 16, v52\n"
    "v_and_b32 v245, 0xffff0000, v52\n"
    "v_lshlrev_b32 v252, 16, v54\n"
    "v_and_b32 v253, 0xffff0000, v54\n"
    "v_lshlrev_b32 v246, 16, v53\n"
    "v_and_b32 v247, 0xffff0000, v53\n"
    "v_lshlrev_b32 v254, 16, v55\n"
    "v_and_b32 v255, 0xffff0000, v55\n"
    "s_add_u32 s47, %[zr], 2752512\n"
    "s_add_u32 s48, %[zi], 2752512\n"
    "s_add_u32 s45, %[zr], 1835008\n"
    "s_add_u32 s46, %[zi], 1835008\n"
    "s_waitcnt lgkmcnt(7)\n"
    "v_mfma_f32_16x16x32_bf16 v[240:243], v[72:75], v[208:211], v[240:243]\n"
    "v_mfma_f32_16x16x32_bf16 v[248:251], v[80:83], v[208:211], v[248:251]\n"
    "s_waitcnt lgkmcnt(6)\n"
    "v_mfma_f32_16x16x32_bf16 v[240:243], v[76:79], v[212:215], v[240:243]\n"
    "v_mfma_f32_16x16x32_bf16 v[248:251], v[84:87], v[212:215], v[248:251]\n"
    "s_waitcnt lgkmcnt(5)\n"
    "v_mfma_f32_16x16x32_bf16 v[240:243], v[88:91], v[216:219], v[240:243]\n"
    "v_mfma_f32_16x16x32_bf16 v[248:251], v[72:75], v[216:219], v[248:251]\n"
    "s_waitcnt lgkmcnt(4)\n"
    "v_mfma_f32_16x16x32_bf16 v[240:243], v[92:95], v[220:223], v[240:243]\n"
    "v_mfma_f32_16x16x32_bf16 v[248:251], v[76:79], v[220:223], v[248:251]\n"
    "s_waitcnt lgkmcnt(3)\n"
    "v_mfma_f32_16x16x32_bf16 v[244:247], v[72:75], v[224:227], v[244:247]\n"
    "v_nop\n"
    "s_nop 3\n"
    "v_cvt_pk_bf16_f32 v176, v240, v241\n"
    "v_mfma_f32_16x16x32_bf16 v[252:255], v[80:83], v[224:227], v[252:255]\n"
    "v_cvt_pk_bf16_f32 v177, v242, v243\n"
    "buffer_store_dwordx2 v[176:177], v59, %[ws], s45 offen sc1\n"
    "s_waitcnt lgkmcnt(2)\n"
    "v_mfma_f32_16x16x32_bf16 v[244:247], v[76:79], v[228:231], v[244:247]\n"
    "v_cvt_pk_bf16_f32 v178, v248, v249\n"
    "v_cvt_pk_bf16_f32 v179, v250, v251\n"
    "v_mfma_f32_16x16x32_bf16 v[252:255], v[84:87], v[228:231], v[252:255]\n"
    "v_bfi_b32 v178, v68, v178, v50\n"
    "v_bfi_b32 v179, v68, v179, v51\n"
    "s_waitcnt lgkmcnt(1)\n"
    "v_mfma_f32_16x16x32_bf16 v[244:247], v[88:91], v[232:235], v[244:247]\n"
    "buffer_store_dwordx2 v[178:179], v59, %[ws], s46 offen sc1\n"
    "v_mfma_f32_16x16x32_bf16 v[252:255], v[72:75], v[232:235], v[252:255]\n"
    "s_waitcnt lgkmcnt(0)\n"
    "v_mfma_f32_16x16x32_bf16 v[244:247], v[92:95], v[236:239], v[244:247]\n"
    "v_mfma_f32_16x16x32_bf16 v[252:255], v[76:79], v[236:239], v[252:255]\n"
    "s_nop 6\n"
    "v_cvt_pk_bf16_f32 v180, v244, v245\n"
    "v_cvt_pk_bf16_f32 v181, v246, v247\n"
    "buffer_store_dwordx2 v[180:181], v59, %[ws], s47 offen sc1\n"
    "v_cvt_pk_bf16_f32 v182, v252, v253\n"
    "v_cvt_pk_bf16_f32 v183, v254, v255\n"
    "v_bfi_b32 v182, v68, v182, v54\n"
    "v_bfi_b32 v183, v68, v183, v55\n"
    "buffer_store_dwordx2 v[182:183], v59, %[ws], s48 offen sc1\n"
    "v_mbcnt_lo_u32_b32 v255, -1, 0\n"
    "s_add_u32 s47, %[lds], 152064\n"
    "v_mbcnt_hi_u32_b32 v255, -1, v255\n"
    "s_lshl_b32 s46, %[w], 8\n"
    "v_lshlrev_b32 v236, 4, v255\n"
    "s_mov_b32 s40, %[xg]\n"
    "s_mov_b32 s41, %[q]\n"
    "v_lshlrev_b32 v242, 2, v255\n"
    "s_add_u32 s47, s47, s46\n"
    "s_mul_i32 s56, %[w], 8192\n"
    "v_mul_u32_u24 v130, 4096, v255\n"
    "s_add_u32 s56, s56, %[bw]\n"
    "v_min_u32 v131, 55, v255\n"
    "s_mul_i32 s57, %[w], 1024\n"
    "v_mul_u32_u24 v131, 16, v131\n"
    "s_add_u32 s57, s57, %[br]\n"
    "v_mov_b32 v132, 1\n"
    "s_mov_b32 s46, 0x10000\n"
    "s_mov_b32 s44, %[hl]\n"
    "s_mov_b32 s45, %[hs]\n"
    "s_mov_b32 s42, s57\n"
    "s_waitcnt vmcnt(0)\n"
    "s_barrier\n"
    "s_mov_b64 exec, 3\n"
    "buffer_store_dword v132, v130, %[ws], s56 offen sc1\n"
    "s_mov_b64 exec, -1\n"
    "s_mov_b32 m0, s47\n"
    "s_nop 0\n"
    "buffer_load_dword v242, %[ws], %[b1] offen lds\n"
    "s_add_u32 m0, m0, 1024\n"
    "s_nop 0\n"
    "buffer_load_dword v242, %[ws], %[b2] offen lds\n"
    "s_mov_b32 m0, %[xl]\n"
    "buffer_load_dwordx4 v[64:67], v236, %[ws], s41 offen offset:0\n"
    "v_and_b32 v242, 3, v255\n"
    "v_and_b32 v243, 12, v255\n"
    "v_lshlrev_b32 v242, 3, v242\n"
    "v_lshl_or_b32 v242, v243, 4, v242\n"
    "v_and_b32 v243, 16, v255\n"
    "v_lshl_or_b32 v242, v243, 1, v242\n"
    "v_and_b32 v243, 32, v255\n"
    "v_lshl_or_b32 v242, v243, 3, v242\n"
    "v_add_u32 v237, %[lds], v242\n"
    "v_add_u32 v239, %[xl], v242\n"
    "v_add_u32 v238, 57344, v237\n"
    "v_and_b32 v243, 16, v255\n"
    "v_mul_u32_u24 v243, 62, v243\n"
    "v_add_u32 v239, v243, v239\n"
    "v_add_u32 v250, %[el], v242\n"
    "v_mov_b32 v244, -1\n"
    "s_mov_b32 s48, 0x22222222\n"
    "s_mov_b32 s49, 0x22222222\n"
    "s_mov_b32 s50, 0x44444444\n"
    "s_mov_b32 s51, 0x44444444\n"
    "s_mov_b32 s52, 0x88888888\n"
    "s_mov_b32 s53, 0x88888888\n"
    "s_mov_b64 exec, 0xffff\n"
    "v_mov_b32 v244, 0xffff0000\n"
    "s_mov_b64 exec, -1\n"
    "buffer_load_dwordx4 v[68:71], v236, %[ws], s41 offen offset:1024\n"
    "v_and_b32 v242, 31, v255\n"
    "s_add_u32 s56, %[lds], 151552\n"
    "v_lshlrev_b32 v245, 4, v242\n"
    "v_add_u32 v245, s56, v245\n"
    "v_mov_b32 v240, 0x409d30fc\n"
    "v_mov_b32 v241, 0x3d00435d\n"
    "s_mul_i32 s56, %[w], 8704\n"
    "v_mul_u32_u24 v248, 272, v242\n"
    "s_add_u32 s57, %[lds], 114688\n"
    "v_lshrrev_b32 v243, 5, v255\n"
    "s_add_u32 s56, s56, s57\n"
    "v_lshl_add_u32 v248, v243, 4, v248\n"
    "v_add_u32 v248, s56, v248\n"
    "s_mul_i32 s56, %[w], 2176\n"
    "v_lshrrev_b32 v249, 3, v255\n"
    "s_add_u32 s56, s56, s57\n"
    "v_mul_u32_u24 v249, 272, v249\n"
    "v_and_b32 v247, 7, v255\n"
    "v_lshl_add_u32 v249, v247, 5, v249\n"
    "v_add_u32 v249, s56, v249\n"
    "s_mul_i32 s56, %[w], 14\n"
    "v_min_u32 v246, 13, v255\n"
    "v_add_u32 v246, s56, v246\n"
    "v_and_b32 v247, 3, v246\n"
    "v_lshrrev_b32 v246, 2, v246\n"
    "v_lshlrev_b32 v247, 2, v247\n"
    "buffer_load_dwordx4 v[72:75], v236, %[ws], s41 offen offset:2048\n"
    "v_lshl_or_b32 v246, v246, 7, v247\n"
    "v_and_b32 v242, 31, v255\n"
    "v_lshrrev_b32 v243, 5, v255\n"
    "v_add_u32 v242, %[d0], v242\n"
    "s_mul_i32 s56, %[w], 56\n"
    "v_lshlrev_b32 v243, 1, v243\n"
    "v_cvt_f32_u32 v251, v242\n"
    "v_add_u32 v243, s56, v243\n"
    "v_add_f32 v252, 0x42000000, v251\n"
    "v_cvt_f32_u32 v243, v243\n"
    "v_mul_f32 v251, 0x3b124925, v251\n"
    "v_mul_f32 v252, 0x3b124925, v252\n"
    "v_mul_f32 v253, v251, v243\n"
    "v_mul_f32 v254, v252, v243\n"
    "v_fract_f32 v253, v253\n"
    "v_fract_f32 v254, v254\n"
    "v_cos_f32 v218, v253\n"
    "v_sin_f32 v219, v253\n"
    "v_cos_f32 v220, v254\n"
    "v_sin_f32 v221, v254\n"
    "v_cvt_pk_bf16_f32 v160, v218, -v219\n"
    "v_cvt_pk_bf16_f32 v188, v220, -v221\n"
    "buffer_load_dwordx4 v[76:79], v236, %[ws], s41 offen offset:3072\n"
    "v_fmamk_f32 v216, v251, 0x3f800000, v253\n"
    "v_fmamk_f32 v217, v252, 0x3f800000, v254\n"
    "v_cos_f32 v218, v216\n"
    "v_sin_f32 v219, v216\n"
    "v_cos_f32 v220, v217\n"
    "v_sin_f32 v221, v217\n"
    "v_cvt_pk_bf16_f32 v161, v218, -v219\n"
    "v_cvt_pk_bf16_f32 v189, v220, -v221\n"
    "v_fmamk_f32 v216, v251, 0x40800000, v253\n"
    "v_fmamk_f32 v217, v252, 0x40800000, v254\n"
    "v_cos_f32 v218, v216\n"
    "v_sin_f32 v219, v216\n"
    "v_cos_f32 v220, v217\n"
    "v_sin_f32 v221, v217\n"
    "v_cvt_pk_bf16_f32 v162, v218, -v219\n"
    "v_cvt_pk_bf16_f32 v190, v220, -v221\n"
    "v_fmamk_f32 v216, v251, 0x40a00000, v253\n"
    "s_add_u32 s41, s41, 4096\n"
    "buffer_load_dwordx4 v[80:83], v236, %[ws], s41 offen offset:0\n"
    "v_fmamk_f32 v217, v252, 0x40a00000, v254\n"
    "v_cos_f32 v218, v216\n"
    "v_sin_f32 v219, v216\n"
    "v_cos_f32 v220, v217\n"
    "v_sin_f32 v221, v217\n"
    "v_cvt_pk_bf16_f32 v163, v218, -v219\n"
    "v_cvt_pk_bf16_f32 v191, v220, -v221\n"
    "v_lshlrev_b32 v216, 31, v255\n"
    "s_cmp_eq_u32 %[w], 0\n"
    "s_cselect_b32 s54, -1, 0\n"
    "s_mov_b32 s55, 0\n"
    "v_or_b32 v216, 0x3f353f35, v216\n"
    "v_cndmask_b32_e64 v160, v160, v216, s[54:55]\n"
    "v_cndmask_b32_e64 v188, v188, v216, s[54:55]\n"
    "v_fmamk_f32 v216, v251, 0x41000000, v253\n"
    "v_fmamk_f32 v217, v252, 0x41000000, v254\n"
    "v_cos_f32 v218, v216\n"
    "v_sin_f32 v219, v216\n"
    "v_cos_f32 v220, v217\n"
    "buffer_load_dwordx4 v[84:87], v236, %[ws], s41 offen offset:1024\n"
    "v_sin_f32 v221, v217\n"
    "v_cvt_pk_bf16_f32 v164, v218, -v219\n"
    "v_cvt_pk_bf16_f32 v192, v220, -v221\n"
    "v_fmamk_f32 v216, v251, 0x41100000, v253\n"
    "v_fmamk_f32 v217, v252, 0x41100000, v254\n"
    "v_cos_f32 v218, v216\n"
    "v_sin_f32 v219, v216\n"
    "v_cos_f32 v220, v217\n"
    "v_sin_f32 v221, v217\n"
    "v_cvt_pk_bf16_f32 v165, v218, -v219\n"
    "v_cvt_pk_bf16_f32 v193, v220, -v221\n"
    "v_fmamk_f32 v216, v251, 0x41400000, v253\n"
    "v_fmamk_f32 v217, v252, 0x41400000, v254\n"
    "v_cos_f32 v218, v216\n"
    "v_sin_f32 v219, v216\n"
    "v_cos_f32 v220, v217\n"
    "v_sin_f32 v221, v217\n"
    "buffer_load_dwordx4 v[88:91], v236, %[ws], s41 offen offset:2048\n"
    "v_cvt_pk_bf16_f32 v166, v218, -v219\n"
    "v_cvt_pk_bf16_f32 v194, v220, -v221\n"
    "v_fmamk_f32 v216, v251, 0x41500000, v253\n"
    "v_fmamk_f32 v217, v252, 0x41500000, v254\n"
    "v_cos_f32 v218, v216\n"
    "v_sin_f32 v219, v216\n"
    "v_cos_f32 v220, v217\n"
    "v_sin_f32 v221, v217\n"
    "v_cvt_pk_bf16_f32 v167, v218, -v219\n"
    "v_cvt_pk_bf16_f32 v195, v220, -v221\n"
    "v_fmamk_f32 v216, v251, 0x41800000, v253\n"
    "v_fmamk_f32 v217, v252, 0x41800000, v254\n"
    "v_cos_f32 v218, v216\n"
    "v_sin_f32 v219, v216\n"
    "v_cos_f32 v220, v217\n"
    "v_sin_f32 v221, v217\n"
    "v_cvt_pk_bf16_f32 v168, v218, -v219\n"
    "buffer_load_dwordx4 v[92:95], v236, %[ws], s41 offen offset:3072\n"
    "v_cvt_pk_bf16_f32 v196, v220, -v221\n"
    "v_fmamk_f32 v216, v251, 0x41880000, v253\n"
    "v_fmamk_f32 v217, v252, 0x41880000, v254\n"
    "v_cos_f32 v218, v216\n"
    "v_sin_f32 v219, v216\n"
    "v_cos_f32 v220, v217\n"
    "v_sin_f32 v221, v217\n"
    "v_cvt_pk_bf16_f32 v169, v218, -v219\n"
    "v_cvt_pk_bf16_f32 v197, v220, -v221\n"
    "v_fmamk_f32 v216, v251, 0x41a00000, v253\n"
    "v_fmamk_f32 v217, v252, 0x41a00000, v254\n"
    "v_cos_f32 v218, v216\n"
    "v_sin_f32 v219, v216\n"
    "v_cos_f32 v220, v217\n"
    "v_sin_f32 v221, v217\n"
    "v_cvt_pk_bf16_f32 v170, v218, -v219\n"
    "v_cvt_pk_bf16_f32 v198, v220, -v221\n"
    "s_add_u32 s41, s41, 4096\n"
    "buffer_load_dwordx4 v[96:99], v236, %[ws], s41 offen offset:0\n"
    "v_fmamk_f32 v216, v251, 0x41a80000, v253\n"
    "v_fmamk_f32 v217, v252, 0x41a80000, v254\n"
    "v_cos_f32 v218, v216\n"
    "v_sin_f32 v219, v216\n"
    "v_cos_f32 v220, v217\n"
    "v_sin_f32 v221, v217\n"
    "v_cvt_pk_bf16_f32 v171, v218, -v219\n"
    "v_cvt_pk_bf16_f32 v199, v220, -v221\n"
    "v_fmamk_f32 v216, v251, 0x41c00000, v253\n"
    "v_fmamk_f32 v217, v252, 0x41c00000, v254\n"
    "v_cos_f32 v218, v216\n"
    "v_sin_f32 v219, v216\n"
    "v_cos_f32 v220, v217\n"
    "v_sin_f32 v221, v217\n"
    "v_cvt_pk_bf16_f32 v172, v218, -v219\n"
    "v_cvt_pk_bf16_f32 v200, v220, -v221\n"
    "v_fmamk_f32 v216, v251, 0x41c80000, v253\n"
    "buffer_load_dwordx4 v[100:103], v236, %[ws], s41 offen offset:1024\n"
    "v_fmamk_f32 v217, v252, 0x41c80000, v254\n"
    "v_cos_f32 v218, v216\n"
    "v_sin_f32 v219, v216\n"
    "v_cos_f32 v220, v217\n"
    "v_sin_f32 v221, v217\n"
    "v_cvt_pk_bf16_f32 v173, v218, -v219\n"
    "v_cvt_pk_bf16_f32 v201, v220, -v221\n"
    "v_fmamk_f32 v216, v251, 0x41e00000, v253\n"
    "v_fmamk_f32 v217, v252, 0x41e00000, v254\n"
    "v_cos_f32 v218, v216\n"
    "v_sin_f32 v219, v216\n"
    "v_cos_f32 v220, v217\n"
    "v_sin_f32 v221, v217\n"
    "v_cvt_pk_bf16_f32 v174, v218, -v219\n"
    "v_cvt_pk_bf16_f32 v202, v220, -v221\n"
    "v_fmamk_f32 v216, v251, 0x41e80000, v253\n"
    "v_fmamk_f32 v217, v252, 0x41e80000, v254\n"
    "buffer_load_dwordx4 v[104:107], v236, %[ws], s41 offen offset:2048\n"
    "v_cos_f32 v218, v216\n"
    "v_sin_f32 v219, v216\n"
    "v_cos_f32 v220, v217\n"
    "v_sin_f32 v221, v217\n"
    "v_cvt_pk_bf16_f32 v175, v218, -v219\n"
    "v_cvt_pk_bf16_f32 v203, v220, -v221\n"
    "s_nop 14\n"
    "buffer_load_dwordx4 v[108:111], v236, %[ws], s41 offen offset:3072\n"
    "s_nop 15\n"
    "buffer_load_dword v133, v131, %[ws], s42 offen sc1\n"
    "buffer_load_dwordx4 a[0:3], v236, %[ws], s58 offen offset:0\n"
    "s_nop 15\n"
    "buffer_load_dwordx4 a[112:115], v236, %[ws], s59 offen offset:0\n"
    "s_nop 15\n"
    "buffer_load_dwordx4 a[4:7], v236, %[ws], s58 offen offset:1024\n"
    "s_nop 15\n"
    "buffer_load_dwordx4 a[116:119], v236, %[ws], s59 offen offset:1024\n"
    "s_nop 15\n"
    "buffer_load_dwordx4 a[8:11], v236, %[ws], s58 offen offset:2048\n"
    "s_nop 15\n"
    "buffer_load_dwordx4 a[120:123], v236, %[ws], s59 offen offset:2048\n"
    "s_nop 15\n"
    "s_waitcnt vmcnt(6)\n"
    "v_cmp_gt_u32 vcc, 1, v133\n"
    "s_nop 4\n"
    "s_cbranch_vccnz LS0_%=\n"
    "LR0_%=:\n"
    "s_barrier\n"
    "buffer_load_dwordx4 v236, %[ws], s40 offen offset:0 sc1 lds\n"
    "buffer_load_dwordx4 v236, %[ws], s40 offen offset:1024 sc1 lds\n"
    "buffer_load_dwordx4 v236, %[ws], s40 offen offset:2048 sc1 lds\n"
    "buffer_load_dwordx4 v236, %[ws], s40 offen offset:3072 sc1 lds\n"
    "s_add_u32 m0, m0, 4096\n"
    "s_add_u32 s40, s40, 4096\n"
    "buffer_load_dwordx4 v236, %[ws], s40 offen offset:0 sc1 lds\n"
    "buffer_load_dwordx4 v236, %[ws], s40 offen offset:1024 sc1 lds\n"
    "s_waitcnt vmcnt(4)\n"
    "ds_read_b64_tr_b16 v[144:145], v239 offset:0\n"
    "ds_read_b64_tr_b16 v[146:147], v239 offset:512\n"
    "ds_read_b64_tr_b16 v[148:149], v239 offset:32\n"
    "ds_read_b64_tr_b16 v[150:151], v239 offset:544\n"
    "s_barrier\n"
    "ds_read_b64_tr_b16 v[112:113], v237 offset:0\n"
    "ds_read_b64_tr_b16 v[114:115], v237 offset:512\n"
    "ds_read_b64_tr_b16 v[116:117], v237 offset:28672\n"
    "ds_read_b64_tr_b16 v[118:119], v237 offset:29184\n"
    "ds_read_b64_tr_b16 v[120:121], v238 offset:0\n"
    "ds_read_b64_tr_b16 v[122:123], v238 offset:512\n"
    "ds_read_b64_tr_b16 v[124:125], v238 offset:28672\n"
    "ds_read_b64_tr_b16 v[126:127], v238 offset:29184\n"
    "s_waitcnt lgkmcnt(4)\n"
    "v_mfma_f32_32x32x16_bf16 v[0:15], v[64:67], v[112:115], 0\n"
    "v_and_b32 v144, v244, v144\n"
    "v_and_b32 v148, v244, v148\n"
    "ds_read_b64_tr_b16 v[128:129], v237 offset:1024\n"
    "ds_read_b64_tr_b16 v[130:131], v237 offset:1536\n"
    "v_mfma_f32_32x32x16_bf16 v[16:31], v[64:67], v[116:119], 0\n"
    "ds_read_b64_tr_b16 v[132:133], v237 offset:29696\n"
    "ds_read_b64_tr_b16 v[134:135], v237 offset:30208\n"
    "buffer_load_dwordx4 v236, %[ws], s40 offen offset:2048 sc1 lds\n"
    "v_mfma_f32_16x16x32_bf16 v[152:155], v[144:147], v[144:147], 0\n"
    "s_waitcnt lgkmcnt(4)\n"
    "v_mfma_f32_32x32x16_bf16 v[32:47], v[64:67], v[120:123], 0\n"
    "ds_read_b64_tr_b16 v[136:137], v238 offset:1024\n"
    "ds_read_b64_tr_b16 v[138:139], v238 offset:1536\n"
    "v_mfma_f32_32x32x16_bf16 v[48:63], v[64:67], v[124:127], 0\n"
    "ds_read_b64_tr_b16 v[140:141], v238 offset:29696\n"
    "ds_read_b64_tr_b16 v[142:143], v238 offset:30208\n"
    "s_add_u32 s41, s41, 4096\n"
    "buffer_load_dwordx4 v[64:67], v236, %[ws], s41 offen offset:0\n"
    "v_mfma_f32_16x16x32_bf16 v[156:159], v[148:151], v[148:151], 0\n"
    "s_waitcnt lgkmcnt(4)\n"
    "v_mfma_f32_32x32x16_bf16 v[0:15], v[68:71], v[128:131], v[0:15]\n"
    "s_waitcnt vmcnt(4)\n"
    "s_barrier\n"
    "ds_read_b64_tr_b16 v[112:113], v237 offset:2048\n"
    "ds_read_b64_tr_b16 v[114:115], v237 offset:2560\n"
    "v_mfma_f32_32x32x16_bf16 v[16:31], v[68:71], v[132:135], v[16:31]\n"
    "ds_read_b64_tr_b16 v[116:117], v237 offset:30720\n"
    "ds_read_b64_tr_b16 v[118:119], v237 offset:31232\n"
    "ds_read_b64_tr_b16 v[144:145], v239 offset:2048\n"
    "buffer_load_dwordx4 v236, %[ws], s40 offen offset:3072 sc1 lds\n"
    "s_waitcnt lgkmcnt(5)\n"
    "v_mfma_f32_32x32x16_bf16 v[32:47], v[68:71], v[136:139], v[32:47]\n"
    "ds_read_b64_tr_b16 v[120:121], v238 offset:2048\n"
    "ds_read_b64_tr_b16 v[122:123], v238 offset:2560\n"
    "ds_read_b64_tr_b16 v[146:147], v239 offset:2560\n"
    "v_mfma_f32_32x32x16_bf16 v[48:63], v[68:71], v[140:143], v[48:63]\n"
    "ds_read_b64_tr_b16 v[124:125], v238 offset:30720\n"
    "ds_read_b64_tr_b16 v[126:127], v238 offset:31232\n"
    "ds_read_b64_tr_b16 v[148:149], v239 offset:2080\n"
    "buffer_load_dwordx4 v[68:71], v236, %[ws], s41 offen offset:1024\n"
    "s_waitcnt lgkmcnt(7)\n"
    "v_mfma_f32_32x32x16_bf16 v[0:15], v[72:75], v[112:115], v[0:15]\n"
    "ds_read_b64_tr_b16 v[128:129], v237 offset:3072\n"
    "ds_read_b64_tr_b16 v[130:131], v237 offset:3584\n"
    "ds_read_b64_tr_b16 v[150:151], v239 offset:2592\n"
    "s_waitcnt lgkmcnt(6)\n"
    "v_mfma_f32_16x16x32_bf16 v[152:155], v[144:147], v[144:147], v[152:155]\n"
    "v_mfma_f32_32x32x16_bf16 v[16:31], v[72:75], v[116:119], v[16:31]\n"
    "ds_read_b64_tr_b16 v[132:133], v237 offset:31744\n"
    "ds_read_b64_tr_b16 v[134:135], v237 offset:32256\n"
    "s_add_u32 m0, m0, 4096\n"
    "s_add_u32 s40, s40, 4096\n"
    "buffer_load_dwordx4 v236, %[ws], s40 offen offset:0 sc1 lds\n"
    "s_waitcnt lgkmcnt(6)\n"
    "v_mfma_f32_32x32x16_bf16 v[32:47], v[72:75], v[120:123], v[32:47]\n"
    "ds_read_b64_tr_b16 v[136:137], v238 offset:3072\n"
    "ds_read_b64_tr_b16 v[138:139], v238 offset:3584\n"
    "s_waitcnt lgkmcnt(4)\n"
    "v_mfma_f32_16x16x32_bf16 v[156:159], v[148:151], v[148:151], v[156:159]\n"
    "v_mfma_f32_32x32x16_bf16 v[48:63], v[72:75], v[124:127], v[48:63]\n"
    "ds_read_b64_tr_b16 v[140:141], v238 offset:31744\n"
    "ds_read_b64_tr_b16 v[142:143], v238 offset:32256\n"
    "buffer_load_dwordx4 v[72:75], v236, %[ws], s41 offen offset:2048\n"
    "s_waitcnt lgkmcnt(4)\n"
    "v_mfma_f32_32x32x16_bf16 v[0:15], v[76:79], v[128:131], v[0:15]\n"
    "s_waitcnt vmcnt(6)\n"
    "s_barrier\n"
    "ds_read_b64_tr_b16 v[112:113], v237 offset:4096\n"
    "ds_read_b64_tr_b16 v[114:115], v237 offset:4608\n"
    "v_mfma_f32_32x32x16_bf16 v[16:31], v[76:79], v[132:135], v[16:31]\n"
    "ds_read_b64_tr_b16 v[116:117], v237 offset:32768\n"
    "ds_read_b64_tr_b16 v[118:119], v237 offset:33280\n"
    "ds_read_b64_tr_b16 v[144:145], v239 offset:4096\n"
    "buffer_load_dwordx4 v236, %[ws], s40 offen offset:1024 sc1 lds\n"
    "s_waitcnt lgkmcnt(5)\n"
    "v_mfma_f32_32x32x16_bf16 v[32:47], v[76:79], v[136:139], v[32:47]\n"
    "ds_read_b64_tr_b16 v[120:121], v238 offset:4096\n"
    "ds_read_b64_tr_b16 v[122:123], v238 offset:4608\n"
    "ds_read_b64_tr_b16 v[146:147], v239 offset:4608\n"
    "v_mfma_f32_32x32x16_bf16 v[48:63], v[76:79], v[140:143], v[48:63]\n"
    "ds_read_b64_tr_b16 v[124:125], v238 offset:32768\n"
    "ds_read_b64_tr_b16 v[126:127], v238 offset:33280\n"
    "ds_read_b64_tr_b16 v[148:149], v239 offset:4128\n"
    "buffer_load_dwordx4 v[76:79], v236, %[ws], s41 offen offset:3072\n"
    "s_waitcnt lgkmcnt(7)\n"
    "v_mfma_f32_32x32x16_bf16 v[0:15], v[80:83], v[112:115], v[0:15]\n"
    "ds_read_b64_tr_b16 v[128:129], v237 offset:5120\n"
    "ds_read_b64_tr_b16 v[130:131], v237 offset:5632\n"
    "ds_read_b64_tr_b16 v[150:151], v239 offset:4640\n"
    "s_waitcnt lgkmcnt(6)\n"
    "v_mfma_f32_16x16x32_bf16 v[152:155], v[144:147], v[144:147], v[152:155]\n"
    "v_mfma_f32_32x32x16_bf16 v[16:31], v[80:83], v[116:119], v[16:31]\n"
    "ds_read_b64_tr_b16 v[132:133], v237 offset:33792\n"
    "ds_read_b64_tr_b16 v[134:135], v237 offset:34304\n"
    "buffer_load_dwordx4 v236, %[ws], s40 offen offset:2048 sc1 lds\n"
    "s_waitcnt lgkmcnt(6)\n"
    "v_mfma_f32_32x32x16_bf16 v[32:47], v[80:83], v[120:123], v[32:47]\n"
    "ds_read_b64_tr_b16 v[136:137], v238 offset:5120\n"
    "ds_read_b64_tr_b16 v[138:139], v238 offset:5632\n"
    "s_waitcnt lgkmcnt(4)\n"
    "v_mfma_f32_16x16x32_bf16 v[156:159], v[148:151], v[148:151], v[156:159]\n"
    "v_mfma_f32_32x32x16_bf16 v[48:63], v[80:83], v[124:127], v[48:63]\n"
    "ds_read_b64_tr_b16 v[140:141], v238 offset:33792\n"
    "ds_read_b64_tr_b16 v[142:143], v238 offset:34304\n"
    "s_add_u32 s41, s41, 4096\n"
    "buffer_load_dwordx4 v[80:83], v236, %[ws], s41 offen offset:0\n"
    "s_waitcnt lgkmcnt(4)\n"
    "v_mfma_f32_32x32x16_bf16 v[0:15], v[84:87], v[128:131], v[0:15]\n"
    "s_waitcnt vmcnt(7)\n"
    "s_barrier\n"
    "ds_read_b64_tr_b16 v[112:113], v237 offset:6144\n"
    "ds_read_b64_tr_b16 v[114:115], v237 offset:6656\n"
    "v_mfma_f32_32x32x16_bf16 v[16:31], v[84:87], v[132:135], v[16:31]\n"
    "ds_read_b64_tr_b16 v[116:117], v237 offset:34816\n"
    "ds_read_b64_tr_b16 v[118:119], v237 offset:35328\n"
    "ds_read_b64_tr_b16 v[144:145], v239 offset:6144\n"
    "buffer_load_dwordx4 v236, %[ws], s40 offen offset:3072 sc1 lds\n"
    "s_waitcnt lgkmcnt(5)\n"
    "v_mfma_f32_32x32x16_bf16 v[32:47], v[84:87], v[136:139], v[32:47]\n"
    "ds_read_b64_tr_b16 v[120:121], v238 offset:6144\n"
    "ds_read_b64_tr_b16 v[122:123], v238 offset:6656\n"
    "ds_read_b64_tr_b16 v[146:147], v239 offset:6656\n"
    "v_mfma_f32_32x32x16_bf16 v[48:63], v[84:87], v[140:143], v[48:63]\n"
    "ds_read_b64_tr_b16 v[124:125], v238 offset:34816\n"
    "ds_read_b64_tr_b16 v[126:127], v238 offset:35328\n"
    "ds_read_b64_tr_b16 v[148:149], v239 offset:6176\n"
    "buffer_load_dwordx4 v[84:87], v236, %[ws], s41 offen offset:1024\n"
    "s_waitcnt lgkmcnt(7)\n"
    "v_mfma_f32_32x32x16_bf16 v[0:15], v[88:91], v[112:115], v[0:15]\n"
    "ds_read_b64_tr_b16 v[128:129], v237 offset:7168\n"
    "ds_read_b64_tr_b16 v[130:131], v237 offset:7680\n"
    "ds_read_b64_tr_b16 v[150:151], v239 offset:6688\n"
    "s_waitcnt lgkmcnt(6)\n"
    "v_mfma_f32_16x16x32_bf16 v[152:155], v[144:147], v[144:147], v[152:155]\n"
    "v_mfma_f32_32x32x16_bf16 v[16:31], v[88:91], v[116:119], v[16:31]\n"
    "ds_read_b64_tr_b16 v[132:133], v237 offset:35840\n"
    "ds_read_b64_tr_b16 v[134:135], v237 offset:36352\n"
    "s_add_u32 m0, m0, 4096\n"
    "s_add_u32 s40, s40, 4096\n"
    "buffer_load_dwordx4 v236, %[ws], s40 offen offset:0 sc1 lds\n"
    "s_waitcnt lgkmcnt(6)\n"
    "v_mfma_f32_32x32x16_bf16 v[32:47], v[88:91], v[120:123], v[32:47]\n"
    "ds_read_b64_tr_b16 v[136:137], v238 offset:7168\n"
    "ds_read_b64_tr_b16 v[138:139], v238 offset:7680\n"
    "s_waitcnt lgkmcnt(4)\n"
    "v_mfma_f32_16x16x32_bf16 v[156:159], v[148:151], v[148:151], v[156:159]\n"
    "v_mfma_f32_32x32x16_bf16 v[48:63], v[88:91], v[124:127], v[48:63]\n"
    "ds_read_b64_tr_b16 v[140:141], v238 offset:35840\n"
    "ds_read_b64_tr_b16 v[142:143], v238 offset:36352\n"
    "buffer_load_dwordx4 v[88:91], v236, %[ws], s41 offen offset:2048\n"
    "s_waitcnt lgkmcnt(4)\n"
    "v_mfma_f32_32x32x16_bf16 v[0:15], v[92:95], v[128:131], v[0:15]\n"
    "s_waitcnt vmcnt(7)\n"
    "s_barrier\n"
    "ds_read_b64_tr_b16 v[112:113], v237 offset:8192\n"
    "ds_read_b64_tr_b16 v[114:115], v237 offset:8704\n"
    "v_mfma_f32_32x32x16_bf16 v[16:31], v[92:95], v[132:135], v[16:31]\n"
    "ds_read_b64_tr_b16 v[116:117], v237 offset:36864\n"
    "ds_read_b64_tr_b16 v[118:119], v237 offset:37376\n"
    "ds_read_b64_tr_b16 v[144:145], v239 offset:8192\n"
    "buffer_load_dwordx4 v236, %[ws], s40 offen offset:1024 sc1 lds\n"
    "s_waitcnt lgkmcnt(5)\n"
    "v_mfma_f32_32x32x16_bf16 v[32:47], v[92:95], v[136:139], v[32:47]\n"
    "ds_read_b64_tr_b16 v[120:121], v238 offset:8192\n"
    "ds_read_b64_tr_b16 v[122:123], v238 offset:8704\n"
    "ds_read_b64_tr_b16 v[146:147], v239 offset:8704\n"
    "v_mfma_f32_32x32x16_bf16 v[48:63], v[92:95], v[140:143], v[48:63]\n"
    "ds_read_b64_tr_b16 v[124:125], v238 offset:36864\n"
    "ds_read_b64_tr_b16 v[126:127], v238 offset:37376\n"
    "ds_read_b64_tr_b16 v[148:149], v239 offset:8224\n"
    "buffer_load_dwordx4 v[92:95], v236, %[ws], s41 offen offset:3072\n"
    "s_waitcnt lgkmcnt(7)\n"
    "v_mfma_f32_32x32x16_bf16 v[0:15], v[96:99], v[112:115], v[0:15]\n"
    "ds_read_b64_tr_b16 v[128:129], v237 offset:9216\n"
    "ds_read_b64_tr_b16 v[130:131], v237 offset:9728\n"
    "ds_read_b64_tr_b16 v[150:151], v239 offset:8736\n"
    "s_waitcnt lgkmcnt(6)\n"
    "v_mfma_f32_16x16x32_bf16 v[152:155], v[144:147], v[144:147], v[152:155]\n"
    "v_mfma_f32_32x32x16_bf16 v[16:31], v[96:99], v[116:119], v[16:31]\n"
    "ds_read_b64_tr_b16 v[132:133], v237 offset:37888\n"
    "ds_read_b64_tr_b16 v[134:135], v237 offset:38400\n"
    "buffer_load_dwordx4 v236, %[ws], s40 offen offset:2048 sc1 lds\n"
    "s_waitcnt lgkmcnt(6)\n"
    "v_mfma_f32_32x32x16_bf16 v[32:47], v[96:99], v[120:123], v[32:47]\n"
    "ds_read_b64_tr_b16 v[136:137], v238 offset:9216\n"
    "ds_read_b64_tr_b16 v[138:139], v238 offset:9728\n"
    "s_waitcnt lgkmcnt(4)\n"
    "v_mfma_f32_16x16x32_bf16 v[156:159], v[148:151], v[148:151], v[156:159]\n"
    "v_mfma_f32_32x32x16_bf16 v[48:63], v[96:99], v[124:127], v[48:63]\n"
    "ds_read_b64_tr_b16 v[140:141], v238 offset:37888\n"
    "ds_read_b64_tr_b16 v[142:143], v238 offset:38400\n"
    "s_add_u32 s41, s41, 4096\n"
    "buffer_load_dwordx4 v[96:99], v236, %[ws], s41 offen offset:0\n"
    "s_waitcnt lgkmcnt(4)\n"
    "v_mfma_f32_32x32x16_bf16 v[0:15], v[100:103], v[128:131], v[0:15]\n"
    "s_waitcnt vmcnt(7)\n"
    "s_barrier\n"
    "ds_read_b64_tr_b16 v[112:113], v237 offset:10240\n"
    "ds_read_b64_tr_b16 v[114:115], v237 offset:10752\n"
    "v_mfma_f32_32x32x16_bf16 v[16:31], v[100:103], v[132:135], v[16:31]\n"
    "ds_read_b64_tr_b16 v[116:117], v237 offset:38912\n"
    "ds_read_b64_tr_b16 v[118:119], v237 offset:39424\n"
    "ds_read_b64_tr_b16 v[144:145], v239 offset:10240\n"
    "buffer_load_dwordx4 v236, %[ws], s40 offen offset:3072 sc1 lds\n"
    "s_waitcnt lgkmcnt(5)\n"
    "v_mfma_f32_32x32x16_bf16 v[32:47], v[100:103], v[136:139], v[32:47]\n"
    "ds_read_b64_tr_b16 v[120:121], v238 offset:10240\n"
    "ds_read_b64_tr_b16 v[122:123], v238 offset:10752\n"
    "ds_read_b64_tr_b16 v[146:147], v239 offset:10752\n"
    "v_mfma_f32_32x32x16_bf16 v[48:63], v[100:103], v[140:143], v[48:63]\n"
    "ds_read_b64_tr_b16 v[124:125], v238 offset:38912\n"
    "ds_read_b64_tr_b16 v[126:127], v238 offset:39424\n"
    "ds_read_b64_tr_b16 v[148:149], v239 offset:10272\n"
    "buffer_load_dwordx4 v[100:103], v236, %[ws], s41 offen offset:1024\n"
    "s_waitcnt lgkmcnt(7)\n"
    "v_mfma_f32_32x32x16_bf16 v[0:15], v[104:107], v[112:115], v[0:15]\n"
    "ds_read_b64_tr_b16 v[128:129], v237 offset:11264\n"
    "ds_read_b64_tr_b16 v[130:131], v237 offset:11776\n"
    "ds_read_b64_tr_b16 v[150:151], v239 offset:10784\n"
    "s_waitcnt lgkmcnt(6)\n"
    "v_mfma_f32_16x16x32_bf16 v[152:155], v[144:147], v[144:147], v[152:155]\n"
    "v_mfma_f32_32x32x16_bf16 v[16:31], v[104:107], v[116:119], v[16:31]\n"
    "ds_read_b64_tr_b16 v[132:133], v237 offset:39936\n"
    "ds_read_b64_tr_b16 v[134:135], v237 offset:40448\n"
    "s_add_u32 m0, m0, 4096\n"
    "s_add_u32 s40, s40, 4096\n"
    "buffer_load_dwordx4 v236, %[ws], s40 offen offset:0 sc1 lds\n"
    "s_waitcnt lgkmcnt(6)\n"
    "v_mfma_f32_32x32x16_bf16 v[32:47], v[104:107], v[120:123], v[32:47]\n"
    "ds_read_b64_tr_b16 v[136:137], v238 offset:11264\n"
    "ds_read_b64_tr_b16 v[138:139], v238 offset:11776\n"
    "s_waitcnt lgkmcnt(4)\n"
    "v_mfma_f32_16x16x32_bf16 v[156:159], v[148:151], v[148:151], v[156:159]\n"
    "v_mfma_f32_32x32x16_bf16 v[48:63], v[104:107], v[124:127], v[48:63]\n"
    "ds_read_b64_tr_b16 v[140:141], v238 offset:39936\n"
    "ds_read_b64_tr_b16 v[142:143], v238 offset:40448\n"
    "buffer_load_dwordx4 v[104:107], v236, %[ws], s41 offen offset:2048\n"
    "s_waitcnt lgkmcnt(4)\n"
    "v_mfma_f32_32x32x16_bf16 v[0:15], v[108:111], v[128:131], v[0:15]\n"
    "s_waitcnt vmcnt(7)\n"
    "s_barrier\n"
    "ds_read_b64_tr_b16 v[112:113], v237 offset:12288\n"
    "ds_read_b64_tr_b16 v[114:115], v237 offset:12800\n"
    "v_mfma_f32_32x32x16_bf16 v[16:31], v[108:111], v[132:135], v[16:31]\n"
    "ds_read_b64_tr_b16 v[116:117], v237 offset:40960\n"
    "ds_read_b64_tr_b16 v[118:119], v237 offset:41472\n"
    "ds_read_b64_tr_b16 v[144:145], v239 offset:12288\n"
    "buffer_load_dwordx4 v236, %[ws], s40 offen offset:1024 sc1 lds\n"
    "s_waitcnt lgkmcnt(5)\n"
    "v_mfma_f32_32x32x16_bf16 v[32:47], v[108:111], v[136:139], v[32:47]\n"
    "ds_read_b64_tr_b16 v[120:121], v238 offset:12288\n"
    "ds_read_b64_tr_b16 v[122:123], v238 offset:12800\n"
    "ds_read_b64_tr_b16 v[146:147], v239 offset:12800\n"
    "v_mfma_f32_32x32x16_bf16 v[48:63], v[108:111], v[140:143], v[48:63]\n"
    "ds_read_b64_tr_b16 v[124:125], v238 offset:40960\n"
    "ds_read_b64_tr_b16 v[126:127], v238 offset:41472\n"
    "ds_read_b64_tr_b16 v[148:149], v239 offset:12320\n"
    "buffer_load_dwordx4 v[108:111], v236, %[ws], s41 offen offset:3072\n"
    "s_waitcnt lgkmcnt(7)\n"
    "v_mfma_f32_32x32x16_bf16 v[0:15], v[64:67], v[112:115], v[0:15]\n"
    "ds_read_b64_tr_b16 v[128:129], v237 offset:13312\n"
    "ds_read_b64_tr_b16 v[130:131], v237 offset:13824\n"
    "ds_read_b64_tr_b16 v[150:151], v239 offset:12832\n"
    "s_waitcnt lgkmcnt(6)\n"
    "v_mfma_f32_16x16x32_bf16 v[152:155], v[144:147], v[144:147], v[152:155]\n"
    "v_mfma_f32_32x32x16_bf16 v[16:31], v[64:67], v[116:119], v[16:31]\n"
    "ds_read_b64_tr_b16 v[132:133], v237 offset:41984\n"
    "ds_read_b64_tr_b16 v[134:135], v237 offset:42496\n"
    "buffer_load_dwordx4 v236, %[ws], s40 offen offset:2048 sc1 lds\n"
    "s_waitcnt lgkmcnt(6)\n"
    "v_mfma_f32_32x32x16_bf16 v[32:47], v[64:67], v[120:123], v[32:47]\n"
    "ds_read_b64_tr_b16 v[136:137], v238 offset:13312\n"
    "ds_read_b64_tr_b16 v[138:139], v238 offset:13824\n"
    "s_waitcnt lgkmcnt(4)\n"
    "v_mfma_f32_16x16x32_bf16 v[156:159], v[148:151], v[148:151], v[156:159]\n"
    "v_mfma_f32_32x32x16_bf16 v[48:63], v[64:67], v[124:127], v[48:63]\n"
    "ds_read_b64_tr_b16 v[140:141], v238 offset:41984\n"
    "ds_read_b64_tr_b16 v[142:143], v238 offset:42496\n"
    "s_add_u32 s41, s41, 4096\n"
    "buffer_load_dwordx4 v[64:67], v236, %[ws], s41 offen offset:0\n"
    "s_waitcnt lgkmcnt(4)\n"
    "v_mfma_f32_32x32x16_bf16 v[0:15], v[68:71], v[128:131], v[0:15]\n"
    "s_waitcnt vmcnt(7)\n"
    "s_barrier\n"
    "ds_read_b64_tr_b16 v[112:113], v237 offset:14336\n"
    "ds_read_b64_tr_b16 v[114:115], v237 offset:14848\n"
    "v_mfma_f32_32x32x16_bf16 v[16:31], v[68:71], v[132:135], v[16:31]\n"
    "ds_read_b64_tr_b16 v[116:117], v237 offset:43008\n"
    "ds_read_b64_tr_b16 v[118:119], v237 offset:43520\n"
    "ds_read_b64_tr_b16 v[144:145], v239 offset:14336\n"
    "buffer_load_dwordx4 v236, %[ws], s40 offen offset:3072 sc1 lds\n"
    "s_waitcnt lgkmcnt(5)\n"
    "v_mfma_f32_32x32x16_bf16 v[32:47], v[68:71], v[136:139], v[32:47]\n"
    "ds_read_b64_tr_b16 v[120:121], v238 offset:14336\n"
    "ds_read_b64_tr_b16 v[122:123], v238 offset:14848\n"
    "ds_read_b64_tr_b16 v[146:147], v239 offset:14848\n"
    "v_mfma_f32_32x32x16_bf16 v[48:63], v[68:71], v[140:143], v[48:63]\n"
    "ds_read_b64_tr_b16 v[124:125], v238 offset:43008\n"
    "ds_read_b64_tr_b16 v[126:127], v238 offset:43520\n"
    "ds_read_b64_tr_b16 v[148:149], v239 offset:14368\n"
    "buffer_load_dwordx4 v[68:71], v236, %[ws], s41 offen offset:1024\n"
    "s_waitcnt lgkmcnt(7)\n"
    "v_mfma_f32_32x32x16_bf16 v[0:15], v[72:75], v[112:115], v[0:15]\n"
    "ds_read_b64_tr_b16 v[128:129], v237 offset:15360\n"
    "ds_read_b64_tr_b16 v[130:131], v237 offset:15872\n"
    "ds_read_b64_tr_b16 v[150:151], v239 offset:14880\n"
    "s_waitcnt lgkmcnt(6)\n"
    "v_mfma_f32_16x16x32_bf16 v[152:155], v[144:147], v[144:147], v[152:155]\n"
    "v_mfma_f32_32x32x16_bf16 v[16:31], v[72:75], v[116:119], v[16:31]\n"
    "ds_read_b64_tr_b16 v[132:133], v237 offset:44032\n"
    "ds_read_b64_tr_b16 v[134:135], v237 offset:44544\n"
    "s_add_u32 m0, m0, 4096\n"
    "s_add_u32 s40, s40, 4096\n"
    "buffer_load_dwordx4 v236, %[ws], s40 offen offset:0 sc1 lds\n"
    "s_waitcnt lgkmcnt(6)\n"
    "v_mfma_f32_32x32x16_bf16 v[32:47], v[72:75], v[120:123], v[32:47]\n"
    "ds_read_b64_tr_b16 v[136:137], v238 offset:15360\n"
    "ds_read_b64_tr_b16 v[138:139], v238 offset:15872\n"
    "s_waitcnt lgkmcnt(4)\n"
    "v_mfma_f32_16x16x32_bf16 v[156:159], v[148:151], v[148:151], v[156:159]\n"
    "v_mfma_f32_32x32x16_bf16 v[48:63], v[72:75], v[124:127], v[48:63]\n"
    "ds_read_b64_tr_b16 v[140:141], v238 offset:44032\n"
    "ds_read_b64_tr_b16 v[142:143], v238 offset:44544\n"
    "buffer_load_dwordx4 v[72:75], v236, %[ws], s41 offen offset:2048\n"
    "s_waitcnt lgkmcnt(4)\n"
    "v_mfma_f32_32x32x16_bf16 v[0:15], v[76:79], v[128:131], v[0:15]\n"
    "s_waitcnt vmcnt(7)\n"
    "s_barrier\n"
    "ds_read_b64_tr_b16 v[112:113], v237 offset:16384\n"
    "ds_read_b64_tr_b16 v[114:115], v237 offset:16896\n"
    "v_mfma_f32_32x32x16_bf16 v[16:31], v[76:79], v[132:135], v[16:31]\n"
    "ds_read_b64_tr_b16 v[116:117], v237 offset:45056\n"
    "ds_read_b64_tr_b16 v[118:119], v237 offset:45568\n"
    "ds_read_b64_tr_b16 v[144:145], v239 offset:16384\n"
    "buffer_load_dwordx4 v236, %[ws], s40 offen offset:1024 sc1 lds\n"
    "s_waitcnt lgkmcnt(5)\n"
    "v_mfma_f32_32x32x16_bf16 v[32:47], v[76:79], v[136:139], v[32:47]\n"
    "ds_read_b64_tr_b16 v[120:121], v238 offset:16384\n"
    "ds_read_b64_tr_b16 v[122:123], v238 offset:16896\n"
    "ds_read_b64_tr_b16 v[146:147], v239 offset:16896\n"
    "v_mfma_f32_32x32x16_bf16 v[48:63], v[76:79], v[140:143], v[48:63]\n"
    "ds_read_b64_tr_b16 v[124:125], v238 offset:45056\n"
    "ds_read_b64_tr_b16 v[126:127], v238 offset:45568\n"
    "ds_read_b64_tr_b16 v[148:149], v239 offset:16416\n"
    "buffer_load_dwordx4 v[76:79], v236, %[ws], s41 offen offset:3072\n"
    "s_waitcnt lgkmcnt(7)\n"
    "v_mfma_f32_32x32x16_bf16 v[0:15], v[80:83], v[112:115], v[0:15]\n"
    "ds_read_b64_tr_b16 v[128:129], v237 offset:17408\n"
    "ds_read_b64_tr_b16 v[130:131], v237 offset:17920\n"
    "ds_read_b64_tr_b16 v[150:151], v239 offset:16928\n"
    "s_waitcnt lgkmcnt(6)\n"
    "v_mfma_f32_16x16x32_bf16 v[152:155], v[144:147], v[144:147], v[152:155]\n"
    "v_mfma_f32_32x32x16_bf16 v[16:31], v[80:83], v[116:119], v[16:31]\n"
    "ds_read_b64_tr_b16 v[132:133], v237 offset:46080\n"
    "ds_read_b64_tr_b16 v[134:135], v237 offset:46592\n"
    "buffer_load_dwordx4 v236, %[ws], s40 offen offset:2048 sc1 lds\n"
    "s_waitcnt lgkmcnt(6)\n"
    "v_mfma_f32_32x32x16_bf16 v[32:47], v[80:83], v[120:123], v[32:47]\n"
    "ds_read_b64_tr_b16 v[136:137], v238 offset:17408\n"
    "ds_read_b64_tr_b16 v[138:139], v238 offset:17920\n"
    "s_waitcnt lgkmcnt(4)\n"
    "v_mfma_f32_16x16x32_bf16 v[156:159], v[148:151], v[148:151], v[156:159]\n"
    "v_mfma_f32_32x32x16_bf16 v[48:63], v[80:83], v[124:127], v[48:63]\n"
    "ds_read_b64_tr_b16 v[140:141], v238 offset:46080\n"
    "ds_read_b64_tr_b16 v[142:143], v238 offset:46592\n"
    "buffer_load_dwordx4 v236, %[ws], s40 offen offset:3072 sc1 lds\n"
    "s_waitcnt lgkmcnt(4)\n"
    "v_mfma_f32_32x32x16_bf16 v[0:15], v[84:87], v[128:131], v[0:15]\n"
    "s_waitcnt vmcnt(7)\n"
    "s_barrier\n"
    "ds_read_b64_tr_b16 v[112:113], v237 offset:18432\n"
    "ds_read_b64_tr_b16 v[114:115], v237 offset:18944\n"
    "v_mfma_f32_32x32x16_bf16 v[16:31], v[84:87], v[132:135], v[16:31]\n"
    "ds_read_b64_tr_b16 v[116:117], v237 offset:47104\n"
    "ds_read_b64_tr_b16 v[118:119], v237 offset:47616\n"
    "ds_read_b64_tr_b16 v[144:145], v239 offset:18432\n"
    "s_add_u32 m0, m0, 4096\n"
    "s_add_u32 s40, s40, 4096\n"
    "buffer_load_dwordx4 v236, %[ws], s40 offen offset:0 sc1 lds\n"
    "s_waitcnt lgkmcnt(5)\n"
    "v_mfma_f32_32x32x16_bf16 v[32:47], v[84:87], v[136:139], v[32:47]\n"
    "ds_read_b64_tr_b16 v[120:121], v238 offset:18432\n"
    "ds_read_b64_tr_b16 v[122:123], v238 offset:18944\n"
    "ds_read_b64_tr_b16 v[146:147], v239 offset:18944\n"
    "v_mfma_f32_32x32x16_bf16 v[48:63], v[84:87], v[140:143], v[48:63]\n"
    "ds_read_b64_tr_b16 v[124:125], v238 offset:47104\n"
    "ds_read_b64_tr_b16 v[126:127], v238 offset:47616\n"
    "ds_read_b64_tr_b16 v[148:149], v239 offset:18464\n"
    "buffer_load_dwordx4 v236, %[ws], s40 offen offset:1024 sc1 lds\n"
    "s_waitcnt lgkmcnt(7)\n"
    "v_mfma_f32_32x32x16_bf16 v[0:15], v[88:91], v[112:115], v[0:15]\n"
    "ds_read_b64_tr_b16 v[128:129], v237 offset:19456\n"
    "ds_read_b64_tr_b16 v[130:131], v237 offset:19968\n"
    "ds_read_b64_tr_b16 v[150:151], v239 offset:18976\n"
    "s_waitcnt lgkmcnt(6)\n"
    "v_mfma_f32_16x16x32_bf16 v[152:155], v[144:147], v[144:147], v[152:155]\n"
    "v_mfma_f32_32x32x16_bf16 v[16:31], v[88:91], v[116:119], v[16:31]\n"
    "ds_read_b64_tr_b16 v[132:133], v237 offset:48128\n"
    "ds_read_b64_tr_b16 v[134:135], v237 offset:48640\n"
    "buffer_load_dwordx4 v236, %[ws], s40 offen offset:2048 sc1 lds\n"
    "s_waitcnt lgkmcnt(6)\n"
    "v_mfma_f32_32x32x16_bf16 v[32:47], v[88:91], v[120:123], v[32:47]\n"
    "ds_read_b64_tr_b16 v[136:137], v238 offset:19456\n"
    "ds_read_b64_tr_b16 v[138:139], v238 offset:19968\n"
    "s_waitcnt lgkmcnt(4)\n"
    "v_mfma_f32_16x16x32_bf16 v[156:159], v[148:151], v[148:151], v[156:159]\n"
    "v_mfma_f32_32x32x16_bf16 v[48:63], v[88:91], v[124:127], v[48:63]\n"
    "ds_read_b64_tr_b16 v[140:141], v238 offset:48128\n"
    "ds_read_b64_tr_b16 v[142:143], v238 offset:48640\n"
    "buffer_load_dwordx4 v236, %[ws], s40 offen offset:3072 sc1 lds\n"
    "s_waitcnt lgkmcnt(4)\n"
    "v_mfma_f32_32x32x16_bf16 v[0:15], v[92:95], v[128:131], v[0:15]\n"
    "s_waitcnt vmcnt(7)\n"
    "s_barrier\n"
    "ds_read_b64_tr_b16 v[112:113], v237 offset:20480\n"
    "ds_read_b64_tr_b16 v[114:115], v237 offset:20992\n"
    "v_mfma_f32_32x32x16_bf16 v[16:31], v[92:95], v[132:135], v[16:31]\n"
    "ds_read_b64_tr_b16 v[116:117], v237 offset:49152\n"
    "ds_read_b64_tr_b16 v[118:119], v237 offset:49664\n"
    "ds_read_b64_tr_b16 v[144:145], v239 offset:20480\n"
    "buffer_load_dwordx4 a[12:15], v236, %[ws], s58 offen offset:3072\n"
    "s_waitcnt lgkmcnt(5)\n"
    "v_mfma_f32_32x32x16_bf16 v[32:47], v[92:95], v[136:139], v[32:47]\n"
    "ds_read_b64_tr_b16 v[120:121], v238 offset:20480\n"
    "ds_read_b64_tr_b16 v[122:123], v238 offset:20992\n"
    "ds_read_b64_tr_b16 v[146:147], v239 offset:20992\n"
    "v_mfma_f32_32x32x16_bf16 v[48:63], v[92:95], v[140:143], v[48:63]\n"
    "ds_read_b64_tr_b16 v[124:125], v238 offset:49152\n"
    "ds_read_b64_tr_b16 v[126:127], v238 offset:49664\n"
    "ds_read_b64_tr_b16 v[148:149], v239 offset:20512\n"
    "buffer_load_dwordx4 a[124:127], v236, %[ws], s59 offen offset:3072\n"
    "s_waitcnt lgkmcnt(7)\n"
    "v_mfma_f32_32x32x16_bf16 v[0:15], v[96:99], v[112:115], v[0:15]\n"
    "ds_read_b64_tr_b16 v[128:129], v237 offset:21504\n"
    "ds_read_b64_tr_b16 v[130:131], v237 offset:22016\n"
    "ds_read_b64_tr_b16 v[150:151], v239 offset:21024\n"
    "s_waitcnt lgkmcnt(6)\n"
    "v_mfma_f32_16x16x32_bf16 v[152:155], v[144:147], v[144:147], v[152:155]\n"
    "v_mfma_f32_32x32x16_bf16 v[16:31], v[96:99], v[116:119], v[16:31]\n"
    "ds_read_b64_tr_b16 v[132:133], v237 offset:50176\n"
    "ds_read_b64_tr_b16 v[134:135], v237 offset:50688\n"
    "s_add_u32 s58, s58, 4096\n"
    "buffer_load_dwordx4 a[16:19], v236, %[ws], s58 offen offset:0\n"
    "s_waitcnt lgkmcnt(6)\n"
    "v_mfma_f32_32x32x16_bf16 v[32:47], v[96:99], v[120:123], v[32:47]\n"
    "ds_read_b64_tr_b16 v[136:137], v238 offset:21504\n"
    "ds_read_b64_tr_b16 v[138:139], v238 offset:22016\n"
    "s_waitcnt lgkmcnt(4)\n"
    "v_mfma_f32_16x16x32_bf16 v[156:159], v[148:151], v[148:151], v[156:159]\n"
    "v_mfma_f32_32x32x16_bf16 v[48:63], v[96:99], v[124:127], v[48:63]\n"
    "ds_read_b64_tr_b16 v[140:141], v238 offset:50176\n"
    "ds_read_b64_tr_b16 v[142:143], v238 offset:50688\n"
    "s_add_u32 s59, s59, 4096\n"
    "buffer_load_dwordx4 a[128:131], v236, %[ws], s59 offen offset:0\n"
    "s_waitcnt lgkmcnt(4)\n"
    "v_mfma_f32_32x32x16_bf16 v[0:15], v[100:103], v[128:131], v[0:15]\n"
    "s_waitcnt vmcnt(8)\n"
    "s_barrier\n"
    "ds_read_b64_tr_b16 v[112:113], v237 offset:22528\n"
    "ds_read_b64_tr_b16 v[114:115], v237 offset:23040\n"
    "v_mfma_f32_32x32x16_bf16 v[16:31], v[100:103], v[132:135], v[16:31]\n"
    "ds_read_b64_tr_b16 v[116:117], v237 offset:51200\n"
    "ds_read_b64_tr_b16 v[118:119], v237 offset:51712\n"
    "ds_read_b64_tr_b16 v[144:145], v239 offset:22528\n"
    "buffer_load_dwordx4 a[20:23], v236, %[ws], s58 offen offset:1024\n"
    "s_waitcnt lgkmcnt(5)\n"
    "v_mfma_f32_32x32x16_bf16 v[32:47], v[100:103], v[136:139], v[32:47]\n"
    "ds_read_b64_tr_b16 v[120:121], v238 offset:22528\n"
    "ds_read_b64_tr_b16 v[122:123], v238 offset:23040\n"
    "ds_read_b64_tr_b16 v[146:147], v239 offset:23040\n"
    "v_mfma_f32_32x32x16_bf16 v[48:63], v[100:103], v[140:143], v[48:63]\n"
    "ds_read_b64_tr_b16 v[124:125], v238 offset:51200\n"
    "ds_read_b64_tr_b16 v[126:127], v238 offset:51712\n"
    "ds_read_b64_tr_b16 v[148:149], v239 offset:22560\n"
    "buffer_load_dwordx4 a[132:135], v236, %[ws], s59 offen offset:1024\n"
    "s_waitcnt lgkmcnt(7)\n"
    "v_mfma_f32_32x32x16_bf16 v[0:15], v[104:107], v[112:115], v[0:15]\n"
    "ds_read_b64_tr_b16 v[128:129], v237 offset:23552\n"
    "ds_read_b64_tr_b16 v[130:131], v237 offset:24064\n"
    "ds_read_b64_tr_b16 v[150:151], v239 offset:23072\n"
    "s_waitcnt lgkmcnt(6)\n"
    "v_mfma_f32_16x16x32_bf16 v[152:155], v[144:147], v[144:147], v[152:155]\n"
    "v_mfma_f32_32x32x16_bf16 v[16:31], v[104:107], v[116:119], v[16:31]\n"
    "ds_read_b64_tr_b16 v[132:133], v237 offset:52224\n"
    "ds_read_b64_tr_b16 v[134:135], v237 offset:52736\n"
    "buffer_load_dwordx4 a[24:27], v236, %[ws], s58 offen offset:2048\n"
    "s_waitcnt lgkmcnt(6)\n"
    "v_mfma_f32_32x32x16_bf16 v[32:47], v[104:107], v[120:123], v[32:47]\n"
    "ds_read_b64_tr_b16 v[136:137], v238 offset:23552\n"
    "ds_read_b64_tr_b16 v[138:139], v238 offset:24064\n"
    "s_waitcnt lgkmcnt(4)\n"
    "v_mfma_f32_16x16x32_bf16 v[156:159], v[148:151], v[148:151], v[156:159]\n"
    "v_mfma_f32_32x32x16_bf16 v[48:63], v[104:107], v[124:127], v[48:63]\n"
    "ds_read_b64_tr_b16 v[140:141], v238 offset:52224\n"
    "ds_read_b64_tr_b16 v[142:143], v238 offset:52736\n"
    "buffer_load_dwordx4 a[136:139], v236, %[ws], s59 offen offset:2048\n"
    "s_waitcnt lgkmcnt(4)\n"
    "v_mfma_f32_32x32x16_bf16 v[0:15], v[108:111], v[128:131], v[0:15]\n"
    "s_waitcnt vmcnt(10)\n"
    "s_barrier\n"
    "ds_read_b64_tr_b16 v[112:113], v237 offset:24576\n"
    "ds_read_b64_tr_b16 v[114:115], v237 offset:25088\n"
    "v_mfma_f32_32x32x16_bf16 v[16:31], v[108:111], v[132:135], v[16:31]\n"
    "ds_read_b64_tr_b16 v[116:117], v237 offset:53248\n"
    "ds_read_b64_tr_b16 v[118:119], v237 offset:53760\n"
    "ds_read_b64_tr_b16 v[144:145], v239 offset:24576\n"
    "buffer_load_dwordx4 a[28:31], v236, %[ws], s58 offen offset:3072\n"
    "s_waitcnt lgkmcnt(5)\n"
    "v_mfma_f32_32x32x16_bf16 v[32:47], v[108:111], v[136:139], v[32:47]\n"
    "ds_read_b64_tr_b16 v[120:121], v238 offset:24576\n"
    "ds_read_b64_tr_b16 v[122:123], v238 offset:25088\n"
    "ds_read_b64_tr_b16 v[146:147], v239 offset:25088\n"
    "v_mfma_f32_32x32x16_bf16 v[48:63], v[108:111], v[140:143], v[48:63]\n"
    "ds_read_b64_tr_b16 v[124:125], v238 offset:53248\n"
    "ds_read_b64_tr_b16 v[126:127], v238 offset:53760\n"
    "ds_read_b64_tr_b16 v[148:149], v239 offset:24608\n"
    "buffer_load_dwordx4 a[140:143], v236, %[ws], s59 offen offset:3072\n"
    "s_waitcnt lgkmcnt(7)\n"
    "v_mfma_f32_32x32x16_bf16 v[0:15], v[64:67], v[112:115], v[0:15]\n"
    "ds_read_b64_tr_b16 v[128:129], v237 offset:25600\n"
    "ds_read_b64_tr_b16 v[130:131], v237 offset:26112\n"
    "ds_read_b64_tr_b16 v[150:151], v239 offset:25120\n"
    "s_waitcnt lgkmcnt(6)\n"
    "v_mfma_f32_16x16x32_bf16 v[152:155], v[144:147], v[144:147], v[152:155]\n"
    "v_mfma_f32_32x32x16_bf16 v[16:31], v[64:67], v[116:119], v[16:31]\n"
    "ds_read_b64_tr_b16 v[132:133], v237 offset:54272\n"
    "ds_read_b64_tr_b16 v[134:135], v237 offset:54784\n"
    "s_add_u32 s58, s58, 4096\n"
    "buffer_load_dwordx4 a[32:35], v236, %[ws], s58 offen offset:0\n"
    "s_waitcnt lgkmcnt(6)\n"
    "v_mfma_f32_32x32x16_bf16 v[32:47], v[64:67], v[120:123], v[32:47]\n"
    "ds_read_b64_tr_b16 v[136:137], v238 offset:25600\n"
    "ds_read_b64_tr_b16 v[138:139], v238 offset:26112\n"
    "s_waitcnt lgkmcnt(4)\n"
    "v_mfma_f32_16x16x32_bf16 v[156:159], v[148:151], v[148:151], v[156:159]\n"
    "v_mfma_f32_32x32x16_bf16 v[48:63], v[64:67], v[124:127], v[48:63]\n"
    "ds_read_b64_tr_b16 v[140:141], v238 offset:54272\n"
    "ds_read_b64_tr_b16 v[142:143], v238 offset:54784\n"
    "s_add_u32 s59, s59, 4096\n"
    "buffer_load_dwordx4 a[144:147], v236, %[ws], s59 offen offset:0\n"
    "s_waitcnt lgkmcnt(4)\n"
    "v_mfma_f32_32x32x16_bf16 v[0:15], v[68:71], v[128:131], v[0:15]\n"
    "s_waitcnt vmcnt(12)\n"
    "s_barrier\n"
    "ds_read_b64_tr_b16 v[112:113], v237 offset:26624\n"
    "ds_read_b64_tr_b16 v[114:115], v237 offset:27136\n"
    "v_mfma_f32_32x32x16_bf16 v[16:31], v[68:71], v[132:135], v[16:31]\n"
    "ds_read_b64_tr_b16 v[116:117], v237 offset:55296\n"
    "ds_read_b64_tr_b16 v[118:119], v237 offset:55808\n"
    "ds_read_b64_tr_b16 v[144:145], v239 offset:26624\n"
    "buffer_load_dwordx4 a[36:39], v236, %[ws], s58 offen offset:1024\n"
    "s_waitcnt lgkmcnt(5)\n"
    "v_mfma_f32_32x32x16_bf16 v[32:47], v[68:71], v[136:139], v[32:47]\n"
    "ds_read_b64_tr_b16 v[120:121], v238 offset:26624\n"
    "ds_read_b64_tr_b16 v[122:123], v238 offset:27136\n"
    "ds_read_b64_tr_b16 v[146:147], v239 offset:27136\n"
    "v_mfma_f32_32x32x16_bf16 v[48:63], v[68:71], v[140:143], v[48:63]\n"
    "ds_read_b64_tr_b16 v[124:125], v238 offset:55296\n"
    "ds_read_b64_tr_b16 v[126:127], v238 offset:55808\n"
    "ds_read_b64_tr_b16 v[148:149], v239 offset:26656\n"
    "buffer_load_dwordx4 a[148:151], v236, %[ws], s59 offen offset:1024\n"
    "s_waitcnt lgkmcnt(7)\n"
    "v_mfma_f32_32x32x16_bf16 v[0:15], v[72:75], v[112:115], v[0:15]\n"
    "ds_read_b64_tr_b16 v[128:129], v237 offset:27648\n"
    "ds_read_b64_tr_b16 v[130:131], v237 offset:28160\n"
    "ds_read_b64_tr_b16 v[150:151], v239 offset:27168\n"
    "s_waitcnt lgkmcnt(6)\n"
    "v_mfma_f32_16x16x32_bf16 v[152:155], v[144:147], v[144:147], v[152:155]\n"
    "v_mfma_f32_32x32x16_bf16 v[16:31], v[72:75], v[116:119], v[16:31]\n"
    "ds_read_b64_tr_b16 v[132:133], v237 offset:56320\n"
    "ds_read_b64_tr_b16 v[134:135], v237 offset:56832\n"
    "buffer_load_dwordx4 a[40:43], v236, %[ws], s58 offen offset:2048\n"
    "s_waitcnt lgkmcnt(6)\n"
    "v_mfma_f32_32x32x16_bf16 v[32:47], v[72:75], v[120:123], v[32:47]\n"
    "ds_read_b64_tr_b16 v[136:137], v238 offset:27648\n"
    "ds_read_b64_tr_b16 v[138:139], v238 offset:28160\n"
    "s_waitcnt lgkmcnt(4)\n"
    "v_mfma_f32_16x16x32_bf16 v[156:159], v[148:151], v[148:151], v[156:159]\n"
    "v_mfma_f32_32x32x16_bf16 v[48:63], v[72:75], v[124:127], v[48:63]\n"
    "ds_read_b64_tr_b16 v[140:141], v238 offset:56320\n"
    "ds_read_b64_tr_b16 v[142:143], v238 offset:56832\n"
    "buffer_load_dwordx4 a[152:155], v236, %[ws], s59 offen offset:2048\n"
    "v_lshrrev_b32 v243, 5, v255\n"
    "v_lshlrev_b32 v243, 4, v243\n"
    "v_add_u32 v243, s47, v243\n"
    "s_waitcnt lgkmcnt(4)\n"
    "v_mfma_f32_32x32x16_bf16 v[0:15], v[76:79], v[128:131], v[0:15]\n"
    "ds_read_b128 v[216:219], v243 offset:0\n"
    "ds_read_b128 v[220:223], v243 offset:32\n"
    "ds_read_b128 v[224:227], v243 offset:64\n"
    "ds_read_b128 v[228:231], v243 offset:96\n"
    "v_mov_b32 v242, 0x3b124984\n"
    "s_lshl_b32 s56, %[w], 2\n"
    "s_or_b64 s[50:51], s[50:51], s[52:53]\n"
    "v_cndmask_b32_e64 v152, v152, v153, s[48:49]\n"
    "v_cndmask_b32_e64 v154, v154, v155, s[52:53]\n"
    "v_cndmask_b32_e64 v156, v156, v157, s[48:49]\n"
    "v_cndmask_b32_e64 v158, v158, v159, s[52:53]\n"
    "v_cndmask_b32_e64 v152, v152, v154, s[50:51]\n"
    "v_cndmask_b32_e64 v156, v156, v158, s[50:51]\n"
    "v_and_b32 v243, 15, v255\n"
    "s_add_u32 s56, s56, %[lds]\n"
    "v_lshl_add_u32 v243, v243, 4, s56\n"
    "v_mfma_f32_32x32x16_bf16 v[16:31], v[76:79], v[132:135], v[16:31]\n"
    "buffer_load_dwordx4 a[44:47], v236, %[ws], s58 offen offset:3072\n"
    "v_fmaak_f32 v152, v242, v152, 0x2edbe6ff\n"
    "v_fmaak_f32 v156, v242, v156, 0x2edbe6ff\n"
    "v_add_u32 v243, 151552, v243\n"
    "v_rsq_f32 v152, v152\n"
    "v_rsq_f32 v156, v156\n"
    "v_mul_f32 v152, 0x3eeff558, v152\n"
    "v_mul_f32 v156, 0x3eeff558, v156\n"
    "s_mov_b32 exec_lo, 0x00f0000f\n"
    "s_mov_b32 exec_hi, 0xf0000f00\n"
    "ds_write_b32 v243, v152\n"
    "ds_write_b32 v243, v156 offset:256\n"
    "s_mov_b64 exec, -1\n"
    "s_waitcnt lgkmcnt(6)\n"
    "s_nop 2\n"
    "v_mfma_f32_32x32x16_bf16 v[32:47], v[76:79], v[136:139], v[32:47]\n"
    "s_waitcnt lgkmcnt(0)\n"
    "s_barrier\n"
    "ds_read_b128 v[232:235], v245\n"
    "v_mfma_f32_32x32x16_bf16 v[48:63], v[76:79], v[140:143], v[48:63]\n"
    "buffer_load_dwordx4 a[156:159], v236, %[ws], s59 offen offset:3072\n"
    "s_waitcnt lgkmcnt(0)\n"
    "v_pk_fma_f32 v[96:97], v[0:1], v[232:233], v[216:217] op_sel:[0,0,0] op_sel_hi:[1,0,1]\n"
    "v_pk_fma_f32 v[98:99], v[2:3], v[232:233], v[218:219] op_sel:[0,0,0] op_sel_hi:[1,0,1]\n"
    "v_pk_fma_f32 v[108:109], v[96:97], v[96:97], v[240:241] op_sel:[0,0,0] op_sel_hi:[1,1,0]\n"
    "s_add_u32 s58, s58, 4096\n"
    "buffer_load_dwordx4 a[48:51], v236, %[ws], s58 offen offset:0\n"
    "v_pk_fma_f32 v[110:111], v[98:99], v[98:99], v[240:241] op_sel:[0,0,0] op_sel_hi:[1,1,0]\n"
    "v_pk_mul_f32 v[108:109], v[96:97], v[108:109] neg_lo:[1,0] neg_hi:[1,0]\n"
    "v_pk_mul_f32 v[110:111], v[98:99], v[110:111] neg_lo:[1,0] neg_hi:[1,0]\n"
    "v_exp_f32 v108, v108\n"
    "v_pk_fma_f32 v[100:101], v[4:5], v[232:233], v[220:221] op_sel:[0,0,0] op_sel_hi:[1,0,1]\n"
    "v_exp_f32 v109, v109\n"
    "v_pk_fma_f32 v[102:103], v[6:7], v[232:233], v[222:223] op_sel:[0,0,0] op_sel_hi:[1,0,1]\n"
    "v_exp_f32 v110, v110\n"
    "v_pk_fma_f32 v[112:113], v[100:101], v[100:101], v[240:241] op_sel:[0,0,0] op_sel_hi:[1,1,0]\n"
    "s_add_u32 s59, s59, 4096\n"
    "buffer_load_dwordx4 a[160:163], v236, %[ws], s59 offen offset:0\n"
    "v_exp_f32 v111, v111\n"
    "v_pk_fma_f32 v[114:115], v[102:103], v[102:103], v[240:241] op_sel:[0,0,0] op_sel_hi:[1,1,0]\n"
    "v_pk_mul_f32 v[112:113], v[100:101], v[112:113] neg_lo:[1,0] neg_hi:[1,0]\n"
    "v_pk_mul_f32 v[114:115], v[102:103], v[114:115] neg_lo:[1,0] neg_hi:[1,0]\n"
    "v_pk_fma_f32 v[108:109], v[108:109], v[240:241], v[240:241] op_sel:[0,1,1] op_sel_hi:[1,1,1]\n"
    "v_pk_fma_f32 v[110:111], v[110:111], v[240:241], v[240:241] op_sel:[0,1,1] op_sel_hi:[1,1,1]\n"
    "v_exp_f32 v112, v112\n"
    "v_pk_fma_f32 v[104:105], v[8:9], v[232:233], v[224:225] op_sel:[0,0,0] op_sel_hi:[1,0,1]\n"
    "v_exp_f32 v113, v113\n"
    "v_pk_fma_f32 v[106:107], v[10:11], v[232:233], v[226:227] op_sel:[0,0,0] op_sel_hi:[1,0,1]\n"
    "v_exp_f32 v114, v114\n"
    "v_pk_fma_f32 v[116:117], v[104:105], v[104:105], v[240:241] op_sel:[0,0,0] op_sel_hi:[1,1,0]\n"
    "v_exp_f32 v115, v115\n"
    "v_pk_fma_f32 v[118:119], v[106:107], v[106:107], v[240:241] op_sel:[0,0,0] op_sel_hi:[1,1,0]\n"
    "v_rcp_f32 v108, v108\n"
    "v_pk_mul_f32 v[116:117], v[104:105], v[116:117] neg_lo:[1,0] neg_hi:[1,0]\n"
    "v_rcp_f32 v109, v109\n"
    "v_pk_mul_f32 v[118:119], v[106:107], v[118:119] neg_lo:[1,0] neg_hi:[1,0]\n"
    "v_rcp_f32 v110, v110\n"
    "v_pk_fma_f32 v[112:113], v[112:113], v[240:241], v[240:241] op_sel:[0,1,1] op_sel_hi:[1,1,1]\n"
    "v_rcp_f32 v111, v111\n"
    "v_pk_mul_f32 v[108:109], v[96:97], v[108:109]\n"
    "v_pk_fma_f32 v[114:115], v[114:115], v[240:241], v[240:241] op_sel:[0,1,1] op_sel_hi:[1,1,1]\n"
    "v_pk_mul_f32 v[110:111], v[98:99], v[110:111]\n"
    "v_cvt_pk_bf16_f32 v120, v108, v109\n"
    "v_cvt_pk_bf16_f32 v121, v110, v111\n"
    "v_exp_f32 v116, v116\n"
    "v_pk_fma_f32 v[96:97], v[12:13], v[232:233], v[228:229] op_sel:[0,0,0] op_sel_hi:[1,0,1]\n"
    "v_exp_f32 v117, v117\n"
    "v_pk_fma_f32 v[98:99], v[14:15], v[232:233], v[230:231] op_sel:[0,0,0] op_sel_hi:[1,0,1]\n"
    "v_exp_f32 v118, v118\n"
    "v_pk_fma_f32 v[108:109], v[96:97], v[96:97], v[240:241] op_sel:[0,0,0] op_sel_hi:[1,1,0]\n"
    "v_exp_f32 v119, v119\n"
    "v_pk_fma_f32 v[110:111], v[98:99], v[98:99], v[240:241] op_sel:[0,0,0] op_sel_hi:[1,1,0]\n"
    "v_rcp_f32 v112, v112\n"
    "v_pk_mul_f32 v[108:109], v[96:97], v[108:109] neg_lo:[1,0] neg_hi:[1,0]\n"
    "v_rcp_f32 v113, v113\n"
    "v_pk_mul_f32 v[110:111], v[98:99], v[110:111] neg_lo:[1,0] neg_hi:[1,0]\n"
    "v_rcp_f32 v114, v114\n"
    "v_pk_fma_f32 v[116:117], v[116:117], v[240:241], v[240:241] op_sel:[0,1,1] op_sel_hi:[1,1,1]\n"
    "v_rcp_f32 v115, v115\n"
    "v_pk_mul_f32 v[112:113], v[100:101], v[112:113]\n"
    "v_pk_fma_f32 v[118:119], v[118:119], v[240:241], v[240:241] op_sel:[0,1,1] op_sel_hi:[1,1,1]\n"
    "v_pk_mul_f32 v[114:115], v[102:103], v[114:115]\n"
    "v_cvt_pk_bf16_f32 v122, v112, v113\n"
    "v_cvt_pk_bf16_f32 v123, v114, v115\n"
    "buffer_store_dwordx4 v[120:123], v236, %[ws], s45 offen offset:0 sc1\n"
    "v_exp_f32 v108, v108\n"
    "v_pk_fma_f32 v[100:101], v[16:17], v[232:233], v[216:217] op_sel:[0,1,0] op_sel_hi:[1,1,1]\n"
    "v_exp_f32 v109, v109\n"
    "v_pk_fma_f32 v[102:103], v[18:19], v[232:233], v[218:219] op_sel:[0,1,0] op_sel_hi:[1,1,1]\n"
    "v_exp_f32 v110, v110\n"
    "v_pk_fma_f32 v[112:113], v[100:101], v[100:101], v[240:241] op_sel:[0,0,0] op_sel_hi:[1,1,0]\n"
    "v_exp_f32 v111, v111\n"
    "v_pk_fma_f32 v[114:115], v[102:103], v[102:103], v[240:241] op_sel:[0,0,0] op_sel_hi:[1,1,0]\n"
    "v_rcp_f32 v116, v116\n"
    "v_pk_mul_f32 v[112:113], v[100:101], v[112:113] neg_lo:[1,0] neg_hi:[1,0]\n"
    "v_rcp_f32 v117, v117\n"
    "v_pk_mul_f32 v[114:115], v[102:103], v[114:115] neg_lo:[1,0] neg_hi:[1,0]\n"
    "v_rcp_f32 v118, v118\n"
    "v_pk_fma_f32 v[108:109], v[108:109], v[240:241], v[240:241] op_sel:[0,1,1] op_sel_hi:[1,1,1]\n"
    "v_rcp_f32 v119, v119\n"
    "v_pk_mul_f32 v[116:117], v[104:105], v[116:117]\n"
    "v_pk_fma_f32 v[110:111], v[110:111], v[240:241], v[240:241] op_sel:[0,1,1] op_sel_hi:[1,1,1]\n"
    "v_pk_mul_f32 v[118:119], v[106:107], v[118:119]\n"
    "v_cvt_pk_bf16_f32 v124, v116, v117\n"
    "v_cvt_pk_bf16_f32 v125, v118, v119\n"
    "v_exp_f32 v112, v112\n"
    "v_pk_fma_f32 v[104:105], v[20:21], v[232:233], v[220:221] op_sel:[0,1,0] op_sel_hi:[1,1,1]\n"
    "v_exp_f32 v113, v113\n"
    "v_pk_fma_f32 v[106:107], v[22:23], v[232:233], v[222:223] op_sel:[0,1,0] op_sel_hi:[1,1,1]\n"
    "v_exp_f32 v114, v114\n"
    "v_pk_fma_f32 v[116:117], v[104:105], v[104:105], v[240:241] op_sel:[0,0,0] op_sel_hi:[1,1,0]\n"
    "v_exp_f32 v115, v115\n"
    "v_pk_fma_f32 v[118:119], v[106:107], v[106:107], v[240:241] op_sel:[0,0,0] op_sel_hi:[1,1,0]\n"
    "v_rcp_f32 v108, v108\n"
    "v_pk_mul_f32 v[116:117], v[104:105], v[116:117] neg_lo:[1,0] neg_hi:[1,0]\n"
    "v_rcp_f32 v109, v109\n"
    "v_pk_mul_f32 v[118:119], v[106:107], v[118:119] neg_lo:[1,0] neg_hi:[1,0]\n"
    "v_rcp_f32 v110, v110\n"
    "v_pk_fma_f32 v[112:113], v[112:113], v[240:241], v[240:241] op_sel:[0,1,1] op_sel_hi:[1,1,1]\n"
    "v_rcp_f32 v111, v111\n"
    "v_pk_mul_f32 v[108:109], v[96:97], v[108:109]\n"
    "v_pk_fma_f32 v[114:115], v[114:115], v[240:241], v[240:241] op_sel:[0,1,1] op_sel_hi:[1,1,1]\n"
    "v_pk_mul_f32 v[110:111], v[98:99], v[110:111]\n"
    "v_cvt_pk_bf16_f32 v126, v108, v109\n"
    "v_cvt_pk_bf16_f32 v127, v110, v111\n"
    "buffer_store_dwordx4 v[124:127], v236, %[ws], s45 offen offset:1024 sc1\n"
    "s_add_u32 s45, s45, 114688\n"
    "v_exp_f32 v116, v116\n"
    "v_pk_fma_f32 v[96:97], v[24:25], v[232:233], v[224:225] op_sel:[0,1,0] op_sel_hi:[1,1,1]\n"
    "v_exp_f32 v117, v117\n"
    "v_pk_fma_f32 v[98:99], v[26:27], v[232:233], v[226:227] op_sel:[0,1,0] op_sel_hi:[1,1,1]\n"
    "v_exp_f32 v118, v118\n"
    "v_pk_fma_f32 v[108:109], v[96:97], v[96:97], v[240:241] op_sel:[0,0,0] op_sel_hi:[1,1,0]\n"
    "v_exp_f32 v119, v119\n"
    "v_pk_fma_f32 v[110:111], v[98:99], v[98:99], v[240:241] op_sel:[0,0,0] op_sel_hi:[1,1,0]\n"
    "v_rcp_f32 v112, v112\n"
    "v_pk_mul_f32 v[108:109], v[96:97], v[108:109] neg_lo:[1,0] neg_hi:[1,0]\n"
    "v_rcp_f32 v113, v113\n"
    "v_pk_mul_f32 v[110:111], v[98:99], v[110:111] neg_lo:[1,0] neg_hi:[1,0]\n"
    "v_rcp_f32 v114, v114\n"
    "v_pk_fma_f32 v[116:117], v[116:117], v[240:241], v[240:241] op_sel:[0,1,1] op_sel_hi:[1,1,1]\n"
    "v_rcp_f32 v115, v115\n"
    "v_pk_mul_f32 v[112:113], v[100:101], v[112:113]\n"
    "v_pk_fma_f32 v[118:119], v[118:119], v[240:241], v[240:241] op_sel:[0,1,1] op_sel_hi:[1,1,1]\n"
    "v_pk_mul_f32 v[114:115], v[102:103], v[114:115]\n"
    "v_cvt_pk_bf16_f32 v120, v112, v113\n"
    "v_cvt_pk_bf16_f32 v121, v114, v115\n"
    "v_exp_f32 v108, v108\n"
    "v_pk_fma_f32 v[100:101], v[28:29], v[232:233], v[228:229] op_sel:[0,1,0] op_sel_hi:[1,1,1]\n"
    "v_exp_f32 v109, v109\n"
    "v_pk_fma_f32 v[102:103], v[30:31], v[232:233], v[230:231] op_sel:[0,1,0] op_sel_hi:[1,1,1]\n"
    "v_exp_f32 v110, v110\n"
    "v_pk_fma_f32 v[112:113], v[100:101], v[100:101], v[240:241] op_sel:[0,0,0] op_sel_hi:[1,1,0]\n"
    "v_exp_f32 v111, v111\n"
    "v_pk_fma_f32 v[114:115], v[102:103], v[102:103], v[240:241] op_sel:[0,0,0] op_sel_hi:[1,1,0]\n"
    "v_rcp_f32 v116, v116\n"
    "v_pk_mul_f32 v[112:113], v[100:101], v[112:113] neg_lo:[1,0] neg_hi:[1,0]\n"
    "v_rcp_f32 v117, v117\n"
    "v_pk_mul_f32 v[114:115], v[102:103], v[114:115] neg_lo:[1,0] neg_hi:[1,0]\n"
    "v_rcp_f32 v118, v118\n"
    "v_pk_fma_f32 v[108:109], v[108:109], v[240:241], v[240:241] op_sel:[0,1,1] op_sel_hi:[1,1,1]\n"
    "v_rcp_f32 v119, v119\n"
    "v_pk_mul_f32 v[116:117], v[104:105], v[116:117]\n"
    "v_pk_fma_f32 v[110:111], v[110:111], v[240:241], v[240:241] op_sel:[0,1,1] op_sel_hi:[1,1,1]\n"
    "v_pk_mul_f32 v[118:119], v[106:107], v[118:119]\n"
    "v_cvt_pk_bf16_f32 v122, v116, v117\n"
    "v_cvt_pk_bf16_f32 v123, v118, v119\n"
    "buffer_store_dwordx4 v[120:123], v236, %[ws], s45 offen offset:0 sc1\n"
    "v_exp_f32 v112, v112\n"
    "v_pk_fma_f32 v[104:105], v[32:33], v[234:235], v[216:217] op_sel:[0,0,0] op_sel_hi:[1,0,1]\n"
    "v_exp_f32 v113, v113\n"
    "v_pk_fma_f32 v[106:107], v[34:35], v[234:235], v[218:219] op_sel:[0,0,0] op_sel_hi:[1,0,1]\n"
    "v_exp_f32 v114, v114\n"
    "v_pk_fma_f32 v[116:117], v[104:105], v[104:105], v[240:241] op_sel:[0,0,0] op_sel_hi:[1,1,0]\n"
    "v_exp_f32 v115, v115\n"
    "v_pk_fma_f32 v[118:119], v[106:107], v[106:107], v[240:241] op_sel:[0,0,0] op_sel_hi:[1,1,0]\n"
    "v_rcp_f32 v108, v108\n"
    "v_pk_mul_f32 v[116:117], v[104:105], v[116:117] neg_lo:[1,0] neg_hi:[1,0]\n"
    "v_rcp_f32 v109, v109\n"
    "v_pk_mul_f32 v[118:119], v[106:107], v[118:119] neg_lo:[1,0] neg_hi:[1,0]\n"
    "v_rcp_f32 v110, v110\n"
    "v_pk_fma_f32 v[112:113], v[112:113], v[240:241], v[240:241] op_sel:[0,1,1] op_sel_hi:[1,1,1]\n"
    "v_rcp_f32 v111, v111\n"
    "v_pk_mul_f32 v[108:109], v[96:97], v[108:109]\n"
    "v_pk_fma_f32 v[114:115], v[114:115], v[240:241], v[240:241] op_sel:[0,1,1] op_sel_hi:[1,1,1]\n"
    "v_pk_mul_f32 v[110:111], v[98:99], v[110:111]\n"
    "v_cvt_pk_bf16_f32 v124, v108, v109\n"
    "v_cvt_pk_bf16_f32 v125, v110, v111\n"
    "v_exp_f32 v116, v116\n"
    "v_pk_fma_f32 v[96:97], v[36:37], v[234:235], v[220:221] op_sel:[0,0,0] op_sel_hi:[1,0,1]\n"
    "v_exp_f32 v117, v117\n"
    "v_pk_fma_f32 v[98:99], v[38:39], v[234:235], v[222:223] op_sel:[0,0,0] op_sel_hi:[1,0,1]\n"
    "v_exp_f32 v118, v118\n"
    "v_pk_fma_f32 v[108:109], v[96:97], v[96:97], v[240:241] op_sel:[0,0,0] op_sel_hi:[1,1,0]\n"
    "buffer_load_dwordx4 a[52:55], v236, %[ws], s58 offen offset:1024\n"
    "v_exp_f32 v119, v119\n"
    "v_pk_fma_f32 v[110:111], v[98:99], v[98:99], v[240:241] op_sel:[0,0,0] op_sel_hi:[1,1,0]\n"
    "v_rcp_f32 v112, v112\n"
    "v_pk_mul_f32 v[108:109], v[96:97], v[108:109] neg_lo:[1,0] neg_hi:[1,0]\n"
    "v_rcp_f32 v113, v113\n"
    "v_pk_mul_f32 v[110:111], v[98:99], v[110:111] neg_lo:[1,0] neg_hi:[1,0]\n"
    "v_rcp_f32 v114, v114\n"
    "v_pk_fma_f32 v[116:117], v[116:117], v[240:241], v[240:241] op_sel:[0,1,1] op_sel_hi:[1,1,1]\n"
    "v_rcp_f32 v115, v115\n"
    "v_pk_mul_f32 v[112:113], v[100:101], v[112:113]\n"
    "v_pk_fma_f32 v[118:119], v[118:119], v[240:241], v[240:241] op_sel:[0,1,1] op_sel_hi:[1,1,1]\n"
    "v_pk_mul_f32 v[114:115], v[102:103], v[114:115]\n"
    "v_cvt_pk_bf16_f32 v126, v112, v113\n"
    "v_cvt_pk_bf16_f32 v127, v114, v115\n"
    "buffer_store_dwordx4 v[124:127], v236, %[ws], s45 offen offset:1024 sc1\n"
    "s_add_u32 s45, s45, 114688\n"
    "v_exp_f32 v108, v108\n"
    "v_pk_fma_f32 v[100:101], v[40:41], v[234:235], v[224:225] op_sel:[0,0,0] op_sel_hi:[1,0,1]\n"
    "v_exp_f32 v109, v109\n"
    "v_pk_fma_f32 v[102:103], v[42:43], v[234:235], v[226:227] op_sel:[0,0,0] op_sel_hi:[1,0,1]\n"
    "v_exp_f32 v110, v110\n"
    "v_pk_fma_f32 v[112:113], v[100:101], v[100:101], v[240:241] op_sel:[0,0,0] op_sel_hi:[1,1,0]\n"
    "buffer_load_dwordx4 a[164:167], v236, %[ws], s59 offen offset:1024\n"
    "v_exp_f32 v111, v111\n"
    "v_pk_fma_f32 v[114:115], v[102:103], v[102:103], v[240:241] op_sel:[0,0,0] op_sel_hi:[1,1,0]\n"
    "v_rcp_f32 v116, v116\n"
    "v_pk_mul_f32 v[112:113], v[100:101], v[112:113] neg_lo:[1,0] neg_hi:[1,0]\n"
    "v_rcp_f32 v117, v117\n"
    "v_pk_mul_f32 v[114:115], v[102:103], v[114:115] neg_lo:[1,0] neg_hi:[1,0]\n"
    "v_rcp_f32 v118, v118\n"
    "v_pk_fma_f32 v[108:109], v[108:109], v[240:241], v[240:241] op_sel:[0,1,1] op_sel_hi:[1,1,1]\n"
    "v_rcp_f32 v119, v119\n"
    "v_pk_mul_f32 v[116:117], v[104:105], v[116:117]\n"
    "v_pk_fma_f32 v[110:111], v[110:111], v[240:241], v[240:241] op_sel:[0,1,1] op_sel_hi:[1,1,1]\n"
    "v_pk_mul_f32 v[118:119], v[106:107], v[118:119]\n"
    "v_cvt_pk_bf16_f32 v120, v116, v117\n"
    "v_cvt_pk_bf16_f32 v121, v118, v119\n"
    "v_exp_f32 v112, v112\n"
    "v_pk_fma_f32 v[104:105], v[44:45], v[234:235], v[228:229] op_sel:[0,0,0] op_sel_hi:[1,0,1]\n"
    "v_exp_f32 v113, v113\n"
    "v_pk_fma_f32 v[106:107], v[46:47], v[234:235], v[230:231] op_sel:[0,0,0] op_sel_hi:[1,0,1]\n"
    "v_exp_f32 v114, v114\n"
    "v_pk_fma_f32 v[116:117], v[104:105], v[104:105], v[240:241] op_sel:[0,0,0] op_sel_hi:[1,1,0]\n"
    "buffer_load_dwordx4 a[56:59], v236, %[ws], s58 offen offset:2048\n"
    "v_exp_f32 v115, v115\n"
    "v_pk_fma_f32 v[118:119], v[106:107], v[106:107], v[240:241] op_sel:[0,0,0] op_sel_hi:[1,1,0]\n"
    "v_rcp_f32 v108, v108\n"
    "v_pk_mul_f32 v[116:117], v[104:105], v[116:117] neg_lo:[1,0] neg_hi:[1,0]\n"
    "v_rcp_f32 v109, v109\n"
    "v_pk_mul_f32 v[118:119], v[106:107], v[118:119] neg_lo:[1,0] neg_hi:[1,0]\n"
    "v_rcp_f32 v110, v110\n"
    "v_pk_fma_f32 v[112:113], v[112:113], v[240:241], v[240:241] op_sel:[0,1,1] op_sel_hi:[1,1,1]\n"
    "v_rcp_f32 v111, v111\n"
    "v_pk_mul_f32 v[108:109], v[96:97], v[108:109]\n"
    "v_pk_fma_f32 v[114:115], v[114:115], v[240:241], v[240:241] op_sel:[0,1,1] op_sel_hi:[1,1,1]\n"
    "v_pk_mul_f32 v[110:111], v[98:99], v[110:111]\n"
    "v_cvt_pk_bf16_f32 v122, v108, v109\n"
    "v_cvt_pk_bf16_f32 v123, v110, v111\n"
    "buffer_store_dwordx4 v[120:123], v236, %[ws], s45 offen offset:0 sc1\n"
    "v_exp_f32 v116, v116\n"
    "v_pk_fma_f32 v[96:97], v[48:49], v[234:235], v[216:217] op_sel:[0,1,0] op_sel_hi:[1,1,1]\n"
    "v_exp_f32 v117, v117\n"
    "v_pk_fma_f32 v[98:99], v[50:51], v[234:235], v[218:219] op_sel:[0,1,0] op_sel_hi:[1,1,1]\n"
    "v_exp_f32 v118, v118\n"
    "v_pk_fma_f32 v[108:109], v[96:97], v[96:97], v[240:241] op_sel:[0,0,0] op_sel_hi:[1,1,0]\n"
    "buffer_load_dwordx4 a[168:171], v236, %[ws], s59 offen offset:2048\n"
    "v_exp_f32 v119, v119\n"
    "v_pk_fma_f32 v[110:111], v[98:99], v[98:99], v[240:241] op_sel:[0,0,0] op_sel_hi:[1,1,0]\n"
    "v_rcp_f32 v112, v112\n"
    "v_pk_mul_f32 v[108:109], v[96:97], v[108:109] neg_lo:[1,0] neg_hi:[1,0]\n"
    "v_rcp_f32 v113, v113\n"
    "v_pk_mul_f32 v[110:111], v[98:99], v[110:111] neg_lo:[1,0] neg_hi:[1,0]\n"
    "v_rcp_f32 v114, v114\n"
    "v_pk_fma_f32 v[116:117], v[116:117], v[240:241], v[240:241] op_sel:[0,1,1] op_sel_hi:[1,1,1]\n"
    "v_rcp_f32 v115, v115\n"
    "v_pk_mul_f32 v[112:113], v[100:101], v[112:113]\n"
    "v_pk_fma_f32 v[118:119], v[118:119], v[240:241], v[240:241] op_sel:[0,1,1] op_sel_hi:[1,1,1]\n"
    "v_pk_mul_f32 v[114:115], v[102:103], v[114:115]\n"
    "v_cvt_pk_bf16_f32 v124, v112, v113\n"
    "v_cvt_pk_bf16_f32 v125, v114, v115\n"
    "v_exp_f32 v108, v108\n"
    "v_pk_fma_f32 v[100:101], v[52:53], v[234:235], v[220:221] op_sel:[0,1,0] op_sel_hi:[1,1,1]\n"
    "v_exp_f32 v109, v109\n"
    "v_pk_fma_f32 v[102:103], v[54:55], v[234:235], v[222:223] op_sel:[0,1,0] op_sel_hi:[1,1,1]\n"
    "v_exp_f32 v110, v110\n"
    "v_pk_fma_f32 v[112:113], v[100:101], v[100:101], v[240:241] op_sel:[0,0,0] op_sel_hi:[1,1,0]\n"
    "buffer_load_dwordx4 a[60:63], v236, %[ws], s58 offen offset:3072\n"
    "v_exp_f32 v111, v111\n"
    "v_pk_fma_f32 v[114:115], v[102:103], v[102:103], v[240:241] op_sel:[0,0,0] op_sel_hi:[1,1,0]\n"
    "v_rcp_f32 v116, v116\n"
    "v_pk_mul_f32 v[112:113], v[100:101], v[112:113] neg_lo:[1,0] neg_hi:[1,0]\n"
    "v_rcp_f32 v117, v117\n"
    "v_pk_mul_f32 v[114:115], v[102:103], v[114:115] neg_lo:[1,0] neg_hi:[1,0]\n"
    "v_rcp_f32 v118, v118\n"
    "v_pk_fma_f32 v[108:109], v[108:109], v[240:241], v[240:241] op_sel:[0,1,1] op_sel_hi:[1,1,1]\n"
    "v_rcp_f32 v119, v119\n"
    "v_pk_mul_f32 v[116:117], v[104:105], v[116:117]\n"
    "v_pk_fma_f32 v[110:111], v[110:111], v[240:241], v[240:241] op_sel:[0,1,1] op_sel_hi:[1,1,1]\n"
    "v_pk_mul_f32 v[118:119], v[106:107], v[118:119]\n"
    "v_cvt_pk_bf16_f32 v126, v116, v117\n"
    "v_cvt_pk_bf16_f32 v127, v118, v119\n"
    "buffer_store_dwordx4 v[124:127], v236, %[ws], s45 offen offset:1024 sc1\n"
    "s_add_u32 s45, s45, 114688\n"
    "v_exp_f32 v112, v112\n"
    "v_pk_fma_f32 v[104:105], v[56:57], v[234:235], v[224:225] op_sel:[0,1,0] op_sel_hi:[1,1,1]\n"
    "v_exp_f32 v113, v113\n"
    "v_pk_fma_f32 v[106:107], v[58:59], v[234:235], v[226:227] op_sel:[0,1,0] op_sel_hi:[1,1,1]\n"
    "v_exp_f32 v114, v114\n"
    "v_pk_fma_f32 v[116:117], v[104:105], v[104:105], v[240:241] op_sel:[0,0,0] op_sel_hi:[1,1,0]\n"
    "buffer_load_dwordx4 a[172:175], v236, %[ws], s59 offen offset:3072\n"
    "v_exp_f32 v115, v115\n"
    "v_pk_fma_f32 v[118:119], v[106:107], v[106:107], v[240:241] op_sel:[0,0,0] op_sel_hi:[1,1,0]\n"
    "v_rcp_f32 v108, v108\n"
    "v_pk_mul_f32 v[116:117], v[104:105], v[116:117] neg_lo:[1,0] neg_hi:[1,0]\n"
    "v_rcp_f32 v109, v109\n"
    "v_pk_mul_f32 v[118:119], v[106:107], v[118:119] neg_lo:[1,0] neg_hi:[1,0]\n"
    "v_rcp_f32 v110, v110\n"
    "v_pk_fma_f32 v[112:113], v[112:113], v[240:241], v[240:241] op_sel:[0,1,1] op_sel_hi:[1,1,1]\n"
    "v_rcp_f32 v111, v111\n"
    "v_pk_mul_f32 v[108:109], v[96:97], v[108:109]\n"
    "v_pk_fma_f32 v[114:115], v[114:115], v[240:241], v[240:241] op_sel:[0,1,1] op_sel_hi:[1,1,1]\n"
    "v_pk_mul_f32 v[110:111], v[98:99], v[110:111]\n"
    "v_cvt_pk_bf16_f32 v120, v108, v109\n"
    "v_cvt_pk_bf16_f32 v121, v110, v111\n"
    "v_exp_f32 v116, v116\n"
    "v_pk_fma_f32 v[96:97], v[60:61], v[234:235], v[228:229] op_sel:[0,1,0] op_sel_hi:[1,1,1]\n"
    "v_exp_f32 v117, v117\n"
    "v_pk_fma_f32 v[98:99], v[62:63], v[234:235], v[230:231] op_sel:[0,1,0] op_sel_hi:[1,1,1]\n"
    "v_exp_f32 v118, v118\n"
    "v_pk_fma_f32 v[108:109], v[96:97], v[96:97], v[240:241] op_sel:[0,0,0] op_sel_hi:[1,1,0]\n"
    "s_add_u32 s58, s58, 4096\n"
    "buffer_load_dwordx4 a[64:67], v236, %[ws], s58 offen offset:0\n"
    "v_exp_f32 v119, v119\n"
    "v_pk_fma_f32 v[110:111], v[98:99], v[98:99], v[240:241] op_sel:[0,0,0] op_sel_hi:[1,1,0]\n"
    "v_rcp_f32 v112, v112\n"
    "v_pk_mul_f32 v[108:109], v[96:97], v[108:109] neg_lo:[1,0] neg_hi:[1,0]\n"
    "v_rcp_f32 v113, v113\n"
    "v_pk_mul_f32 v[110:111], v[98:99], v[110:111] neg_lo:[1,0] neg_hi:[1,0]\n"
    "v_rcp_f32 v114, v114\n"
    "v_pk_fma_f32 v[116:117], v[116:117], v[240:241], v[240:241] op_sel:[0,1,1] op_sel_hi:[1,1,1]\n"
    "v_rcp_f32 v115, v115\n"
    "v_pk_mul_f32 v[112:113], v[100:101], v[112:113]\n"
    "v_pk_fma_f32 v[118:119], v[118:119], v[240:241], v[240:241] op_sel:[0,1,1] op_sel_hi:[1,1,1]\n"
    "v_pk_mul_f32 v[114:115], v[102:103], v[114:115]\n"
    "v_cvt_pk_bf16_f32 v122, v112, v113\n"
    "v_cvt_pk_bf16_f32 v123, v114, v115\n"
    "buffer_store_dwordx4 v[120:123], v236, %[ws], s45 offen offset:0 sc1\n"
    "s_waitcnt vmcnt(9)\n"
    "v_mov_b32 v242, 1\n"
    "s_mov_b64 exec, 1\n"
    "buffer_store_dword v242, off, %[ws], %[fw] sc1\n"
    "s_mov_b64 exec, -1\n"
    "v_exp_f32 v108, v108\n"
    "v_exp_f32 v109, v109\n"
    "v_exp_f32 v110, v110\n"
    "s_add_u32 s59, s59, 4096\n"
    "buffer_load_dwordx4 a[176:179], v236, %[ws], s59 offen offset:0\n"
    "v_exp_f32 v111, v111\n"
    "v_rcp_f32 v116, v116\n"
    "v_rcp_f32 v117, v117\n"
    "v_rcp_f32 v118, v118\n"
    "v_pk_fma_f32 v[108:109], v[108:109], v[240:241], v[240:241] op_sel:[0,1,1] op_sel_hi:[1,1,1]\n"
    "v_rcp_f32 v119, v119\n"
    "v_pk_mul_f32 v[116:117], v[104:105], v[116:117]\n"
    "v_pk_fma_f32 v[110:111], v[110:111], v[240:241], v[240:241] op_sel:[0,1,1] op_sel_hi:[1,1,1]\n"
    "v_pk_mul_f32 v[118:119], v[106:107], v[118:119]\n"
    "v_cvt_pk_bf16_f32 v124, v116, v117\n"
    "v_cvt_pk_bf16_f32 v125, v118, v119\n"
    "buffer_load_dwordx4 a[68:71], v236, %[ws], s58 offen offset:1024\n"
    "v_rcp_f32 v108, v108\n"
    "v_rcp_f32 v109, v109\n"
    "v_rcp_f32 v110, v110\n"
    "v_rcp_f32 v111, v111\n"
    "v_pk_mul_f32 v[108:109], v[96:97], v[108:109]\n"
    "v_pk_mul_f32 v[110:111], v[98:99], v[110:111]\n"
    "v_cvt_pk_bf16_f32 v126, v108, v109\n"
    "v_cvt_pk_bf16_f32 v127, v110, v111\n"
    "buffer_store_dwordx4 v[124:127], v236, %[ws], s45 offen offset:1024 sc1\n"
    "s_add_u32 s45, s45, 114688\n"
    "ds_read_b64_tr_b16 v[96:97], v250 offset:0\n"
    "ds_read_b64_tr_b16 v[98:99], v250 offset:512\n"
    "ds_read_b64_tr_b16 v[100:101], v250 offset:57344\n"
    "ds_read_b64_tr_b16 v[102:103], v250 offset:57856\n"
    "ds_read_b64_tr_b16 v[104:105], v250 offset:1024\n"
    "ds_read_b64_tr_b16 v[106:107], v250 offset:1536\n"
    "ds_read_b64_tr_b16 v[108:109], v250 offset:58368\n"
    "ds_read_b64_tr_b16 v[110:111], v250 offset:58880\n"
    "v_fmamk_f32 v216, v251, 0x42000000, v253\n"
    "v_fmamk_f32 v217, v252, 0x42000000, v254\n"
    "v_cos_f32 v218, v216\n"
    "v_sin_f32 v219, v216\n"
    "v_cos_f32 v220, v217\n"
    "s_waitcnt lgkmcnt(6)\n"
    "v_mfma_f32_32x32x16_bf16 a[224:239], v[160:163], v[96:99], 0\n"
    "v_sin_f32 v221, v217\n"
    "v_cvt_pk_bf16_f32 v176, v218, -v219\n"
    "v_cvt_pk_bf16_f32 v204, v220, -v221\n"
    "buffer_load_dwordx4 a[180:183], v236, %[ws], s59 offen offset:1024\n"
    "v_fmamk_f32 v216, v251, 0x42040000, v253\n"
    "v_mfma_f32_32x32x16_bf16 a[240:255], v[188:191], v[96:99], 0\n"
    "v_fmamk_f32 v217, v252, 0x42040000, v254\n"
    "v_cos_f32 v218, v216\n"
    "v_sin_f32 v219, v216\n"
    "v_cos_f32 v220, v217\n"
    "s_waitcnt lgkmcnt(4)\n"
    "v_mfma_f32_32x32x16_bf16 v[64:79], v[160:163], v[100:103], 0\n"
    "v_sin_f32 v221, v217\n"
    "v_cvt_pk_bf16_f32 v177, v218, -v219\n"
    "buffer_load_dwordx4 a[72:75], v236, %[ws], s58 offen offset:2048\n"
    "v_cvt_pk_bf16_f32 v205, v220, -v221\n"
    "v_fmamk_f32 v216, v251, 0x42100000, v253\n"
    "v_mfma_f32_32x32x16_bf16 v[80:95], v[188:191], v[100:103], 0\n"
    "v_fmamk_f32 v217, v252, 0x42100000, v254\n"
    "v_cos_f32 v218, v216\n"
    "v_sin_f32 v219, v216\n"
    "v_cos_f32 v220, v217\n"
    "ds_read_b64_tr_b16 v[96:97], v250 offset:2048\n"
    "ds_read_b64_tr_b16 v[98:99], v250 offset:2560\n"
    "s_waitcnt lgkmcnt(4)\n"
    "v_mfma_f32_32x32x16_bf16 a[224:239], v[164:167], v[104:107], a[224:239]\n"
    "v_sin_f32 v221, v217\n"
    "buffer_load_dwordx4 a[184:187], v236, %[ws], s59 offen offset:2048\n"
    "v_cvt_pk_bf16_f32 v178, v218, -v219\n"
    "v_cvt_pk_bf16_f32 v206, v220, -v221\n"
    "v_fmamk_f32 v216, v251, 0x42140000, v253\n"
    "v_mfma_f32_32x32x16_bf16 a[240:255], v[192:195], v[104:107], a[240:255]\n"
    "v_fmamk_f32 v217, v252, 0x42140000, v254\n"
    "v_cos_f32 v218, v216\n"
    "v_sin_f32 v219, v216\n"
    "v_cos_f32 v220, v217\n"
    "ds_read_b64_tr_b16 v[100:101], v250 offset:59392\n"
    "ds_read_b64_tr_b16 v[102:103], v250 offset:59904\n"
    "s_waitcnt lgkmcnt(4)\n"
    "v_mfma_f32_32x32x16_bf16 v[64:79], v[164:167], v[108:111], v[64:79]\n"
    "v_sin_f32 v221, v217\n"
    "buffer_load_dwordx4 a[76:79], v236, %[ws], s58 offen offset:3072\n"
    "v_cvt_pk_bf16_f32 v179, v218, -v219\n"
    "v_cvt_pk_bf16_f32 v207, v220, -v221\n"
    "v_fmamk_f32 v216, v251, 0x42200000, v253\n"
    "v_mfma_f32_32x32x16_bf16 v[80:95], v[192:195], v[108:111], v[80:95]\n"
    "v_fmamk_f32 v217, v252, 0x42200000, v254\n"
    "v_cos_f32 v218, v216\n"
    "v_sin_f32 v219, v216\n"
    "v_cos_f32 v220, v217\n"
    "buffer_load_dword v247, v246, %[ws], %[fr] offen sc1\n"
    "ds_read_b64_tr_b16 v[104:105], v250 offset:3072\n"
    "ds_read_b64_tr_b16 v[106:107], v250 offset:3584\n"
    "s_waitcnt lgkmcnt(4)\n"
    "v_mfma_f32_32x32x16_bf16 a[224:239], v[168:171], v[96:99], a[224:239]\n"
    "v_sin_f32 v221, v217\n"
    "buffer_load_dwordx4 a[188:191], v236, %[ws], s59 offen offset:3072\n"
    "v_cvt_pk_bf16_f32 v180, v218, -v219\n"
    "v_cvt_pk_bf16_f32 v208, v220, -v221\n"
    "v_fmamk_f32 v216, v251, 0x42240000, v253\n"
    "v_mfma_f32_32x32x16_bf16 a[240:255], v[196:199], v[96:99], a[240:255]\n"
    "v_fmamk_f32 v217, v252, 0x42240000, v254\n"
    "v_cos_f32 v218, v216\n"
    "v_sin_f32 v219, v216\n"
    "v_cos_f32 v220, v217\n"
    "ds_read_b64_tr_b16 v[108:109], v250 offset:60416\n"
    "ds_read_b64_tr_b16 v[110:111], v250 offset:60928\n"
    "s_waitcnt lgkmcnt(4)\n"
    "v_mfma_f32_32x32x16_bf16 v[64:79], v[168:171], v[100:103], v[64:79]\n"
    "v_sin_f32 v221, v217\n"
    "s_add_u32 s58, s58, 4096\n"
    "buffer_load_dwordx4 a[80:83], v236, %[ws], s58 offen offset:0\n"
    "v_cvt_pk_bf16_f32 v181, v218, -v219\n"
    "v_cvt_pk_bf16_f32 v209, v220, -v221\n"
    "v_fmamk_f32 v216, v251, 0x42300000, v253\n"
    "v_mfma_f32_32x32x16_bf16 v[80:95], v[196:199], v[100:103], v[80:95]\n"
    "v_fmamk_f32 v217, v252, 0x42300000, v254\n"
    "v_cos_f32 v218, v216\n"
    "v_sin_f32 v219, v216\n"
    "v_cos_f32 v220, v217\n"
    "ds_read_b64_tr_b16 v[96:97], v250 offset:4096\n"
    "ds_read_b64_tr_b16 v[98:99], v250 offset:4608\n"
    "s_waitcnt lgkmcnt(4)\n"
    "v_mfma_f32_32x32x16_bf16 a[224:239], v[172:175], v[104:107], a[224:239]\n"
    "v_sin_f32 v221, v217\n"
    "s_add_u32 s59, s59, 4096\n"
    "buffer_load_dwordx4 a[192:195], v236, %[ws], s59 offen offset:0\n"
    "v_cvt_pk_bf16_f32 v182, v218, -v219\n"
    "v_cvt_pk_bf16_f32 v210, v220, -v221\n"
    "v_fmamk_f32 v216, v251, 0x42340000, v253\n"
    "v_mfma_f32_32x32x16_bf16 a[240:255], v[200:203], v[104:107], a[240:255]\n"
    "v_fmamk_f32 v217, v252, 0x42340000, v254\n"
    "v_cos_f32 v218, v216\n"
    "v_sin_f32 v219, v216\n"
    "v_cos_f32 v220, v217\n"
    "ds_read_b64_tr_b16 v[100:101], v250 offset:61440\n"
    "ds_read_b64_tr_b16 v[102:103], v250 offset:61952\n"
    "s_waitcnt lgkmcnt(4)\n"
    "v_mfma_f32_32x32x16_bf16 v[64:79], v[172:175], v[108:111], v[64:79]\n"
    "v_sin_f32 v221, v217\n"
    "buffer_load_dwordx4 a[84:87], v236, %[ws], s58 offen offset:1024\n"
    "v_cvt_pk_bf16_f32 v183, v218, -v219\n"
    "v_cvt_pk_bf16_f32 v211, v220, -v221\n"
    "v_fmamk_f32 v216, v251, 0x42400000, v253\n"
    "v_mfma_f32_32x32x16_bf16 v[80:95], v[200:203], v[108:111], v[80:95]\n"
    "v_fmamk_f32 v217, v252, 0x42400000, v254\n"
    "v_cos_f32 v218, v216\n"
    "v_sin_f32 v219, v216\n"
    "v_cos_f32 v220, v217\n"
    "ds_read_b64_tr_b16 v[104:105], v250 offset:5120\n"
    "ds_read_b64_tr_b16 v[106:107], v250 offset:5632\n"
    "s_waitcnt lgkmcnt(4)\n"
    "v_mfma_f32_32x32x16_bf16 a[224:239], v[176:179], v[96:99], a[224:239]\n"
    "v_sin_f32 v221, v217\n"
    "buffer_load_dwordx4 a[196:199], v236, %[ws], s59 offen offset:1024\n"
    "v_cvt_pk_bf16_f32 v184, v218, -v219\n"
    "v_cvt_pk_bf16_f32 v212, v220, -v221\n"
    "v_fmamk_f32 v216, v251, 0x42440000, v253\n"
    "v_mfma_f32_32x32x16_bf16 a[240:255], v[204:207], v[96:99], a[240:255]\n"
    "v_fmamk_f32 v217, v252, 0x42440000, v254\n"
    "v_cos_f32 v218, v216\n"
    "v_sin_f32 v219, v216\n"
    "v_cos_f32 v220, v217\n"
    "ds_read_b64_tr_b16 v[108:109], v250 offset:62464\n"
    "ds_read_b64_tr_b16 v[110:111], v250 offset:62976\n"
    "s_waitcnt lgkmcnt(4)\n"
    "v_mfma_f32_32x32x16_bf16 v[64:79], v[176:179], v[100:103], v[64:79]\n"
    "v_sin_f32 v221, v217\n"
    "buffer_load_dwordx4 a[88:91], v236, %[ws], s58 offen offset:2048\n"
    "v_cvt_pk_bf16_f32 v185, v218, -v219\n"
    "v_cvt_pk_bf16_f32 v213, v220, -v221\n"
    "v_fmamk_f32 v216, v251, 0x42500000, v253\n"
    "v_mfma_f32_32x32x16_bf16 v[80:95], v[204:207], v[100:103], v[80:95]\n"
    "v_fmamk_f32 v217, v252, 0x42500000, v254\n"
    "v_cos_f32 v218, v216\n"
    "v_sin_f32 v219, v216\n"
    "v_cos_f32 v220, v217\n"
    "ds_read_b64_tr_b16 v[96:97], v250 offset:6144\n"
    "ds_read_b64_tr_b16 v[98:99], v250 offset:6656\n"
    "s_waitcnt lgkmcnt(4)\n"
    "v_mfma_f32_32x32x16_bf16 a[224:239], v[180:183], v[104:107], a[224:239]\n"
    "v_sin_f32 v221, v217\n"
    "buffer_load_dwordx4 a[200:203], v236, %[ws], s59 offen offset:2048\n"
    "v_cvt_pk_bf16_f32 v186, v218, -v219\n"
    "v_cvt_pk_bf16_f32 v214, v220, -v221\n"
    "v_fmamk_f32 v216, v251, 0x42540000, v253\n"
    "v_mfma_f32_32x32x16_bf16 a[240:255], v[208:211], v[104:107], a[240:255]\n"
    "v_fmamk_f32 v217, v252, 0x42540000, v254\n"
    "v_cos_f32 v218, v216\n"
    "v_sin_f32 v219, v216\n"
    "v_cos_f32 v220, v217\n"
    "ds_read_b64_tr_b16 v[100:101], v250 offset:63488\n"
    "ds_read_b64_tr_b16 v[102:103], v250 offset:64000\n"
    "s_waitcnt lgkmcnt(4)\n"
    "v_mfma_f32_32x32x16_bf16 v[64:79], v[180:183], v[108:111], v[64:79]\n"
    "v_sin_f32 v221, v217\n"
    "buffer_load_dwordx4 a[92:95], v236, %[ws], s58 offen offset:3072\n"
    "v_cvt_pk_bf16_f32 v187, v218, -v219\n"
    "v_cvt_pk_bf16_f32 v215, v220, -v221\n"
    "v_mfma_f32_32x32x16_bf16 v[80:95], v[208:211], v[108:111], v[80:95]\n"
    "buffer_load_dwordx4 a[204:207], v236, %[ws], s59 offen offset:3072\n"
    "s_waitcnt lgkmcnt(2)\n"
    "v_mfma_f32_32x32x16_bf16 a[224:239], v[184:187], v[96:99], a[224:239]\n"
    "v_mfma_f32_32x32x16_bf16 a[240:255], v[212:215], v[96:99], a[240:255]\n"
    "s_add_u32 s58, s58, 4096\n"
    "buffer_load_dwordx4 a[96:99], v236, %[ws], s58 offen offset:0\n"
    "s_waitcnt lgkmcnt(0)\n"
    "v_mfma_f32_32x32x16_bf16 v[64:79], v[184:187], v[100:103], v[64:79]\n"
    "v_mfma_f32_32x32x16_bf16 v[80:95], v[212:215], v[100:103], v[80:95]\n"
    "v_and_b32 v243, 7, v255\n"
    "v_lshlrev_b32 v243, 5, v243\n"
    "v_add_u32 v243, s47, v243\n"
    "ds_read_b128 v[160:163], v243 offset:1024\n"
    "ds_read_b128 v[164:167], v243 offset:1040\n"
    "s_add_u32 s59, s59, 4096\n"
    "buffer_load_dwordx4 a[208:211], v236, %[ws], s59 offen offset:0\n"
    "v_lshrrev_b32 v250, 3, v255\n"
    "v_mul_u32_u24 v250, 28672, v250\n"
    "v_and_b32 v243, 7, v255\n"
    "v_lshl_add_u32 v250, v243, 4, v250\n"
    "v_mov_b32 v168, 0x3d88d677\n"
    "s_waitcnt lgkmcnt(0)\n"
    "buffer_load_dwordx4 a[100:103], v236, %[ws], s58 offen offset:1024\n"
    "buffer_load_dwordx4 a[212:215], v236, %[ws], s59 offen offset:1024\n"
    "buffer_load_dwordx4 a[104:107], v236, %[ws], s58 offen offset:2048\n"
    "buffer_load_dwordx4 a[216:219], v236, %[ws], s59 offen offset:2048\n"
    "buffer_load_dwordx4 a[108:111], v236, %[ws], s58 offen offset:3072\n"
    "buffer_load_dwordx4 a[220:223], v236, %[ws], s59 offen offset:3072\n"
    "s_add_u32 s58, %[hl], 28672\n"
    "s_waitcnt vmcnt(17)\n"
    "v_cmp_gt_u32 vcc, 1, v247\n"
    "s_nop 4\n"
    "s_cbranch_vccnz LS1_%=\n"
    "LR1_%=:\n"
    "v_mov_b32 v242, 2\n"
    "s_mov_b64 exec, 1\n"
    "buffer_store_dword v242, off, %[ws], %[fw] sc1\n"
    "s_mov_b64 exec, -1\n"
    "buffer_load_dwordx4 v[0:3], v236, %[ws], s44 offen offset:0 sc1\n"
    "buffer_load_dwordx4 v[4:7], v236, %[ws], s44 offen offset:1024 sc1\n"
    "buffer_load_dwordx4 v[8:11], v236, %[ws], s44 offen offset:2048 sc1\n"
    "buffer_load_dwordx4 v[12:15], v236, %[ws], s44 offen offset:3072 sc1\n"
    "s_add_u32 s44, s44, 4096\n"
    "buffer_load_dwordx4 v[16:19], v236, %[ws], s44 offen offset:0 sc1\n"
    "buffer_load_dwordx4 v[20:23], v236, %[ws], s44 offen offset:1024 sc1\n"
    "buffer_load_dwordx4 v[24:27], v236, %[ws], s44 offen offset:2048 sc1\n"
    "buffer_load_dwordx4 v[28:31], v236, %[ws], s44 offen offset:3072 sc1\n"
    "s_add_u32 s44, s44, 4096\n"
    "buffer_load_dwordx4 v[32:35], v236, %[ws], s44 offen offset:0 sc1\n"
    "buffer_load_dwordx4 v[36:39], v236, %[ws], s44 offen offset:1024 sc1\n"
    "buffer_load_dwordx4 v[40:43], v236, %[ws], s44 offen offset:2048 sc1\n"
    "buffer_load_dwordx4 v[44:47], v236, %[ws], s44 offen offset:3072 sc1\n"
    "s_add_u32 s44, s44, 4096\n"
    "buffer_load_dwordx4 v[48:51], v236, %[ws], s44 offen offset:0 sc1\n"
    "s_waitcnt vmcnt(12)\n"
    "v_mfma_f32_32x32x16_bf16 a[224:239], a[0:3], v[0:3], a[224:239]\n"
    "v_mfma_f32_32x32x16_bf16 a[240:255], a[112:115], v[0:3], a[240:255]\n"
    "buffer_load_dwordx4 v[52:55], v236, %[ws], s44 offen offset:1024 sc1\n"
    "s_waitcnt vmcnt(12)\n"
    "v_mfma_f32_32x32x16_bf16 a[224:239], a[4:7], v[4:7], a[224:239]\n"
    "v_mfma_f32_32x32x16_bf16 a[240:255], a[116:119], v[4:7], a[240:255]\n"
    "buffer_load_dwordx4 v[56:59], v236, %[ws], s44 offen offset:2048 sc1\n"
    "s_waitcnt vmcnt(12)\n"
    "v_mfma_f32_32x32x16_bf16 a[224:239], a[8:11], v[8:11], a[224:239]\n"
    "v_mfma_f32_32x32x16_bf16 a[240:255], a[120:123], v[8:11], a[240:255]\n"
    "buffer_load_dwordx4 v[60:63], v236, %[ws], s44 offen offset:3072 sc1\n"
    "s_waitcnt vmcnt(12)\n"
    "v_mfma_f32_32x32x16_bf16 a[224:239], a[12:15], v[12:15], a[224:239]\n"
    "v_mfma_f32_32x32x16_bf16 a[240:255], a[124:127], v[12:15], a[240:255]\n"
    "s_add_u32 s44, s44, 4096\n"
    "buffer_load_dwordx4 v[0:3], v236, %[ws], s44 offen offset:0 sc1\n"
    "s_waitcnt vmcnt(12)\n"
    "v_mfma_f32_32x32x16_bf16 a[224:239], a[16:19], v[16:19], a[224:239]\n"
    "v_mfma_f32_32x32x16_bf16 a[240:255], a[128:131], v[16:19], a[240:255]\n"
    "buffer_load_dwordx4 v[4:7], v236, %[ws], s44 offen offset:1024 sc1\n"
    "s_waitcnt vmcnt(12)\n"
    "v_mfma_f32_32x32x16_bf16 a[224:239], a[20:23], v[20:23], a[224:239]\n"
    "v_mfma_f32_32x32x16_bf16 a[240:255], a[132:135], v[20:23], a[240:255]\n"
    "buffer_load_dwordx4 v[8:11], v236, %[ws], s44 offen offset:2048 sc1\n"
    "s_waitcnt vmcnt(12)\n"
    "v_mfma_f32_32x32x16_bf16 a[224:239], a[24:27], v[24:27], a[224:239]\n"
    "v_mfma_f32_32x32x16_bf16 a[240:255], a[136:139], v[24:27], a[240:255]\n"
    "buffer_load_dwordx4 v[12:15], v236, %[ws], s44 offen offset:3072 sc1\n"
    "s_waitcnt vmcnt(12)\n"
    "v_mfma_f32_32x32x16_bf16 a[224:239], a[28:31], v[28:31], a[224:239]\n"
    "v_mfma_f32_32x32x16_bf16 a[240:255], a[140:143], v[28:31], a[240:255]\n"
    "s_add_u32 s44, s44, 4096\n"
    "buffer_load_dwordx4 v[16:19], v236, %[ws], s44 offen offset:0 sc1\n"
    "s_waitcnt vmcnt(12)\n"
    "v_mfma_f32_32x32x16_bf16 a[224:239], a[32:35], v[32:35], a[224:239]\n"
    "v_mfma_f32_32x32x16_bf16 a[240:255], a[144:147], v[32:35], a[240:255]\n"
    "buffer_load_dwordx4 v[20:23], v236, %[ws], s44 offen offset:1024 sc1\n"
    "s_waitcnt vmcnt(12)\n"
    "v_mfma_f32_32x32x16_bf16 a[224:239], a[36:39], v[36:39], a[224:239]\n"
    "v_mfma_f32_32x32x16_bf16 a[240:255], a[148:151], v[36:39], a[240:255]\n"
    "buffer_load_dwordx4 v[24:27], v236, %[ws], s44 offen offset:2048 sc1\n"
    "s_waitcnt vmcnt(12)\n"
    "v_mfma_f32_32x32x16_bf16 a[224:239], a[40:43], v[40:43], a[224:239]\n"
    "v_mfma_f32_32x32x16_bf16 a[240:255], a[152:155], v[40:43], a[240:255]\n"
    "buffer_load_dwordx4 v[28:31], v236, %[ws], s44 offen offset:3072 sc1\n"
    "s_waitcnt vmcnt(12)\n"
    "v_mfma_f32_32x32x16_bf16 a[224:239], a[44:47], v[44:47], a[224:239]\n"
    "v_mfma_f32_32x32x16_bf16 a[240:255], a[156:159], v[44:47], a[240:255]\n"
    "s_add_u32 s44, s44, 4096\n"
    "buffer_load_dwordx4 v[32:35], v236, %[ws], s44 offen offset:0 sc1\n"
    "buffer_load_dword v247, v246, %[ws], %[fr] offen sc1\n"
    "s_waitcnt vmcnt(13)\n"
    "v_mfma_f32_32x32x16_bf16 a[224:239], a[48:51], v[48:51], a[224:239]\n"
    "v_mfma_f32_32x32x16_bf16 a[240:255], a[160:163], v[48:51], a[240:255]\n"
    "buffer_load_dwordx4 v[36:39], v236, %[ws], s44 offen offset:1024 sc1\n"
    "s_waitcnt vmcnt(13)\n"
    "v_mfma_f32_32x32x16_bf16 a[224:239], a[52:55], v[52:55], a[224:239]\n"
    "v_mfma_f32_32x32x16_bf16 a[240:255], a[164:167], v[52:55], a[240:255]\n"
    "buffer_load_dwordx4 v[40:43], v236, %[ws], s44 offen offset:2048 sc1\n"
    "s_waitcnt vmcnt(13)\n"
    "v_mfma_f32_32x32x16_bf16 a[224:239], a[56:59], v[56:59], a[224:239]\n"
    "v_mfma_f32_32x32x16_bf16 a[240:255], a[168:171], v[56:59], a[240:255]\n"
    "buffer_load_dwordx4 v[44:47], v236, %[ws], s44 offen offset:3072 sc1\n"
    "s_waitcnt vmcnt(13)\n"
    "v_mfma_f32_32x32x16_bf16 a[224:239], a[60:63], v[60:63], a[224:239]\n"
    "v_mfma_f32_32x32x16_bf16 a[240:255], a[172:175], v[60:63], a[240:255]\n"
    "s_waitcnt vmcnt(3)\n"
    "v_cmp_gt_u32 vcc, 2, v247\n"
    "s_nop 4\n"
    "s_cbranch_vccnz LS2_%=\n"
    "LR2_%=:\n"
    "s_add_u32 s59, %[hl], 229376\n"
    "s_add_u32 s58, s59, 28672\n"
    "s_mov_b32 s44, s59\n"
    "buffer_load_dwordx4 v[48:51], v236, %[ws], s44 offen offset:0 sc1\n"
    "v_mfma_f32_32x32x16_bf16 a[224:239], a[64:67], v[0:3], a[224:239]\n"
    "v_mfma_f32_32x32x16_bf16 a[240:255], a[176:179], v[0:3], a[240:255]\n"
    "buffer_load_dwordx4 v[52:55], v236, %[ws], s44 offen offset:1024 sc1\n"
    "v_mfma_f32_32x32x16_bf16 a[224:239], a[68:71], v[4:7], a[224:239]\n"
    "v_mfma_f32_32x32x16_bf16 a[240:255], a[180:183], v[4:7], a[240:255]\n"
    "buffer_load_dwordx4 v[56:59], v236, %[ws], s44 offen offset:2048 sc1\n"
    "v_mfma_f32_32x32x16_bf16 a[224:239], a[72:75], v[8:11], a[224:239]\n"
    "v_mfma_f32_32x32x16_bf16 a[240:255], a[184:187], v[8:11], a[240:255]\n"
    "buffer_load_dwordx4 v[60:63], v236, %[ws], s44 offen offset:3072 sc1\n"
    "v_mfma_f32_32x32x16_bf16 a[224:239], a[76:79], v[12:15], a[224:239]\n"
    "v_mfma_f32_32x32x16_bf16 a[240:255], a[188:191], v[12:15], a[240:255]\n"
    "s_add_u32 s44, s44, 4096\n"
    "buffer_load_dwordx4 v[0:3], v236, %[ws], s44 offen offset:0 sc1\n"
    "v_mfma_f32_32x32x16_bf16 a[224:239], a[80:83], v[16:19], a[224:239]\n"
    "v_mfma_f32_32x32x16_bf16 a[240:255], a[192:195], v[16:19], a[240:255]\n"
    "buffer_load_dwordx4 v[4:7], v236, %[ws], s44 offen offset:1024 sc1\n"
    "v_mfma_f32_32x32x16_bf16 a[224:239], a[84:87], v[20:23], a[224:239]\n"
    "v_mfma_f32_32x32x16_bf16 a[240:255], a[196:199], v[20:23], a[240:255]\n"
    "buffer_load_dwordx4 v[8:11], v236, %[ws], s44 offen offset:2048 sc1\n"
    "v_mfma_f32_32x32x16_bf16 a[224:239], a[88:91], v[24:27], a[224:239]\n"
    "v_mfma_f32_32x32x16_bf16 a[240:255], a[200:203], v[24:27], a[240:255]\n"
    "buffer_load_dwordx4 v[12:15], v236, %[ws], s44 offen offset:3072 sc1\n"
    "v_mfma_f32_32x32x16_bf16 a[224:239], a[92:95], v[28:31], a[224:239]\n"
    "v_mfma_f32_32x32x16_bf16 a[240:255], a[204:207], v[28:31], a[240:255]\n"
    "s_add_u32 s44, s44, 4096\n"
    "buffer_load_dwordx4 v[16:19], v236, %[ws], s44 offen offset:0 sc1\n"
    "v_mfma_f32_32x32x16_bf16 a[224:239], a[96:99], v[32:35], a[224:239]\n"
    "v_mfma_f32_32x32x16_bf16 a[240:255], a[208:211], v[32:35], a[240:255]\n"
    "buffer_load_dwordx4 v[20:23], v236, %[ws], s44 offen offset:1024 sc1\n"
    "s_waitcnt vmcnt(12)\n"
    "v_mfma_f32_32x32x16_bf16 a[224:239], a[100:103], v[36:39], a[224:239]\n"
    "v_mfma_f32_32x32x16_bf16 a[240:255], a[212:215], v[36:39], a[240:255]\n"
    "buffer_load_dwordx4 v[24:27], v236, %[ws], s44 offen offset:2048 sc1\n"
    "s_waitcnt vmcnt(12)\n"
    "v_mfma_f32_32x32x16_bf16 a[224:239], a[104:107], v[40:43], a[224:239]\n"
    "v_mfma_f32_32x32x16_bf16 a[240:255], a[216:219], v[40:43], a[240:255]\n"
    "buffer_load_dwordx4 v[28:31], v236, %[ws], s44 offen offset:3072 sc1\n"
    "s_waitcnt vmcnt(12)\n"
    "v_mfma_f32_32x32x16_bf16 a[224:239], a[108:111], v[44:47], a[224:239]\n"
    "v_mfma_f32_32x32x16_bf16 a[240:255], a[220:223], v[44:47], a[240:255]\n"
    "s_add_u32 s44, s44, 4096\n"
    "buffer_load_dwordx4 v[32:35], v236, %[ws], s44 offen offset:0 sc1\n"
    "s_waitcnt vmcnt(12)\n"
    "v_mfma_f32_32x32x16_bf16 v[64:79], a[0:3], v[48:51], v[64:79]\n"
    "v_mfma_f32_32x32x16_bf16 v[80:95], a[112:115], v[48:51], v[80:95]\n"
    "buffer_load_dwordx4 v[36:39], v236, %[ws], s44 offen offset:1024 sc1\n"
    "s_waitcnt vmcnt(12)\n"
    "v_mfma_f32_32x32x16_bf16 v[64:79], a[4:7], v[52:55], v[64:79]\n"
    "v_mfma_f32_32x32x16_bf16 v[80:95], a[116:119], v[52:55], v[80:95]\n"
    "buffer_load_dwordx4 v[40:43], v236, %[ws], s44 offen offset:2048 sc1\n"
    "s_waitcnt vmcnt(12)\n"
    "v_mfma_f32_32x32x16_bf16 v[64:79], a[8:11], v[56:59], v[64:79]\n"
    "ds_write_b128 v248, a[224:227] offset:0\n"
    "v_mfma_f32_32x32x16_bf16 v[80:95], a[120:123], v[56:59], v[80:95]\n"
    "ds_write_b128 v248, a[228:231] offset:32\n"
    "buffer_load_dwordx4 v[44:47], v236, %[ws], s44 offen offset:3072 sc1\n"
    "s_waitcnt vmcnt(12)\n"
    "v_mfma_f32_32x32x16_bf16 v[64:79], a[12:15], v[60:63], v[64:79]\n"
    "ds_write_b128 v248, a[232:235] offset:64\n"
    "v_mfma_f32_32x32x16_bf16 v[80:95], a[124:127], v[60:63], v[80:95]\n"
    "ds_write_b128 v248, a[236:239] offset:96\n"
    "s_add_u32 s44, s44, 4096\n"
    "buffer_load_dwordx4 v[48:51], v236, %[ws], s44 offen offset:0 sc1\n"
    "s_waitcnt vmcnt(12)\n"
    "v_mfma_f32_32x32x16_bf16 v[64:79], a[16:19], v[0:3], v[64:79]\n"
    "ds_write_b128 v248, a[240:243] offset:128\n"
    "v_mfma_f32_32x32x16_bf16 v[80:95], a[128:131], v[0:3], v[80:95]\n"
    "ds_write_b128 v248, a[244:247] offset:160\n"
    "buffer_load_dwordx4 v[52:55], v236, %[ws], s44 offen offset:1024 sc1\n"
    "s_waitcnt vmcnt(12)\n"
    "v_mfma_f32_32x32x16_bf16 v[64:79], a[20:23], v[4:7], v[64:79]\n"
    "ds_write_b128 v248, a[248:251] offset:192\n"
    "v_mfma_f32_32x32x16_bf16 v[80:95], a[132:135], v[4:7], v[80:95]\n"
    "ds_write_b128 v248, a[252:255] offset:224\n"
    "buffer_load_dwordx4 v[56:59], v236, %[ws], s44 offen offset:2048 sc1\n"
    "s_waitcnt vmcnt(12)\n"
    "v_mfma_f32_32x32x16_bf16 v[64:79], a[24:27], v[8:11], v[64:79]\n"
    "v_mfma_f32_32x32x16_bf16 v[80:95], a[136:139], v[8:11], v[80:95]\n"
    "buffer_load_dwordx4 v[60:63], v236, %[ws], s44 offen offset:3072 sc1\n"
    "s_waitcnt vmcnt(12)\n"
    "v_mfma_f32_32x32x16_bf16 v[64:79], a[28:31], v[12:15], v[64:79]\n"
    "v_mfma_f32_32x32x16_bf16 v[80:95], a[140:143], v[12:15], v[80:95]\n"
    "s_add_u32 s44, s44, 4096\n"
    "buffer_load_dwordx4 v[0:3], v236, %[ws], s44 offen offset:0 sc1\n"
    "s_waitcnt vmcnt(12)\n"
    "v_mfma_f32_32x32x16_bf16 v[64:79], a[32:35], v[16:19], v[64:79]\n"
    "v_mfma_f32_32x32x16_bf16 v[80:95], a[144:147], v[16:19], v[80:95]\n"
    "buffer_load_dwordx4 v[4:7], v236, %[ws], s44 offen offset:1024 sc1\n"
    "s_waitcnt vmcnt(12)\n"
    "v_mfma_f32_32x32x16_bf16 v[64:79], a[36:39], v[20:23], v[64:79]\n"
    "v_mfma_f32_32x32x16_bf16 v[80:95], a[148:151], v[20:23], v[80:95]\n"
    "buffer_load_dwordx4 v[8:11], v236, %[ws], s44 offen offset:2048 sc1\n"
    "s_waitcnt vmcnt(12)\n"
    "v_mfma_f32_32x32x16_bf16 v[64:79], a[40:43], v[24:27], v[64:79]\n"
    "v_mfma_f32_32x32x16_bf16 v[80:95], a[152:155], v[24:27], v[80:95]\n"
    "buffer_load_dwordx4 v[12:15], v236, %[ws], s44 offen offset:3072 sc1\n"
    "s_waitcnt vmcnt(12)\n"
    "v_mfma_f32_32x32x16_bf16 v[64:79], a[44:47], v[28:31], v[64:79]\n"
    "v_mfma_f32_32x32x16_bf16 v[80:95], a[156:159], v[28:31], v[80:95]\n"
    "s_add_u32 s44, s44, 4096\n"
    "buffer_load_dwordx4 v[16:19], v236, %[ws], s44 offen offset:0 sc1\n"
    "s_waitcnt vmcnt(12)\n"
    "v_mfma_f32_32x32x16_bf16 v[64:79], a[48:51], v[32:35], v[64:79]\n"
    "v_mfma_f32_32x32x16_bf16 v[80:95], a[160:163], v[32:35], v[80:95]\n"
    "buffer_load_dwordx4 v[20:23], v236, %[ws], s44 offen offset:1024 sc1\n"
    "s_waitcnt vmcnt(12)\n"
    "v_mfma_f32_32x32x16_bf16 v[64:79], a[52:55], v[36:39], v[64:79]\n"
    "v_mfma_f32_32x32x16_bf16 v[80:95], a[164:167], v[36:39], v[80:95]\n"
    "buffer_load_dwordx4 v[24:27], v236, %[ws], s44 offen offset:2048 sc1\n"
    "s_waitcnt vmcnt(12)\n"
    "v_mfma_f32_32x32x16_bf16 v[64:79], a[56:59], v[40:43], v[64:79]\n"
    "v_mfma_f32_32x32x16_bf16 v[80:95], a[168:171], v[40:43], v[80:95]\n"
    "buffer_load_dwordx4 v[28:31], v236, %[ws], s44 offen offset:3072 sc1\n"
    "s_waitcnt vmcnt(12)\n"
    "v_mfma_f32_32x32x16_bf16 v[64:79], a[60:63], v[44:47], v[64:79]\n"
    "v_mfma_f32_32x32x16_bf16 v[80:95], a[172:175], v[44:47], v[80:95]\n"
    "s_waitcnt lgkmcnt(0)\n"
    "s_barrier\n"
    "s_waitcnt vmcnt(11)\n"
    "v_mfma_f32_32x32x16_bf16 v[64:79], a[64:67], v[48:51], v[64:79]\n"
    "ds_read_b128 v[96:99], v249 offset:0\n"
    "ds_read_b128 v[100:103], v249 offset:16\n"
    "ds_read_b128 v[104:107], v249 offset:8704\n"
    "ds_read_b128 v[108:111], v249 offset:8720\n"
    "ds_read_b128 v[112:115], v249 offset:17408\n"
    "v_mfma_f32_32x32x16_bf16 v[80:95], a[176:179], v[48:51], v[80:95]\n"
    "ds_read_b128 v[116:119], v249 offset:17424\n"
    "ds_read_b128 v[120:123], v249 offset:26112\n"
    "ds_read_b128 v[124:127], v249 offset:26128\n"
    "s_waitcnt lgkmcnt(4)\n"
    "v_add_f32 v96, v96, v104\n"
    "s_waitcnt vmcnt(10)\n"
    "v_mfma_f32_32x32x16_bf16 v[64:79], a[68:71], v[52:55], v[64:79]\n"
    "v_add_f32 v97, v97, v105\n"
    "v_add_f32 v98, v98, v106\n"
    "v_add_f32 v99, v99, v107\n"
    "v_add_f32 v100, v100, v108\n"
    "v_add_f32 v101, v101, v109\n"
    "v_mfma_f32_32x32x16_bf16 v[80:95], a[180:183], v[52:55], v[80:95]\n"
    "v_add_f32 v102, v102, v110\n"
    "v_add_f32 v103, v103, v111\n"
    "s_waitcnt lgkmcnt(0)\n"
    "v_add_f32 v112, v112, v120\n"
    "v_add_f32 v113, v113, v121\n"
    "s_waitcnt vmcnt(9)\n"
    "v_mfma_f32_32x32x16_bf16 v[64:79], a[72:75], v[56:59], v[64:79]\n"
    "v_add_f32 v114, v114, v122\n"
    "v_add_f32 v115, v115, v123\n"
    "v_add_f32 v116, v116, v124\n"
    "v_add_f32 v117, v117, v125\n"
    "v_add_f32 v118, v118, v126\n"
    "v_mfma_f32_32x32x16_bf16 v[80:95], a[184:187], v[56:59], v[80:95]\n"
    "v_add_f32 v119, v119, v127\n"
    "v_add_f32 v96, v96, v112\n"
    "v_add_f32 v97, v97, v113\n"
    "v_add_f32 v98, v98, v114\n"
    "v_add_f32 v99, v99, v115\n"
    "s_waitcnt vmcnt(8)\n"
    "v_mfma_f32_32x32x16_bf16 v[64:79], a[76:79], v[60:63], v[64:79]\n"
    "v_add_f32 v100, v100, v116\n"
    "v_add_f32 v101, v101, v117\n"
    "v_add_f32 v102, v102, v118\n"
    "v_add_f32 v103, v103, v119\n"
    "v_fma_f32 v96, v96, v168, v160\n"
    "v_mfma_f32_32x32x16_bf16 v[80:95], a[188:191], v[60:63], v[80:95]\n"
    "v_fma_f32 v97, v97, v168, v161\n"
    "v_fma_f32 v98, v98, v168, v162\n"
    "v_fma_f32 v99, v99, v168, v163\n"
    "v_fma_f32 v100, v100, v168, v164\n"
    "s_waitcnt vmcnt(7)\n"
    "v_mfma_f32_32x32x16_bf16 v[64:79], a[80:83], v[0:3], v[64:79]\n"
    "v_fma_f32 v101, v101, v168, v165\n"
    "v_fma_f32 v102, v102, v168, v166\n"
    "v_fma_f32 v103, v103, v168, v167\n"
    "v_cvt_pk_bf16_f32 v104, v96, v97\n"
    "v_mfma_f32_32x32x16_bf16 v[80:95], a[192:195], v[0:3], v[80:95]\n"
    "v_cvt_pk_bf16_f32 v105, v98, v99\n"
    "v_cvt_pk_bf16_f32 v106, v100, v101\n"
    "v_cvt_pk_bf16_f32 v107, v102, v103\n"
    "buffer_store_dwordx4 v[104:107], v250, %[out], %[oo] offen offset:0\n"
    "s_waitcnt vmcnt(7)\n"
    "v_mfma_f32_32x32x16_bf16 v[64:79], a[84:87], v[4:7], v[64:79]\n"
    "v_mfma_f32_32x32x16_bf16 v[80:95], a[196:199], v[4:7], v[80:95]\n"
    "s_waitcnt vmcnt(6)\n"
    "v_mfma_f32_32x32x16_bf16 v[64:79], a[88:91], v[8:11], v[64:79]\n"
    "v_mfma_f32_32x32x16_bf16 v[80:95], a[200:203], v[8:11], v[80:95]\n"
    "s_waitcnt vmcnt(5)\n"
    "v_mfma_f32_32x32x16_bf16 v[64:79], a[92:95], v[12:15], v[64:79]\n"
    "v_mfma_f32_32x32x16_bf16 v[80:95], a[204:207], v[12:15], v[80:95]\n"
    "s_waitcnt vmcnt(4)\n"
    "v_mfma_f32_32x32x16_bf16 v[64:79], a[96:99], v[16:19], v[64:79]\n"
    "v_mfma_f32_32x32x16_bf16 v[80:95], a[208:211], v[16:19], v[80:95]\n"
    "s_waitcnt vmcnt(3)\n"
    "v_mfma_f32_32x32x16_bf16 v[64:79], a[100:103], v[20:23], v[64:79]\n"
    "v_mfma_f32_32x32x16_bf16 v[80:95], a[212:215], v[20:23], v[80:95]\n"
    "s_waitcnt vmcnt(2)\n"
    "v_mfma_f32_32x32x16_bf16 v[64:79], a[104:107], v[24:27], v[64:79]\n"
    "v_mfma_f32_32x32x16_bf16 v[80:95], a[216:219], v[24:27], v[80:95]\n"
    "s_waitcnt vmcnt(1)\n"
    "v_mfma_f32_32x32x16_bf16 v[64:79], a[108:111], v[28:31], v[64:79]\n"
    "v_mfma_f32_32x32x16_bf16 v[80:95], a[220:223], v[28:31], v[80:95]\n"
    "v_subrev_u32 v248, 114688, v248\n"
    "v_subrev_u32 v249, 114688, v249\n"
    "s_nop 8\n"
    "ds_write_b128 v248, v[64:67] offset:0\n"
    "ds_write_b128 v248, v[68:71] offset:32\n"
    "ds_write_b128 v248, v[72:75] offset:64\n"
    "ds_write_b128 v248, v[76:79] offset:96\n"
    "ds_write_b128 v248, v[80:83] offset:128\n"
    "ds_write_b128 v248, v[84:87] offset:160\n"
    "ds_write_b128 v248, v[88:91] offset:192\n"
    "ds_write_b128 v248, v[92:95] offset:224\n"
    "s_waitcnt lgkmcnt(0)\n"
    "s_barrier\n"
    "ds_read_b128 v[96:99], v249 offset:0\n"
    "ds_read_b128 v[100:103], v249 offset:16\n"
    "ds_read_b128 v[104:107], v249 offset:8704\n"
    "ds_read_b128 v[108:111], v249 offset:8720\n"
    "ds_read_b128 v[112:115], v249 offset:17408\n"
    "ds_read_b128 v[116:119], v249 offset:17424\n"
    "ds_read_b128 v[120:123], v249 offset:26112\n"
    "ds_read_b128 v[124:127], v249 offset:26128\n"
    "s_waitcnt lgkmcnt(4)\n"
    "v_pk_add_f32 v[96:97], v[96:97], v[104:105]\n"
    "v_pk_add_f32 v[98:99], v[98:99], v[106:107]\n"
    "v_pk_add_f32 v[100:101], v[100:101], v[108:109]\n"
    "v_pk_add_f32 v[102:103], v[102:103], v[110:111]\n"
    "s_waitcnt lgkmcnt(0)\n"
    "v_pk_add_f32 v[112:113], v[112:113], v[120:121]\n"
    "v_pk_add_f32 v[114:115], v[114:115], v[122:123]\n"
    "v_pk_add_f32 v[116:117], v[116:117], v[124:125]\n"
    "v_pk_add_f32 v[118:119], v[118:119], v[126:127]\n"
    "v_pk_add_f32 v[96:97], v[96:97], v[112:113]\n"
    "v_pk_add_f32 v[98:99], v[98:99], v[114:115]\n"
    "v_pk_add_f32 v[100:101], v[100:101], v[116:117]\n"
    "v_pk_add_f32 v[102:103], v[102:103], v[118:119]\n"
    "v_pk_fma_f32 v[96:97], v[96:97], v[168:169], v[160:161] op_sel:[0,0,0] op_sel_hi:[1,0,1]\n"
    "v_pk_fma_f32 v[98:99], v[98:99], v[168:169], v[162:163] op_sel:[0,0,0] op_sel_hi:[1,0,1]\n"
    "v_pk_fma_f32 v[100:101], v[100:101], v[168:169], v[164:165] op_sel:[0,0,0] op_sel_hi:[1,0,1]\n"
    "v_pk_fma_f32 v[102:103], v[102:103], v[168:169], v[166:167] op_sel:[0,0,0] op_sel_hi:[1,0,1]\n"
    "v_cvt_pk_bf16_f32 v104, v96, v97\n"
    "v_cvt_pk_bf16_f32 v105, v98, v99\n"
    "v_cvt_pk_bf16_f32 v106, v100, v101\n"
    "v_cvt_pk_bf16_f32 v107, v102, v103\n"
    "buffer_store_dwordx4 v[104:107], v250, %[out], %[oo] offen offset:896\n"
    "s_branch LE_%=\n"
    "LS0_%=:\n"
    "s_sleep 4\n"
    "buffer_load_dword v133, v131, %[ws], s42 offen sc1\n"
    "s_waitcnt vmcnt(0)\n"
    "v_cmp_gt_u32 vcc, 1, v133\n"
    "s_sub_u32 s46, s46, 1\n"
    "s_cbranch_scc1 LR0_%=\n"
    "s_nop 4\n"
    "s_cbranch_vccnz LS0_%=\n"
    "s_branch LR0_%=\n"
    "LS1_%=:\n"
    "s_sleep 4\n"
    "buffer_load_dword v247, v246, %[ws], %[fr] offen sc1\n"
    "s_waitcnt vmcnt(0)\n"
    "v_cmp_gt_u32 vcc, 1, v247\n"
    "s_sub_u32 s46, s46, 1\n"
    "s_cbranch_scc1 LR1_%=\n"
    "s_nop 4\n"
    "s_cbranch_vccnz LS1_%=\n"
    "s_branch LR1_%=\n"
    "LS2_%=:\n"
    "s_sleep 4\n"
    "buffer_load_dword v247, v246, %[ws], %[fr] offen sc1\n"
    "s_waitcnt vmcnt(0)\n"
    "v_cmp_gt_u32 vcc, 2, v247\n"
    "s_sub_u32 s46, s46, 1\n"
    "s_cbranch_scc1 LR2_%=\n"
    "s_nop 4\n"
    "s_cbranch_vccnz LS2_%=\n"
    "s_branch LR2_%=\n"
    "LE_%=:\n"
    "ilg_end1:\n"
            :: G_OPS, [out] "s"(out), [oo] "s"(oo), [el] "s"(el), [d0] "s"(d0) : G_CLOB);
    }
}

__device__ __forceinline__ void w2t_job(const Params& p, LAS unsigned char* lds, int tt) {
    const int lane = threadIdx.x & 63, w = __builtin_amdgcn_readfirstlane(threadIdx.x >> 6), h = lane >> 5, rb = w >> 1;
    const int v2 = (16 * w + (lane >> 3)) * (D_ * 2) + (((lane & 7) ^ (lane >> 4)) << 4);
    const u32x4 d2 = mkdesc(p.w2 + tt * 64 * D_, 64 * D_ * 2);
    const rsrc_t ws = mkrsrc(p.ws, WS_BYTES);
#pragma unroll
    for (int cg = 0; cg < 7; ++cg) {
        dma16(d2, v2, 128 * cg, (int)(size_t)lds + (8 * cg + 2 * w) * 1024);
        dma16(d2, (v2 + 8 * D_ * 2) ^ 64, 128 * cg, (int)(size_t)lds + (8 * cg + 2 * w + 1) * 1024);
    }
    DRAIN_VM();
    __builtin_amdgcn_s_barrier();
    const int row = 32 * rb + 4 * h + ((lane >> 2) & 3), ch = 4 * (w & 1) + ((lane >> 3) & 2) + ((lane >> 1) & 1);
    const int a = (row >> 3) * 1024 + (row & 7) * 128 + ((ch ^ ((row >> 1) & 7)) << 4) + 8 * (lane & 1);
#pragma unroll
    for (int jt = 0; jt < 7; ++jt)
#pragma unroll
        for (int s = 0; s < 2; ++s) {
            const s16x4 lo = __builtin_amdgcn_ds_read_tr16_b64_v4i16((LAS s16x4*)(lds + jt * 8192 + a + 2048 * s));
            const s16x4 hi = __builtin_amdgcn_ds_read_tr16_b64_v4i16((LAS s16x4*)(lds + jt * 8192 + (a ^ 64) + 2048 * s + 1024));
            const u32x2 l = __builtin_bit_cast(u32x2, lo), hh = __builtin_bit_cast(u32x2, hi);
            bst128<16>(u32x4{l.x, l.y, hh.x, hh.y}, ws, OFF_W2T + ((2 * jt + (w & 1)) * 112 + 4 * tt + 2 * rb + s) * 1024 + lane * 16);
        }
}

__global__ void __launch_bounds__(256, 1) k_mlp0(Params p) {
    __shared__ __attribute__((aligned(16))) unsigned char smem[G_LDS];
    if (blockIdx.x < 224) mlp_body<0>(p, (int)(size_t)(LAS unsigned char*)smem);
    else w2t_job(p, (LAS unsigned char*)smem, blockIdx.x - 224);
}
__global__ void __launch_bounds__(256, 1) k_mlp1(Params p) {
    __shared__ __attribute__((aligned(16))) unsigned char smem[G_LDS];
    mlp_body<1>(p, (int)(size_t)(LAS unsigned char*)smem);
}

extern "C" void kernel_launch(void* const* d_in, const int* in_sizes, int n_in, void* d_out, int out_size, void* d_ws, size_t ws_size,
                              hipStream_t stream) {
    Params p;
    p.x0 = (const u16*)d_in[0]; p.wr = (const u16*)d_in[1]; p.wi = (const u16*)d_in[2];
    p.w1 = (const u16*)d_in[4]; p.b1 = (const u16*)d_in[5]; p.w2 = (const u16*)d_in[6]; p.b2 = (const u16*)d_in[7];
    p.out = (u16*)d_out; p.ws = (unsigned char*)d_ws;
    p.layer = 0; p.unused = 0;
    k_first<<<256, 256, 0, stream>>>(p);
    k_mlp0<<<252, 256, 0, stream>>>(p);
    k_mlp1<<<224, 256, 0, stream>>>(p);
}
```
